```python
import torch

def _softmax(x, axis=-1):
    return torch.softmax(x, dim=axis)
def _gelu(x):
    return torch.nn.functional.gelu(x, approximate="tanh")
def _silu(x):
    return torch.nn.functional.silu(x)
def _sigmoid(x):
    return torch.sigmoid(x)
def _layernorm(x, axis=-1, eps=1e-5):
    m = x.mean(dim=axis, keepdim=True)
    v = x.var(dim=axis, keepdim=True, unbiased=False)
    return (x - m) / torch.sqrt(v + eps)
def _scatter_add(tgt, idx, src, axis=0):
    return tgt.clone().index_add_(axis, idx, src)
def _segment_sum(src, seg, n):
    out = torch.zeros((n,) + src.shape[1:], dtype=src.dtype)
    return out.index_add_(0, seg, src)
def _pad(x, pw):
    flat = tuple(v for pair in reversed(pw) for v in pair)
    return torch.nn.functional.pad(x, flat)

kernel_name = 'sequential_preLN__mha_block__swiglu_ffn__prompt'

def setup_inputs():

    _w = (lambda a: (lambda t: t.to(torch.bfloat16) if t.is_floating_point() else t)(torch.as_tensor(a)))
    def _A_setup():
        B, S, D, H = 1, 2048, 640, 32
        return {
            "x": _w((lambda *s: torch.randn(*s, dtype=torch.bfloat16) * 0.05)(B, S, D)),
            "wqkv": _w((lambda *s: torch.randn(*s, dtype=torch.bfloat16) * 0.05)(D, 3 * D)),
            "wo": _w((lambda *s: torch.randn(*s, dtype=torch.bfloat16) * 0.05)(D, D)),
        }
    _A_in = _A_setup()

    def _B_setup():
        B, S, D, F = 1, 2048, 640, 1728
        return {
            "x": _w((lambda *s: torch.randn(*s, dtype=torch.bfloat16) * 0.05)(B, S, D)),
            "w_gate": _w((lambda *s: torch.randn(*s, dtype=torch.bfloat16) * 0.05)(D, F)),
            "w_up": _w((lambda *s: torch.randn(*s, dtype=torch.bfloat16) * 0.05)(D, F)),
            "w_down": _w((lambda *s: torch.randn(*s, dtype=torch.bfloat16) * 0.05)(F, D)),
        }
    _B_in = _B_setup()

    return {
        "A_x": _A_in["x"],
        "A_wqkv": _A_in["wqkv"],
        "A_wo": _A_in["wo"],
        "B_x": _B_in["x"],
        "B_w_gate": _B_in["w_gate"],
        "B_w_up": _B_in["w_up"],
        "B_w_down": _B_in["w_down"],
    }

def reference(A_x, A_wqkv, A_wo, B_x, B_w_gate, B_w_up, B_w_down):
    def _A_ref(x, wqkv, wo):
        B, S, D = x.shape
        H = 32
        Dh = D // H
        qkv = (lambda x, *d: x.permute(*d))((x @ wqkv).reshape(B, S, 3, H, Dh), 2, 0, 3, 1, 4)
        q, k, v = qkv[0], qkv[1], qkv[2]
        scores = torch.einsum("bhqd,bhkd->bhqk", q, k) * (Dh ** -0.5)
        p = _softmax(scores, axis=-1)
        o = torch.einsum("bhqk,bhkd->bhqd", p, v)
        return (lambda x, *d: x.permute(*d))(o, 0, 2, 1, 3).reshape(B, S, D) @ wo

    def _B_ref(x, w_gate, w_up, w_down):
        g = _silu(x @ w_gate)
        u = x @ w_up
        return (g * u) @ w_down

    x = A_x
    h = x + _A_ref(_layernorm(x, axis=-1), A_wqkv, A_wo)
    h2 = h + _B_ref(_layernorm(h, axis=-1), B_w_gate, B_w_up, B_w_down)
    return h2
```

```cpp
#include <hip/hip_runtime.h>
#include <hip/hip_bf16.h>
#include <stdint.h>

typedef __attribute__((ext_vector_type(8))) short bf16x8;
typedef __attribute__((ext_vector_type(4))) short bf16x4;
typedef __attribute__((ext_vector_type(2))) float f32x2;
typedef __attribute__((ext_vector_type(4))) float f32x4;
typedef __attribute__((ext_vector_type(16))) float f32x16;
typedef __attribute__((ext_vector_type(4))) unsigned u32x4;
typedef __attribute__((ext_vector_type(2))) unsigned u32x2;
typedef __attribute__((ext_vector_type(2))) __bf16 bf2;
typedef __attribute__((address_space(3))) const char* lds_cptr;
typedef __attribute__((address_space(3))) char* lds_ptr;
#define LDS_AS __attribute__((address_space(3)))
#define DI __device__ __forceinline__

constexpr int SEQ = 2048, DM = 640, NH = 32, DH = 20, FF = 1728, NQKV = 1920;
constexpr float LN_EPS = 1e-5f;
constexpr float QSCALE = 0.22360679774997896f * 1.4426950408889634f;

constexpr int KV_TILE = 2688;
constexpr int KV_HEAD = 64 * KV_TILE;
constexpr size_t OFF_Q    = 0;
constexpr size_t OFF_KV   = OFF_Q + (size_t)SEQ * DM * 2;
constexpr size_t OFF_OF   = OFF_KV + (size_t)NH * KV_HEAD;
constexpr size_t OFF_HF   = OFF_OF + (size_t)SEQ * DM * 2;
constexpr size_t OFF_ACT  = OFF_HF + (size_t)SEQ * DM * 2;
constexpr size_t OFF_WOF  = OFF_ACT + (size_t)SEQ * FF * 2;
constexpr size_t OFF_WGUF = OFF_WOF + (size_t)DM * DM * 2;
constexpr size_t OFF_WDF  = OFF_WGUF + (size_t)DM * FF * 4;
constexpr size_t OFF_STAT = OFF_WDF + (size_t)FF * DM * 2;
constexpr size_t OFF_CSUM = OFF_STAT + (size_t)SEQ * 64;

DI void glds16(unsigned voff, const void* sbase, unsigned lds) {
  unsigned keep;
  asm volatile("s_mov_b32 %0, m0\n\ts_mov_b32 m0, %3\n\ts_nop 0\n\tglobal_load_lds_dwordx4 %1, %2\n\ts_mov_b32 m0, %0"
               : "=&s"(keep) : "v"(voff), "s"(sbase), "s"(lds) : "memory");
}
template <int N> DI void wait_vm_bar() { asm volatile("s_waitcnt vmcnt(%0)\n\ts_barrier" :: "n"(N) : "memory"); }
DI void wait_lgkm_bar() { asm volatile("s_waitcnt lgkmcnt(0)\n\ts_barrier" ::: "memory"); }
#define SBAR() __builtin_amdgcn_sched_barrier(0)
DI bf16x8 lds_b128(lds_cptr p) { return *(const LDS_AS bf16x8*)p; }
DI bf16x4 lds_b64(lds_cptr p) { return *(const LDS_AS bf16x4*)p; }
DI bf16x4 lds_tr(lds_cptr p) { return __builtin_amdgcn_ds_read_tr16_b64_v4i16((LDS_AS bf16x4*)p); }
DI f32x4 mfma16(bf16x8 a, bf16x8 b, f32x4 c) { return __builtin_amdgcn_mfma_f32_16x16x32_bf16(a, b, c, 0, 0, 0); }
DI f32x16 mfma32(bf16x8 a, bf16x8 b, f32x16 c) { return __builtin_amdgcn_mfma_f32_32x32x16_bf16(a, b, c, 0, 0, 0); }
DI f32x16 mfma32k8(bf16x4 a, bf16x4 b, f32x16 c) { return __builtin_amdgcn_mfma_f32_32x32x8bf16_1k(a, b, c, 0, 0, 0); }
DI unsigned pk2(float lo, float hi) { f32x2 v = {lo, hi}; return __builtin_bit_cast(unsigned, __builtin_convertvector(v, bf2)); }
DI float bflo(unsigned u) { return __uint_as_float(u << 16); }
DI float bfhi(unsigned u) { return __uint_as_float(u & 0xffff0000u); }
DI int div20(int c) { return (c * 3277) >> 16; }

template <int MODE>
DI void qkv_compute(lds_cptr L, lds_ptr scr, int wid, int lane, int row0, int col0, char* __restrict__ ws) {
  const int wr = wid >> 1, wc = wid & 1, r16 = lane & 15, g = lane >> 4, q = r16 >> 2, p = lane & 3;
  const int f = q | ((g & 1) << 2);
  int aoff[2], boff[4];
#pragma unroll
  for (int ks = 0; ks < 2; ++ks) aoff[ks] = (64 * wr + r16) * 128 + (((4 * ks + g) ^ (r16 >> 1)) << 4);
#pragma unroll
  for (int jj = 0; jj < 4; ++jj) { const int j = (jj + 2 * wr) & 3; boff[jj] = 16384 + (8 * g + q) * 256 + ((((4 * wc + j) ^ f) * 2 + (p >> 1)) << 4) + (p & 1) * 8; }
  int amoff[4];
#pragma unroll
  for (int ii = 0; ii < 4; ++ii) amoff[ii] = ((ii + 2 * wc) & 3) * 2048;

  f32x4 acc[4][4], rs[2], gr[2], cs[2];
#pragma unroll
  for (int i = 0; i < 4; ++i)
#pragma unroll
    for (int j = 0; j < 4; ++j) acc[i][j] = f32x4{0.f, 0.f, 0.f, 0.f};
#pragma unroll
  for (int i = 0; i < 2; ++i) { rs[i] = f32x4{0.f, 0.f, 0.f, 0.f}; gr[i] = rs[i]; cs[i] = rs[i]; }
  const bf16x8 ones = {0x3f80, 0x3f80, 0x3f80, 0x3f80, 0x3f80, 0x3f80, 0x3f80, 0x3f80};
  bf16x8 a0[4], b0[4], a1[4], b1[4];

  auto rd = [&](bf16x8 (&a)[4], bf16x8 (&b)[4], int slot, int ks) {
    lds_cptr S = L + slot;
#pragma unroll
    for (int jj = 0; jj < 4; ++jj) {
      bf16x4 lo = lds_tr(S + boff[jj] + ks * 8192), hi = lds_tr(S + boff[jj] + ks * 8192 + 1024);
      b[jj] = __builtin_shufflevector(lo, hi, 0, 1, 2, 3, 4, 5, 6, 7);
    }
#pragma unroll
    for (int ii = 0; ii < 4; ++ii) a[ii] = lds_b128(S + aoff[ks] + amoff[ii]);
  };
  auto mm = [&](bf16x8 (&a)[4], bf16x8 (&b)[4]) {
#pragma unroll
    for (int ii = 0; ii < 4; ++ii)
#pragma unroll
      for (int jj = 0; jj < 4; ++jj)
        acc[ii][jj] = (MODE == 2) ? mfma16(a[ii], b[jj], acc[ii][jj]) : mfma16(b[jj], a[ii], acc[ii][jj]);
#pragma unroll
    for (int u = 0; u < 2; ++u) {
      rs[u] = mfma16(a[u], ones, rs[u]);
      gr[u] = mfma16(a[u], a[u], gr[u]);
      cs[u] = mfma16(ones, b[u], cs[u]);
    }
  };

  wait_lgkm_bar(); SBAR();
  rd(a0, b0, 0, 0);
#pragma unroll 1
  for (int kt = 0; kt < 10; ++kt) {
    const int slot = (kt & 3) * 32768;
    rd(a1, b1, slot, 1);
    SBAR();
    mm(a0, b0);
    SBAR();
    if (kt < 9) {
      wait_lgkm_bar(); SBAR();
      rd(a0, b0, ((kt + 1) & 3) * 32768, 0);
      SBAR();
    }
    mm(a1, b1);
    SBAR();
  }
  LDS_AS float* rsum = (LDS_AS float*)scr; LDS_AS float* rsq = rsum + 128; LDS_AS float* csum = rsum + 256;
#pragma unroll
  for (int u = 0; u < 2; ++u) {
    const int rb = 64 * wr + amoff[u] / 128;
    if (r16 == 0) *(LDS_AS f32x4*)(rsum + rb + 4 * g) = rs[u];
    if (q == g) { const float d = p == 0 ? gr[u][0] : p == 1 ? gr[u][1] : p == 2 ? gr[u][2] : gr[u][3]; rsq[rb + r16] = d; }
    const int cb = 64 * wc + 16 * ((u + 2 * wr) & 3);
    if (g == 0) csum[cb + r16] = cs[u][0];
  }
  wait_lgkm_bar(); SBAR();
  constexpr float invD = 1.0f / DM;
  if (MODE != 2) {
#pragma unroll
    for (int ii = 0; ii < 4; ++ii) {
      const int rl = 64 * wr + amoff[ii] / 128 + r16, tok = row0 + rl;
      const float mean = rsum[rl] * invD, var = fmaxf(rsq[rl] * invD - mean * mean, 0.f);
      const float rstd = __builtin_amdgcn_rsqf(var + LN_EPS) * (MODE == 0 ? QSCALE : 1.0f);
#pragma unroll
      for (int jj = 0; jj < 4; ++jj) {
        const int cl = 64 * wc + 16 * ((jj + 2 * wr) & 3) + 4 * g;
        const f32x4 c4 = *(const LDS_AS f32x4*)(csum + cl);
        u32x2 o;
        o[0] = pk2(rstd * (acc[ii][jj][0] - mean * c4[0]), rstd * (acc[ii][jj][1] - mean * c4[1]));
        o[1] = pk2(rstd * (acc[ii][jj][2] - mean * c4[2]), rstd * (acc[ii][jj][3] - mean * c4[3]));
        const int c = col0 + cl;
        if (MODE == 0) {
          *(u32x2*)(ws + OFF_Q + (size_t)tok * 1280 + c * 2) = o;
        } else {
          const int ck = c - DM, head = div20(ck), d = ck - head * DH, r = tok & 31;
          const int off = d < 16 ? (d >> 3) * 512 + r * 16 + (d & 7) * 2 : 1024 + r * 8;
          *(u32x2*)(ws + OFF_KV + (size_t)head * KV_HEAD + (tok >> 5) * KV_TILE + off) = o;
        }
      }
    }
  } else {
#pragma unroll
    for (int ii = 0; ii < 4; ++ii) {
      const int rl = 64 * wr + amoff[ii] / 128 + 4 * g, tok = row0 + rl;
      const f32x4 s4 = *(const LDS_AS f32x4*)(rsum + rl), q4 = *(const LDS_AS f32x4*)(rsq + rl);
      float mean[4], rstd[4];
#pragma unroll
      for (int i = 0; i < 4; ++i) { mean[i] = s4[i] * invD; rstd[i] = __builtin_amdgcn_rsqf(fmaxf(q4[i] * invD - mean[i] * mean[i], 0.f) + LN_EPS); }
      const int tbase = (tok >> 5) * KV_TILE + 1280 + ((tok >> 4) & 1) * 704 + (g & 1) * 352 + (g >> 1) * 8;
#pragma unroll
      for (int jj = 0; jj < 4; ++jj) {
        const int cl = 64 * wc + 16 * ((jj + 2 * wr) & 3) + r16;
        const float c1 = csum[cl];
        u32x2 o;
        o[0] = pk2(rstd[0] * (acc[ii][jj][0] - mean[0] * c1), rstd[1] * (acc[ii][jj][1] - mean[1] * c1));
        o[1] = pk2(rstd[2] * (acc[ii][jj][2] - mean[2] * c1), rstd[3] * (acc[ii][jj][3] - mean[3] * c1));
        const int cv = col0 + cl - 2 * DM, head = div20(cv), d = cv - head * DH;
        char* vb = ws + OFF_KV + (size_t)head * KV_HEAD + tbase;
        *(u32x2*)(vb + d * 16) = o;
        if (d == 0) *(u32x2*)(vb + 20 * 16) = u32x2{0x3f803f80u, 0x3f803f80u};
      }
    }
  }
}

__global__ __launch_bounds__(512, 2) void k_qkv(const char* __restrict__ x, const char* __restrict__ w, char* __restrict__ ws) {
  __shared__ __attribute__((aligned(16))) char smem[4 * 32768 + 2048];
  const int bid = blockIdx.x, xcd = bid & 7, idx = bid >> 3;
  const int rt = (xcd >> 1) * 4 + (idx & 3), ct = (xcd & 1) * 8 + (idx >> 2);
  if (ct >= 15) return;
  const int tid = threadIdx.x, lane = tid & 63, wid = __builtin_amdgcn_readfirstlane(tid >> 6);
  if (wid >= 4) {
    const unsigned lds0 = (unsigned)(uintptr_t)smem;
    const int lw = wid - 4;
    unsigned voA[4], voB[4];
#pragma unroll
    for (int j = 0; j < 4; ++j) {
      const int i = lw + 4 * j;
      const int ra = 8 * i + (lane >> 3); voA[j] = ra * 1280 + (((lane & 7) ^ ((ra >> 1) & 7)) << 4);
      const int rb = 4 * i + (lane >> 4), f = (rb & 3) | ((rb >> 1) & 4); voB[j] = rb * 3840 + (((lane & 15) ^ (f << 1)) << 4);
    }
    const char* pa = x + (size_t)rt * 128 * 1280; const char* pb = w + ct * 256;
    auto issue = [&](int st) {
      const unsigned base = lds0 + (st & 3) * 32768 + lw * 1024;
#pragma unroll
      for (int j = 0; j < 4; ++j) { glds16(voA[j], pa + st * 128, base + j * 4096); glds16(voB[j], pb + (size_t)st * 64 * 3840, base + 16384 + j * 4096); }
    };
    issue(0); issue(1); issue(2); issue(3);
    wait_vm_bar<24>();
#pragma unroll
    for (int s = 1; s < 10; ++s) {
      if (s <= 7) wait_vm_bar<16>(); else if (s == 8) wait_vm_bar<8>(); else wait_vm_bar<0>();
      if (s + 3 < 10) issue(s + 3);
    }
    return;
  }
  lds_cptr L = (lds_cptr)smem; lds_ptr scr = (lds_ptr)smem + 4 * 32768;
  if (ct < 5) qkv_compute<0>(L, scr, wid, lane, rt * 128, ct * 128, ws);
  else if (ct < 10) qkv_compute<1>(L, scr, wid, lane, rt * 128, ct * 128, ws);
  else qkv_compute<2>(L, scr, wid, lane, rt * 128, ct * 128, ws);
}

constexpr int SC_BYTES = 8 * KV_TILE;
constexpr int SC_SLOT = 24576;

DI float rowmax16(const f32x16& s) {
  float a = fmaxf(fmaxf(s[0], s[1]), s[2]), b = fmaxf(fmaxf(s[3], s[4]), s[5]);
  a = fmaxf(fmaxf(a, s[6]), s[7]); b = fmaxf(fmaxf(b, s[8]), s[9]);
  a = fmaxf(fmaxf(a, s[10]), s[11]); b = fmaxf(fmaxf(b, s[12]), s[13]);
  a = fmaxf(fmaxf(a, s[14]), s[15]);
  const float m = fmaxf(a, b);
  auto rr = __builtin_amdgcn_permlane32_swap(__float_as_uint(m), __float_as_uint(m), false, false);
  return fmaxf(__uint_as_float(rr[0]), __uint_as_float(rr[1]));
}
DI void exp_pack(const f32x16& s, bf16x8& p0, bf16x8& p1) {
  u32x4 a, b;
#pragma unroll
  for (int i = 0; i < 4; ++i) {
    a[i] = pk2(__builtin_amdgcn_exp2f(s[2 * i]), __builtin_amdgcn_exp2f(s[2 * i + 1]));
    b[i] = pk2(__builtin_amdgcn_exp2f(s[8 + 2 * i]), __builtin_amdgcn_exp2f(s[9 + 2 * i]));
  }
  p0 = __builtin_bit_cast(bf16x8, a); p1 = __builtin_bit_cast(bf16x8, b);
}

__global__ __launch_bounds__(512, 2) void k_attn(const char* __restrict__ wo, const char* __restrict__ wg, const char* __restrict__ wu,
                                                 const char* __restrict__ wd, char* __restrict__ ws) {
  __shared__ __attribute__((aligned(16))) char smem[6 * SC_SLOT];
  const int bid = blockIdx.x, xcd = bid & 7, idx = bid >> 3, head = xcd * 4 + (idx >> 3), qt = idx & 7;
  const int tid = threadIdx.x, lane = tid & 63, wid = __builtin_amdgcn_readfirstlane(tid >> 6);
  const int r = lane & 31, h = lane >> 5;
  const unsigned lds0 = (unsigned)(uintptr_t)smem;
  const int tok = qt * 256 + wid * 32 + r;
  const char* kvb = ws + OFF_KV + (size_t)head * KV_HEAD;

  u32x2 q0, q1, q2;
  {
    const char* qp = ws + OFF_Q + (size_t)tok * 1280 + head * 40;
    const char* qa = qp + h * 16;
    asm volatile("global_load_dwordx2 %0, %1, off" : "=v"(q0) : "v"(qa) : "memory");
    asm volatile("global_load_dwordx2 %0, %1, off offset:8" : "=v"(q1) : "v"(qa) : "memory");
    asm volatile("global_load_dwordx2 %0, %1, off offset:32" : "=v"(q2) : "v"(qp) : "memory");
  }
  unsigned vo[3];
#pragma unroll
  for (int j = 0; j < 3; ++j) { const int c = j * 512 + tid; vo[j] = c < SC_BYTES / 16 ? c * 16 : 0; }
  auto issue = [&](int sc, int slot) {
#pragma unroll
    for (int j = 0; j < 3; ++j) glds16(vo[j], kvb + sc * SC_BYTES, lds0 + slot * SC_SLOT + j * 8192 + wid * 1024);
  };
#pragma unroll
  for (int s = 0; s < 6; ++s) issue(s, s);
  asm volatile("s_waitcnt vmcnt(15)\n\ts_barrier" : "+v"(q0), "+v"(q1), "+v"(q2) :: "memory");
  SBAR();
  const bf16x8 qA = __builtin_bit_cast(bf16x8, u32x4{q0[0], q0[1], q1[0], q1[1]});
  const bf16x4 qB = __builtin_bit_cast(bf16x4, h ? u32x2{0u, 0u} : q2);

  lds_cptr L = (lds_cptr)smem;
  const int rm = r <= 20 ? r : (r < 28 ? 20 : r - 16);
  const int kaO = lane * 16, kbO = 1024 + r * 8, vO = 1280 + h * 352 + rm * 16;
  f32x16 negm, o, sC;
#pragma unroll
  for (int i = 0; i < 16; ++i) { negm[i] = 0.f; o[i] = 0.f; }
  auto qk = [&](int tb) { return mfma32(lds_b128(L + tb + kaO), qA, mfma32k8(lds_b64(L + tb + kbO), qB, negm)); };
  sC = qk(0);
  { const float m = rowmax16(sC);
#pragma unroll
    for (int i = 0; i < 16; ++i) { negm[i] = -m; sC[i] -= m; } }

  auto chunk = [&](int so, int nxt, auto sync) {
#pragma unroll
    for (int t = 0; t < 8; ++t) {
      if (t == 7) sync();
      const f32x16 sN = qk(t < 7 ? so + (t + 1) * KV_TILE : nxt);
      bf16x8 p0, p1; exp_pack(sC, p0, p1);
      o = mfma32(lds_b128(L + so + t * KV_TILE + vO), p0, o);
      o = mfma32(lds_b128(L + so + t * KV_TILE + vO + 704), p1, o);
      sC = sN;
    }
  };
  chunk(0, SC_SLOT, [&] { wait_vm_bar<12>(); SBAR(); });
  chunk(SC_SLOT, 2 * SC_SLOT, [&] { asm volatile("s_waitcnt vmcnt(0) lgkmcnt(0)\n\ts_barrier" ::: "memory"); SBAR(); issue(6, 0); });
  chunk(2 * SC_SLOT, 3 * SC_SLOT, [&] { wait_lgkm_bar(); SBAR(); issue(7, 1); });

  const int job = bid * 8 + wid;
  const int n = lane & 15, g = lane >> 4;
  const char* sp; int pitch, nks; size_t dst; int kq = 0, nfj = 0;
  if (job < 1080) { kq = job / 216; nfj = job - kq * 216; sp = (n < 8 ? wg : wu) + (nfj * 8 + (n & 7)) * 2 + (size_t)(kq * 128) * (FF * 2); pitch = FF * 2; nks = 4;
                    dst = OFF_WGUF + (size_t)(nfj * 20 + kq * 4) * 1024; }
  else if (job < 1280) { const int j = job - 1080; kq = j / 40; nfj = j - kq * 40; sp = wo + (nfj * 16 + n) * 2 + (size_t)(kq * 128) * (DM * 2); pitch = DM * 2; nks = 4;
                    dst = OFF_WOF + (size_t)(nfj * 20 + kq * 4) * 1024; }
  else if (job < 1840) { const int j = job - 1280; kq = j / 40; nfj = j - kq * 40; sp = wd + (nfj * 16 + n) * 2 + (size_t)(kq * 128) * (DM * 2); pitch = DM * 2; nks = kq == 13 ? 2 : 4;
                    dst = OFF_WDF + (size_t)(nfj * 54 + kq * 4) * 1024; }
  else { sp = wo; pitch = 0; nks = 0; dst = 0; }
  unsigned short wv[4][8];
#pragma unroll
  for (int u = 0; u < 4; ++u)
#pragma unroll
    for (int j = 0; j < 8; ++j) wv[u][j] = *(const unsigned short*)(sp + (size_t)((u < nks ? u * 32 : 0) + 8 * g + j) * pitch);
  SBAR();

#pragma unroll 1
  for (int sc = 3; sc < 8; ++sc) {
    const int so = (sc < 6 ? sc : sc - 6) * SC_SLOT;
    const int nx = sc == 7 ? so : (sc + 1 < 6 ? sc + 1 : sc - 5) * SC_SLOT;
    chunk(so, nx, [&] { if (sc == 5) { wait_vm_bar<0>(); SBAR(); } });
  }

  {
    float csum = 0.f;
#pragma unroll
    for (int u = 0; u < 4; ++u) {
      u32x4 fr;
#pragma unroll
      for (int j = 0; j < 4; ++j) { fr[j] = (unsigned)wv[u][2 * j] | ((unsigned)wv[u][2 * j + 1] << 16); csum += bflo(fr[j]) + bfhi(fr[j]); }
      if (u < nks) *(u32x4*)(ws + dst + u * 1024 + lane * 16) = fr;
    }
    csum += __shfl_xor(csum, 16); csum += __shfl_xor(csum, 32);
    if (job < 1080 && lane < 16) *(float*)(ws + OFF_CSUM + (size_t)(kq * 3456 + nfj * 16 + n) * 4) = csum;
  }

  float l = h ? o[8] : o[12];
  {
    bool ok = l < 1.0e30f;
#pragma unroll
    for (int i = 0; i < 12; ++i) ok = ok && (fabsf(o[i]) < 3.0e38f);
    if (!__all(ok)) {
      float m = -INFINITY;
      f32x16 zero;
#pragma unroll
      for (int i = 0; i < 16; ++i) { o[i] = 0.f; zero[i] = 0.f; }
#pragma unroll 1
      for (int t = 0; t < 64; ++t) {
        const char* tb = kvb + t * KV_TILE;
        f32x16 s = mfma32(*(const bf16x8*)(tb + kaO), qA, mfma32k8(*(const bf16x4*)(tb + kbO), qB, zero));
        const float mn = fmaxf(m, rowmax16(s)), alpha = __builtin_amdgcn_exp2f(m - mn);
        m = mn;
#pragma unroll
        for (int i = 0; i < 16; ++i) { s[i] -= mn; o[i] *= alpha; }
        bf16x8 p0, p1; exp_pack(s, p0, p1);
        o = mfma32(*(const bf16x8*)(tb + vO), p0, o);
        o = mfma32(*(const bf16x8*)(tb + vO + 704), p1, o);
      }
      l = h ? o[8] : o[12];
    }
  }
  const float inv = 1.0f / l;
  char* ob = ws + OFF_OF + (size_t)(tok >> 4) * 20480 + (tok & 15) * 16;
#pragma unroll
  for (int u = 0; u < 3; ++u) {
    if (u < 2 || h == 0) {
      const int c = head * DH + 8 * u + 4 * h;
      u32x2 v; v[0] = pk2(o[4 * u] * inv, o[4 * u + 1] * inv); v[1] = pk2(o[4 * u + 2] * inv, o[4 * u + 3] * inv);
      *(u32x2*)(ob + (c >> 5) * 1024 + ((c >> 3) & 3) * 256 + ((c >> 2) & 1) * 8) = v;
    }
  }
}

constexpr int S_STAGE = 18 * 1024, S_SLOTS = 8;

template <int NST, int CNT>
DI void small_loader(const char* __restrict__ ws, unsigned lds0, int first, int lane, unsigned abase, unsigned bbase) {
  constexpr int NKS = 2 * NST;
  unsigned vo[CNT];
#pragma unroll
  for (int j = 0; j < CNT; ++j) {
    const int i = first + j;
    vo[j] = (i < 8 ? abase + ((i >> 1) * NKS + (i & 1)) * 1024 : bbase + (((i - 8) >> 1) * NKS + (i & 1)) * 1024) + lane * 16;
  }
  auto issue = [&](int st) {
    const unsigned base = lds0 + (st & (S_SLOTS - 1)) * S_STAGE + first * 1024;
#pragma unroll
    for (int j = 0; j < CNT; ++j) glds16(vo[j], ws + st * 2048, base + j * 1024);
  };
#pragma unroll
  for (int s = 0; s < S_SLOTS; ++s) issue(s);
  wait_vm_bar<7 * CNT>();
#pragma unroll 1
  for (int s = 1; s <= NST - 8; ++s) { wait_vm_bar<6 * CNT>(); issue(s + 7); }
  wait_vm_bar<6 * CNT>(); wait_vm_bar<5 * CNT>(); wait_vm_bar<4 * CNT>(); wait_vm_bar<3 * CNT>();
  wait_vm_bar<2 * CNT>(); wait_vm_bar<1 * CNT>(); wait_vm_bar<0>();
}

template <int NST, int MODE>
DI void small_gemm(char* smem, const char* __restrict__ x, char* __restrict__ ws, char* __restrict__ out) {
  constexpr int NKS = 2 * NST;
  const int bid = blockIdx.x, xcd = bid & 7, idx = bid >> 3;
  const int rt = (xcd >> 1) * 8 + (idx & 7), ct = (xcd & 1) * 4 + (idx >> 3);
  const int tid = threadIdx.x, lane = tid & 63, wid = __builtin_amdgcn_readfirstlane(tid >> 6);
  if (wid >= 4) {
    const unsigned lds0 = (unsigned)(uintptr_t)smem;
    const unsigned abase = (unsigned)(MODE == 0 ? OFF_OF : OFF_ACT) + rt * 4 * NKS * 1024, bbase = (unsigned)(MODE == 0 ? OFF_WOF : OFF_WDF) + ct * 5 * NKS * 1024;
    if (wid < 6) small_loader<NST, 5>(ws, lds0, (wid - 4) * 5, lane, abase, bbase);
    else small_loader<NST, 4>(ws, lds0, 10 + (wid - 6) * 4, lane, abase, bbase);
    return;
  }
  lds_cptr L = (lds_cptr)smem;
  const int rh = wid >> 1, kp = wid & 1, t16 = lane & 15, g = lane >> 4;
  const int aO = (4 * rh + kp) * 1024 + lane * 16, bO = (8 + kp) * 1024 + lane * 16;
  f32x4 acc[2][5];
#pragma unroll
  for (int u = 0; u < 2; ++u)
#pragma unroll
    for (int v = 0; v < 5; ++v) acc[u][v] = f32x4{0.f, 0.f, 0.f, 0.f};
  bf16x8 a0[2], b0[5], a1[2], b1[5];
  auto rd = [&](bf16x8 (&a)[2], bf16x8 (&b)[5], int s) {
    lds_cptr S = L + (s & (S_SLOTS - 1)) * S_STAGE;
#pragma unroll
    for (int u = 0; u < 2; ++u) a[u] = lds_b128(S + aO + u * 2048);
#pragma unroll
    for (int v = 0; v < 5; ++v) b[v] = lds_b128(S + bO + v * 2048);
  };
  auto mm = [&](bf16x8 (&a)[2], bf16x8 (&b)[5]) {
#pragma unroll
    for (int u = 0; u < 2; ++u)
#pragma unroll
      for (int v = 0; v < 5; ++v) acc[u][v] = mfma16(b[v], a[u], acc[u][v]);
  };
  wait_lgkm_bar(); SBAR(); rd(a0, b0, 0);
  int s = 1;
#pragma unroll 1
  for (; s + 1 < NST; s += 2) {
    wait_lgkm_bar(); SBAR(); rd(a1, b1, s); SBAR(); mm(a0, b0); SBAR();
    wait_lgkm_bar(); SBAR(); rd(a0, b0, s + 1); SBAR(); mm(a1, b1); SBAR();
  }
  if (NST % 2 == 0) { wait_lgkm_bar(); SBAR(); rd(a1, b1, NST - 1); SBAR(); mm(a0, b0); mm(a1, b1); }
  else mm(a0, b0);
  static_assert(((NST - 1) & (S_SLOTS - 1)) < 4, "scratch reuses ring slots 4 and 5, which are drained by now");
  lds_ptr scr = (lds_ptr)smem + 4 * S_STAGE;
#pragma unroll
  for (int v = 0; v < 5; ++v) *(LDS_AS f32x4*)(scr + wid * 5120 + v * 1024 + lane * 16) = kp ? acc[0][v] : acc[1][v];
  wait_lgkm_bar(); SBAR();
  const int mf = rt * 4 + 2 * rh + kp, tok = mf * 16 + t16;
  float ssum = 0.f, ssq = 0.f;
#pragma unroll
  for (int v = 0; v < 5; ++v) {
    const f32x4 oth = *(const LDS_AS f32x4*)(scr + (wid ^ 1) * 5120 + v * 1024 + lane * 16);
    const f32x4 r4 = (kp ? acc[1][v] : acc[0][v]) + oth;
    const int n = ct * 80 + v * 16 + 4 * g;
    const unsigned y0 = pk2(r4[0], r4[1]), y1 = pk2(r4[2], r4[3]);
    char* hp = ws + OFF_HF + (size_t)(mf * 20 + (n >> 5)) * 1024 + (t16 + 16 * ((n >> 3) & 3)) * 16 + ((n >> 2) & 1) * 8;
    const u32x2 res = MODE == 0 ? *(const u32x2*)(x + (size_t)tok * 1280 + n * 2) : *(const u32x2*)hp;
    u32x2 o; o[0] = pk2(bflo(res[0]) + bflo(y0), bfhi(res[0]) + bfhi(y0)); o[1] = pk2(bflo(res[1]) + bflo(y1), bfhi(res[1]) + bfhi(y1));
    if (MODE == 0) {
      *(u32x2*)hp = o;
      const float e0 = bflo(o[0]), e1 = bfhi(o[0]), e2 = bflo(o[1]), e3 = bfhi(o[1]);
      ssum += (e0 + e1) + (e2 + e3); ssq += (e0 * e0 + e1 * e1) + (e2 * e2 + e3 * e3);
    } else {
      *(u32x2*)(out + (size_t)tok * 1280 + n * 2) = o;
    }
  }
  if (MODE == 0) {
    ssum += __shfl_xor(ssum, 16); ssq += __shfl_xor(ssq, 16); ssum += __shfl_xor(ssum, 32); ssq += __shfl_xor(ssq, 32);
    if (g == 0) *(f32x2*)(ws + OFF_STAT + (size_t)tok * 64 + ct * 8) = f32x2{ssum, ssq};
  }
}
__global__ __launch_bounds__(512, 2) void k_wo(const char* __restrict__ x, char* __restrict__ ws) {
  __shared__ __attribute__((aligned(16))) char smem[S_SLOTS * S_STAGE];
  small_gemm<10, 0>(smem, x, ws, nullptr);
}
__global__ __launch_bounds__(512, 2) void k_down(char* __restrict__ ws, char* __restrict__ out) {
  __shared__ __attribute__((aligned(16))) char smem[S_SLOTS * S_STAGE];
  small_gemm<27, 1>(smem, nullptr, ws, out);
}

constexpr int G_STAGE = 44 * 1024;
__global__ __launch_bounds__(512, 2) void k_gateup(char* __restrict__ ws) {
  __shared__ __attribute__((aligned(16))) char smem[3 * G_STAGE + 2048];
  const int bid = blockIdx.x, xcd = bid & 7, idx = bid >> 3;
  const int rt = (xcd >> 2) * 8 + (idx & 7), ct = (xcd & 3) * 4 + (idx >> 3);
  const int nf0 = (ct >> 1) * 27 + (ct & 1) * 14, cnt = 14 - (ct & 1);
  const int tid = threadIdx.x, lane = tid & 63, wid = __builtin_amdgcn_readfirstlane(tid >> 6);
  if (wid >= 4) {
    const unsigned lds0 = (unsigned)(uintptr_t)smem;
    const int first = (wid - 4) * 11;
    unsigned vo[11];
#pragma unroll
    for (int j = 0; j < 11; ++j) {
      const int i = first + j;
      const int nf = min(nf0 + ((i - 16) >> 1), 215);
      vo[j] = (i < 16 ? (unsigned)OFF_HF + ((rt * 8 + (i >> 1)) * 20 + (i & 1)) * 1024 : (unsigned)OFF_WGUF + (nf * 20 + (i & 1)) * 1024) + lane * 16;
    }
    auto issue = [&](int st, int slot) {
      const unsigned base = lds0 + slot * G_STAGE + first * 1024;
#pragma unroll
      for (int j = 0; j < 11; ++j) glds16(vo[j], ws + st * 2048, base + j * 1024);
    };
    issue(0, 0); issue(1, 1); issue(2, 2);
    wait_vm_bar<22>();
#pragma unroll
    for (int s = 1; s < 10; ++s) {
      if (s <= 8) wait_vm_bar<11>(); else wait_vm_bar<0>();
      if (s + 2 < 10) issue(s + 2, (s + 2) % 3);
    }
    return;
  }
  lds_cptr L = (lds_cptr)smem;
  LDS_AS float* sst = (LDS_AS float*)((lds_ptr)smem + 3 * G_STAGE);
  LDS_AS float* scs = sst + 256;
  if (tid < 128) {
    const f32x4* sp = (const f32x4*)(ws + OFF_STAT + (size_t)(rt * 128 + tid) * 64);
    const f32x4 v0 = sp[0], v1 = sp[1], v2 = sp[2], v3 = sp[3];
    const float s = ((v0[0] + v0[2]) + (v1[0] + v1[2])) + ((v2[0] + v2[2]) + (v3[0] + v3[2]));
    const float qq = ((v0[1] + v0[3]) + (v1[1] + v1[3])) + ((v2[1] + v2[3]) + (v3[1] + v3[3]));
    const float mean = s * (1.0f / DM);
    *(LDS_AS f32x2*)(sst + tid * 2) = f32x2{mean, __builtin_amdgcn_rsqf(fmaxf(qq * (1.0f / DM) - mean * mean, 0.f) + LN_EPS)};
  }
  if (tid < 224) {
    const float* cp = (const float*)(ws + OFF_CSUM) + min(nf0 + (tid >> 4), 215) * 16 + (tid & 15);
    scs[tid] = ((cp[0] + cp[3456]) + (cp[2 * 3456] + cp[3 * 3456])) + cp[4 * 3456];
  }
  const int wr = wid >> 1, wc = wid & 1, t16 = lane & 15, g = lane >> 4;
  const int aO = wr * 8192 + lane * 16, bO = (16 + 14 * wc) * 1024 + lane * 16;
  f32x4 acc[4][7];
#pragma unroll
  for (int u = 0; u < 4; ++u)
#pragma unroll
    for (int v = 0; v < 7; ++v) acc[u][v] = f32x4{0.f, 0.f, 0.f, 0.f};
  bf16x8 a0[4], b0[7], a1[4], b1[7];
  auto rd = [&](bf16x8 (&a)[4], bf16x8 (&b)[7], int so) {
#pragma unroll
    for (int v = 0; v < 7; ++v) b[v] = lds_b128(L + so + bO + v * 2048);
#pragma unroll
    for (int u = 0; u < 4; ++u) a[u] = lds_b128(L + so + aO + u * 2048);
  };
  auto mm = [&](bf16x8 (&a)[4], bf16x8 (&b)[7]) {
#pragma unroll
    for (int u = 0; u < 4; ++u)
#pragma unroll
      for (int v = 0; v < 7; ++v) acc[u][v] = mfma16(b[v], a[u], acc[u][v]);
  };
  wait_lgkm_bar(); SBAR();
  rd(a0, b0, 0);
  int so = 0;
#pragma unroll 1
  for (int kt = 0; kt < 10; ++kt) {
    rd(a1, b1, so + 1024);
    SBAR();
    mm(a0, b0);
    SBAR();
    so = so == 2 * G_STAGE ? 0 : so + G_STAGE;
    if (kt < 9) { wait_lgkm_bar(); SBAR(); rd(a0, b0, so); SBAR(); }
    mm(a1, b1);
    SBAR();
  }
  const int up = lane >> 5;
#pragma unroll
  for (int u = 0; u < 4; ++u) {
    const int rl = 64 * wr + 16 * u + t16;
    const f32x2 st = *(const LDS_AS f32x2*)(sst + rl * 2);
#pragma unroll
    for (int v = 0; v < 7; ++v) {
      const f32x4 c4 = *(const LDS_AS f32x4*)(scs + (7 * wc + v) * 16 + 4 * g);
#pragma unroll
      for (int i = 0; i < 4; ++i) acc[u][v][i] = st[1] * (acc[u][v][i] - st[0] * c4[i]);
    }
    char* ab = ws + OFF_ACT + (size_t)(rt * 8 + 4 * wr + u) * 54 * 1024 + t16 * 16 + (g & 1) * 8;
#pragma unroll
    for (int pr = 0; pr < 4; ++pr) {
      const int v0 = 2 * pr, v1 = pr < 3 ? v0 + 1 : v0;
      float a[4];
#pragma unroll
      for (int i = 0; i < 4; ++i) {
        auto rr = __builtin_amdgcn_permlane32_swap(__float_as_uint(acc[u][v0][i]), __float_as_uint(acc[u][v1][i]), false, false);
        const float gt = __uint_as_float(rr[0]), upv = __uint_as_float(rr[1]);
        a[i] = gt * __builtin_amdgcn_rcpf(1.0f + __builtin_amdgcn_exp2f(gt * -1.4426950408889634f)) * upv;
      }
      const int fl = 7 * wc + v0 + (pr < 3 ? up : 0), nf = nf0 + fl;
      if (fl < cnt && (pr < 3 || up == 0)) *(u32x2*)(ab + (nf >> 2) * 1024 + (nf & 3) * 256) = u32x2{pk2(a[0], a[1]), pk2(a[2], a[3])};
    }
  }
}

extern "C" void kernel_launch(void* const* d_in, const int* in_sizes, int n_in, void* d_out, int out_size, void* d_ws, size_t ws_size,
                              hipStream_t stream) {
  const char* x = (const char*)d_in[0]; const char* wqkv = (const char*)d_in[1]; const char* wo = (const char*)d_in[2];
  const char* wg = (const char*)d_in[4]; const char* wu = (const char*)d_in[5]; const char* wd = (const char*)d_in[6];
  char* ws = (char*)d_ws;
  k_qkv<<<256, 512, 0, stream>>>(x, wqkv, ws);
  k_attn<<<256, 512, 0, stream>>>(wo, wg, wu, wd, ws);
  k_wo<<<256, 512, 0, stream>>>(x, ws);
  k_gateup<<<256, 512, 0, stream>>>(ws);
  k_down<<<256, 512, 0, stream>>>(ws, (char*)d_out);
}
```

```cpp
#include <hip/hip_runtime.h>
#include <hip/hip_bf16.h>
#include <stdint.h>

typedef __attribute__((ext_vector_type(8))) short bf16x8;
typedef __attribute__((ext_vector_type(4))) short bf16x4;
typedef __attribute__((ext_vector_type(2))) float f32x2;
typedef __attribute__((ext_vector_type(4))) float f32x4;
typedef __attribute__((ext_vector_type(16))) float f32x16;
typedef __attribute__((ext_vector_type(4))) unsigned u32x4;
typedef __attribute__((ext_vector_type(2))) unsigned u32x2;
typedef __attribute__((ext_vector_type(2))) __bf16 bf2;
typedef __attribute__((address_space(3))) const char* lds_cptr;
typedef __attribute__((address_space(3))) char* lds_ptr;
#define LDS_AS __attribute__((address_space(3)))
#define DI __device__ __forceinline__

constexpr int SEQ = 2048, DM = 640, NH = 32, DH = 20, FF = 1728;
constexpr float LN_EPS = 1e-5f;
constexpr float LOG2E = 1.4426950408889634f;
constexpr float QSCALE = 0.22360679774997896f * LOG2E;

constexpr int Q_TILE = 1280;
constexpr int Q_HEAD = 64 * Q_TILE;
constexpr int KV_TILE = 2688;
constexpr int KV_HEAD = 64 * KV_TILE;
constexpr unsigned OFF_Q    = 0;
constexpr unsigned OFF_KV   = OFF_Q + NH * Q_HEAD;
constexpr unsigned OFF_OF   = OFF_KV + NH * KV_HEAD;
constexpr unsigned OFF_HF   = OFF_OF + SEQ * DM * 2;
constexpr unsigned OFF_ACT  = OFF_HF + SEQ * DM * 2;
constexpr unsigned OFF_WOF  = OFF_ACT + SEQ * FF * 2;
constexpr unsigned OFF_WGUF = OFF_WOF + DM * DM * 2;
constexpr unsigned OFF_WDF  = OFF_WGUF + DM * FF * 4;
constexpr unsigned OFF_STAT = OFF_WDF + FF * DM * 2;
constexpr unsigned OFF_CSUM = OFF_STAT + SEQ * 64;

#define PROF_DECL
#define STAMP(i)
#define PROF_END(k)
#define PROF_ARG
#define PROF_PASS
DI void glds16(unsigned voff, const void* sbase, unsigned lds) {
  unsigned keep;
  asm volatile("s_mov_b32 %0, m0\n\ts_mov_b32 m0, %3\n\ts_nop 0\n\tglobal_load_lds_dwordx4 %1, %2\n\ts_mov_b32 m0, %0"
               : "=&s"(keep) : "v"(voff), "s"(sbase), "s"(lds) : "memory");
}
template <int N> DI void wait_vm_bar() { asm volatile("s_waitcnt vmcnt(%0)\n\ts_barrier" :: "n"(N) : "memory"); }
template <int N> DI void wait_vm() { asm volatile("s_waitcnt vmcnt(%0)" :: "n"(N) : "memory"); }
#define SBAR() __builtin_amdgcn_sched_barrier(0)
#define SGB(mask, n) __builtin_amdgcn_sched_group_barrier(mask, n, 0)
DI void wait_lgkm_bar() { SBAR(); __builtin_amdgcn_s_waitcnt(0xC07F); asm volatile("s_barrier" ::: "memory"); SBAR(); }
#define GEMM_OFF 0
template <int I> struct IC { static constexpr int value = I; };
template <int B, int E, class F> DI void static_for(F&& f) { if constexpr (B < E) { f(IC<B>{}); static_for<B + 1, E>(f); } }
DI bf16x8 lds_b128(lds_cptr p) { return *(const LDS_AS bf16x8*)p; }
DI bf16x4 lds_b64(lds_cptr p) { return *(const LDS_AS bf16x4*)p; }
DI bf16x4 lds_tr(lds_cptr p) { return __builtin_amdgcn_ds_read_tr16_b64_v4i16((LDS_AS bf16x4*)p); }
DI f32x4 mfma16(bf16x8 a, bf16x8 b, f32x4 c) { return __builtin_amdgcn_mfma_f32_16x16x32_bf16(a, b, c, 0, 0, 0); }
DI f32x16 mfma32(bf16x8 a, bf16x8 b, f32x16 c) { return __builtin_amdgcn_mfma_f32_32x32x16_bf16(a, b, c, 0, 0, 0); }
DI f32x16 mfma32k8(bf16x4 a, bf16x4 b, f32x16 c) { return __builtin_amdgcn_mfma_f32_32x32x8bf16_1k(a, b, c, 0, 0, 0); }
DI unsigned pk2(float lo, float hi) { f32x2 v = {lo, hi}; return __builtin_bit_cast(unsigned, __builtin_convertvector(v, bf2)); }
DI float bflo(unsigned u) { return __uint_as_float(u << 16); }
DI float bfhi(unsigned u) { return __uint_as_float(u & 0xffff0000u); }
DI int div20(int c) { return (c * 3277) >> 16; }
DI float xsum32(float v) { auto r = __builtin_amdgcn_permlane32_swap(__float_as_uint(v), __float_as_uint(v), false, false); return __uint_as_float(r[0]) + __uint_as_float(r[1]); }
DI float xsum16(float v) { auto r = __builtin_amdgcn_permlane16_swap(__float_as_uint(v), __float_as_uint(v), false, false); return __uint_as_float(r[0]) + __uint_as_float(r[1]); }
constexpr bf16x8 ONES8 = {0x3f80, 0x3f80, 0x3f80, 0x3f80, 0x3f80, 0x3f80, 0x3f80, 0x3f80};

template <int MODE>
DI void qkv_compute(lds_cptr L, lds_ptr scr, int wid, int lane, int row0, int col0, char* __restrict__ ws PROF_ARG) {
  const int wr = wid >> 1, wc = wid & 1, r16 = lane & 15, g = lane >> 4, q = r16 >> 2, p = lane & 3;
  const int f = q | ((g & 1) << 2);
  const int rp = MODE == 2 ? 16 * (r16 >> 3) + (r16 & 7) : r16;
  constexpr int RSTEP = MODE == 2 ? 8 : 16;
  int aoff[2][2], boff[4];
#pragma unroll
  for (int e = 0; e < 2; ++e)
#pragma unroll
    for (int ks = 0; ks < 2; ++ks) { const int r = rp + e * RSTEP; aoff[e][ks] = (64 * wr + r) * 128 + (((4 * ks + g) ^ ((r >> 1) & 7) ^ ((r >> 2) & 4)) << 4); }
#pragma unroll
  for (int jj = 0; jj < 4; ++jj) { const int j = (jj + 2 * wr) & 3; boff[jj] = 16384 + (8 * g + q) * 256 + ((((4 * wc + j) ^ f) * 2 + (p >> 1)) << 4) + (p & 1) * 8; }
  const int pr0 = wc * 4096, pr1 = 4096 - pr0;

  f32x4 acc[4][4], rs[2], gr[2], cs[2];
#pragma unroll
  for (int i = 0; i < 4; ++i)
#pragma unroll
    for (int j = 0; j < 4; ++j) acc[i][j] = f32x4{0.f, 0.f, 0.f, 0.f};
#pragma unroll
  for (int i = 0; i < 2; ++i) { rs[i] = f32x4{0.f, 0.f, 0.f, 0.f}; gr[i] = rs[i]; cs[i] = rs[i]; }
  bf16x8 a0[4], b0[4], a1[4], b1[4];

  auto rd = [&](bf16x8 (&a)[4], bf16x8 (&b)[4], int slot, int ks) {
    if (GEMM_OFF && slot + ks > 0) return;
    lds_cptr S = L + slot;
#pragma unroll
    for (int jj = 0; jj < 4; ++jj) {
      bf16x4 lo = lds_tr(S + boff[jj] + ks * 8192), hi = lds_tr(S + boff[jj] + ks * 8192 + 1024);
      b[jj] = __builtin_shufflevector(lo, hi, 0, 1, 2, 3, 4, 5, 6, 7);
      a[jj] = lds_b128(S + aoff[jj & 1][ks] + (jj < 2 ? pr0 : pr1));
    }
  };
  auto mm = [&](bf16x8 (&a)[4], bf16x8 (&b)[4]) {
    if (GEMM_OFF) return;
#pragma unroll
    for (int ii = 0; ii < 4; ++ii)
#pragma unroll
      for (int jj = 0; jj < 4; ++jj)
        acc[ii][jj] = (MODE == 2) ? mfma16(a[ii], b[jj], acc[ii][jj]) : mfma16(b[jj], a[ii], acc[ii][jj]);
#pragma unroll
    for (int u = 0; u < 2; ++u) {
      rs[u] = mfma16(a[u], ONES8, rs[u]);
      gr[u] = mfma16(a[u], a[u], gr[u]);
      cs[u] = mfma16(ONES8, b[u], cs[u]);
    }
  };
  auto ilv = [&] {
#pragma unroll
    for (int i = 0; i < 12; ++i) { SGB(0x8, 1); SGB(0x100, 1); }
    SGB(0x8, 10);
    SBAR();
  };

  wait_lgkm_bar();
  STAMP(1);
  rd(a0, b0, 0, 0);
  SBAR();
#pragma unroll 1
  for (int kt = 0; kt < 9; ++kt) {
    rd(a1, b1, (kt & 3) * 32768, 1); mm(a0, b0); ilv();
    wait_lgkm_bar();
    rd(a0, b0, ((kt + 1) & 3) * 32768, 0); mm(a1, b1); ilv();
  }
  rd(a1, b1, 32768, 1); mm(a0, b0); ilv();
  mm(a1, b1);
  SBAR();
  STAMP(2);
  LDS_AS float* rsum = (LDS_AS float*)scr; LDS_AS float* rsq = rsum + 128; LDS_AS float* csum = rsum + 256;
#pragma unroll
  for (int u = 0; u < 2; ++u) {
    const int rb = 64 * wr + 32 * wc + 16 * u;
    if (r16 == 0) *(LDS_AS f32x4*)(rsum + rb + 4 * g) = rs[u];
    if (q == g) { const float d = p == 0 ? gr[u][0] : p == 1 ? gr[u][1] : p == 2 ? gr[u][2] : gr[u][3]; rsq[rb + r16] = d; }
    if (g == 0) csum[64 * wc + 32 * wr + 16 * u + r16] = cs[u][0];
  }
  constexpr float invD = 1.0f / DM;
  if (MODE != 2) {
    constexpr int TS = MODE == 0 ? Q_TILE : KV_TILE, HS = MODE == 0 ? Q_HEAD : KV_HEAD;
    unsigned off[4][2];
#pragma unroll
    for (int jj = 0; jj < 4; ++jj) {
      const int ck = col0 - MODE * DM + 64 * wc + 16 * ((jj + 2 * wr) & 3) + 4 * g, head = div20(ck), d = ck - head * DH;
      off[jj][0] = head * HS + (d < 16 ? (d >> 3) * 512 + (d & 7) * 2 + r16 * 16 : 1024 + r16 * 8);
      off[jj][1] = off[jj][0] + (d < 16 ? 256 : 128);
    }
    char* base = ws + (MODE == 0 ? OFF_Q : OFF_KV) + ((row0 >> 5) + 2 * wr) * TS;
    wait_lgkm_bar();
    f32x4 c4[4];
#pragma unroll
    for (int jj = 0; jj < 4; ++jj) c4[jj] = *(const LDS_AS f32x4*)(csum + 64 * wc + 16 * ((jj + 2 * wr) & 3) + 4 * g);
#pragma unroll
    for (int ii = 0; ii < 4; ++ii) {
      const int half = ((ii >> 1) + wc) & 1, rl = 64 * wr + 32 * half + 16 * (ii & 1) + r16;
      const float mean = rsum[rl] * invD, var = fmaxf(rsq[rl] * invD - mean * mean, 0.f);
      const float rstd = __builtin_amdgcn_rsqf(var + LN_EPS) * (MODE == 0 ? QSCALE : 1.0f), nb = -mean * rstd;
      char* tb = base + half * TS;
#pragma unroll
      for (int jj = 0; jj < 4; ++jj) {
        const f32x4 v = acc[ii][jj] * rstd + c4[jj] * nb;
        *(u32x2*)(tb + off[jj][ii & 1]) = u32x2{pk2(v[0], v[1]), pk2(v[2], v[3])};
      }
    }
  } else {
    unsigned off[4];
#pragma unroll
    for (int jj = 0; jj < 4; ++jj) {
      const int cv = col0 - 2 * DM + 64 * wc + 16 * ((jj + 2 * wr) & 3) + r16, head = div20(cv), d = cv - head * DH;
      off[jj] = head * KV_HEAD + 1280 + g * 352 + d * 16;
    }
    char* base = ws + OFF_KV + ((row0 >> 5) + 2 * wr) * KV_TILE;
    { const int head = div20(col0 - 2 * DM + 19) + (lane >> 3), t = lane & 7;
      if (wc == 0 && head * DH < col0 - 2 * DM + 128) *(bf16x8*)(base + head * KV_HEAD + (t >> 2) * KV_TILE + 1280 + (t & 3) * 352 + 320) = ONES8; }
    wait_lgkm_bar();
    float c1[4];
#pragma unroll
    for (int jj = 0; jj < 4; ++jj) c1[jj] = csum[64 * wc + 16 * ((jj + 2 * wr) & 3) + r16];
#pragma unroll
    for (int ip = 0; ip < 2; ++ip) {
      const int half = (ip + wc) & 1;
      f32x4 rstd[2], nb[2];
#pragma unroll
      for (int e = 0; e < 2; ++e) {
        const int rl = 64 * wr + 32 * half + 16 * e + 4 * g;
        const f32x4 mean = *(const LDS_AS f32x4*)(rsum + rl) * invD, q4 = *(const LDS_AS f32x4*)(rsq + rl);
#pragma unroll
        for (int i = 0; i < 4; ++i) rstd[e][i] = __builtin_amdgcn_rsqf(fmaxf(q4[i] * invD - mean[i] * mean[i], 0.f) + LN_EPS);
        nb[e] = -mean * rstd[e];
      }
      char* tb = base + half * KV_TILE;
#pragma unroll
      for (int jj = 0; jj < 4; ++jj) {
        const f32x4 v0 = acc[2 * ip][jj] * rstd[0] + nb[0] * c1[jj], v1 = acc[2 * ip + 1][jj] * rstd[1] + nb[1] * c1[jj];
        *(u32x4*)(tb + off[jj]) = u32x4{pk2(v0[0], v0[1]), pk2(v0[2], v0[3]), pk2(v1[0], v1[1]), pk2(v1[2], v1[3])};
      }
    }
  }
}

__global__ __launch_bounds__(512, 2) void k_qkv(const char* __restrict__ x, const char* __restrict__ w, char* __restrict__ ws) {
  __shared__ __attribute__((aligned(16))) char smem[4 * 32768 + 2048];
  const int bid = blockIdx.x, xcd = bid & 7, idx = bid >> 3;
  const int rt = (xcd >> 1) * 4 + (idx & 3), ct = (xcd & 1) * 8 + (idx >> 2);
  if (ct >= 15) return;
  PROF_DECL STAMP(0);
  const int tid = threadIdx.x, lane = tid & 63, wid = __builtin_amdgcn_readfirstlane(tid >> 6);
  if (wid >= 4) {
    const int lw = wid - 4;
    const unsigned lds0 = (unsigned)(uintptr_t)smem + lw * 1024;
    const int ra = 8 * lw + (lane >> 3), rb = 4 * lw + (lane >> 4), f = (rb & 3) | ((rb >> 1) & 4);
    const unsigned voA = ra * 1280 + (((lane & 7) ^ ((ra >> 1) & 7) ^ ((ra >> 2) & 4)) << 4), voB = rb * 3840 + (((lane & 15) ^ (f << 1)) << 4);
    const char* pa = x + (size_t)rt * 128 * 1280; const char* pb = w + ct * 256;
    auto issue = [&](int st) {
      const unsigned base = lds0 + (st & 3) * 32768;
#pragma unroll
      for (int j = 0; j < 4; ++j) { glds16(voA, pa + st * 128 + j * 32 * 1280, base + j * 4096); glds16(voB, pb + (st * 64 + j * 16) * 3840, base + 16384 + j * 4096); }
    };
    STAMP(1); issue(0); issue(1); STAMP(2);
    wait_vm_bar<8>(); STAMP(3);
    issue(2); issue(3);
#pragma unroll 1
    for (int s = 1; s < 7; ++s) { wait_vm_bar<16>(); issue(s + 3); }
    wait_vm_bar<16>(); wait_vm_bar<8>(); wait_vm_bar<0>();
    PROF_END(0);
    return;
  }
  lds_cptr L = (lds_cptr)smem; lds_ptr scr = (lds_ptr)smem + 4 * 32768;
  if (ct < 5) qkv_compute<0>(L, scr, wid, lane, rt * 128, ct * 128, ws PROF_PASS);
  else if (ct < 10) qkv_compute<1>(L, scr, wid, lane, rt * 128, ct * 128, ws PROF_PASS);
  else qkv_compute<2>(L, scr, wid, lane, rt * 128, ct * 128, ws PROF_PASS);
  PROF_END(0);
}

constexpr int SC_BYTES = 8 * KV_TILE;
constexpr int STG_OFF = 6 * SC_BYTES;

DI float rowmax16(const f32x16& s) {
  float a = fmaxf(fmaxf(s[0], s[1]), s[2]), b = fmaxf(fmaxf(s[3], s[4]), s[5]);
  a = fmaxf(fmaxf(a, s[6]), s[7]); b = fmaxf(fmaxf(b, s[8]), s[9]);
  a = fmaxf(fmaxf(a, s[10]), s[11]); b = fmaxf(fmaxf(b, s[12]), s[13]);
  a = fmaxf(fmaxf(a, s[14]), s[15]);
  const float m = fmaxf(a, b);
  auto rr = __builtin_amdgcn_permlane32_swap(__float_as_uint(m), __float_as_uint(m), false, false);
  return fmaxf(__uint_as_float(rr[0]), __uint_as_float(rr[1]));
}
DI unsigned exp_pk(float a, float b) { return pk2(__builtin_amdgcn_exp2f(a), __builtin_amdgcn_exp2f(b)); }

template <int KIND>
DI void retile_issue(const char* __restrict__ w0, const char* __restrict__ w1, int tile, int ks, int lane, unsigned stg) {
  if (KIND == 0) {
    const unsigned vo = (ks * 32 + (lane >> 3)) * (FF * 2) + tile * 128 + (lane & 7) * 16;
#pragma unroll
    for (int j = 0; j < 4; ++j) { glds16(vo, w0 + j * 8 * (FF * 2), stg + j * 1024); glds16(vo, w1 + j * 8 * (FF * 2), stg + 4096 + j * 1024); }
  } else {
#pragma unroll
    for (int j = 0; j < 8; ++j) {
      int k = ks * 32 + 4 * j + (lane >> 4);
      if (KIND == 2) { const int pp = div20(k) >> 1, e = k - 40 * pp; k = 40 * pp + (e < 16 ? e : e < 32 ? e + 4 : e < 36 ? e - 16 : e); }
      glds16(k * (DM * 2) + tile * 256 + (lane & 15) * 16, w0, stg + j * 1024);
    }
  }
}
template <int KIND>
DI void retile_store(char* __restrict__ ws, lds_cptr S, int tile, int ks, int lane) {
  const int g = lane >> 4, q = (lane & 15) >> 2, p = lane & 3;
#pragma unroll
  for (int f = 0; f < 8; ++f) {
    lds_cptr a; int hi; unsigned dst;
    if (KIND == 0) {
      a = S + (p >> 1) * 4096 + (8 * g + q) * 128 + (p & 1) * 16 + (32 * (f >> 2) + 16 * (f & 1) + 4 * ((f >> 1) & 1)) * 2; hi = 512;
      dst = OFF_WGUF + ((8 * tile + f) * 20 + ks) * 1024;
    } else {
      const int U = 4 * tile + (f >> 1), j = f & 1;
      a = S + (8 * g + q) * 256 + (32 * (f >> 1) + (U % 5 == 2 ? 16 * j + 4 * p : 8 * p + 4 * j)) * 2; hi = 1024;
      dst = (KIND == 1 ? OFF_WDF : OFF_WOF) + ((2 * U + j) * (KIND == 1 ? 54 : 20) + ks) * 1024;
    }
    const bf16x8 fr = __builtin_shufflevector(lds_tr(a), lds_tr(a + hi), 0, 1, 2, 3, 4, 5, 6, 7);
    *(bf16x8*)(ws + dst + lane * 16) = fr;
    if (KIND == 0) {
      const f32x4 c = mfma16(fr, ONES8, f32x4{0.f, 0.f, 0.f, 0.f});
      if ((lane & 15) == 0) *(f32x4*)(ws + OFF_CSUM + ((ks * 216 + 8 * tile + f) * 16 + 4 * g) * 4) = c;
    }
  }
}

__global__ __launch_bounds__(768, 3) void k_attn(const char* __restrict__ wo, const char* __restrict__ wg, const char* __restrict__ wu,
                                                 const char* __restrict__ wd, char* __restrict__ ws) {
  __shared__ __attribute__((aligned(16))) char smem[6 * SC_BYTES + 4 * 8192];
  const int bid = blockIdx.x, xcd = bid & 7, idx = bid >> 3, head = xcd * 4 + (idx >> 3), qt = idx & 7;
  const int tid = threadIdx.x, lane = tid & 63, wid = __builtin_amdgcn_readfirstlane(tid >> 6);
  PROF_DECL STAMP(0);
  lds_cptr L = (lds_cptr)smem;
  if (wid >= 8) {
    const int hw = wid - 8, kh = hw >> 1, sub = hw & 1;
    const unsigned lds0 = (unsigned)(uintptr_t)smem, vo = lane * 16;
    const char* src = ws + OFF_KV + head * KV_HEAD + kh * 4 * SC_BYTES + sub * 1024;
    const unsigned dst = lds0 + kh * SC_BYTES + sub * 1024;
    auto piece = [&](int sc, int slot, int i) { const int o = (i == 10 ? (sub ? 9 : 10) : i) * 2048; glds16(vo, src + sc * SC_BYTES + o, dst + slot * 2 * SC_BYTES + o); };
#pragma unroll
    for (int i = 0; i < 4; ++i) piece(0, 0, i);
    STAMP(1);
    wait_vm_bar<0>();
    STAMP(2);
#pragma unroll
    for (int i = 4; i < 11; ++i) piece(0, 0, i);
    wait_vm_bar<0>();
#pragma unroll
    for (int i = 0; i < 11; ++i) piece(1, 1, i);
#pragma unroll
    for (int i = 0; i < 11; ++i) piece(2, 2, i);
    const int job = bid * 4 + hw;
    const unsigned stg = lds0 + STG_OFF + hw * 8192;
    int tile = 0, ks = 0;
    if (job < 540)      { ks = job / 27; tile = job - ks * 27; retile_issue<0>(wg, wu, tile, ks, lane, stg); }
    else if (job < 810) { ks = (job - 540) / 5; tile = job - 540 - ks * 5; retile_issue<1>(wd, wd, tile, ks, lane, stg); }
    else                { ks = min(job - 810, 99) / 5; tile = min(job - 810, 99) - ks * 5; retile_issue<2>(wo, wo, tile, ks, lane, stg); }
    wait_vm_bar<8>();
    STAMP(3);
#pragma unroll
    for (int i = 0; i < 11; ++i) piece(3, 0, i);
    wait_vm<11>();
    lds_cptr S = L + STG_OFF + hw * 8192;
    if (job < 540) retile_store<0>(ws, S, tile, ks, lane);
    else if (job < 810) retile_store<1>(ws, S, tile, ks, lane);
    else if (job < 910) retile_store<2>(ws, S, tile, ks, lane);
    STAMP(4);
    wait_vm_bar<0>();
    PROF_END(1);
    return;
  }
  const int r = lane & 31, h = lane >> 5, qp = wid & 3, kh = wid >> 2;
  const char* qb = ws + OFF_Q + head * Q_HEAD + (qt * 8 + qp * 2) * Q_TILE;
  const bf16x8 qaA = *(const bf16x8*)(qb + lane * 16), qaB = *(const bf16x8*)(qb + Q_TILE + lane * 16);
  u32x2 qtA = *(const u32x2*)(qb + 1024 + r * 8), qtB = *(const u32x2*)(qb + Q_TILE + 1024 + r * 8);
  if (h) { qtA = u32x2{0u, 0u}; qtB = qtA; }

  const int rm = r <= 20 ? r : (r < 28 ? 20 : r - 16);
  const int kaO = lane * 16, kbO = h ? 1280 + 320 : 1024 + r * 8, vO = 1280 + h * 352 + rm * 16;
  f32x16 zero, oA, oB, sA, sB;
#pragma unroll
  for (int i = 0; i < 16; ++i) { zero[i] = 0.f; oA[i] = 0.f; oB[i] = 0.f; }
  bf16x8 ka[2], v0[2], v1[2]; bf16x4 kb[2];
  lds_cptr pk = L + kh * SC_BYTES + kaO, pb = L + kh * SC_BYTES + kbO, pv = L + kh * SC_BYTES + vO;
  wait_lgkm_bar();
  STAMP(1);
  kb[0] = lds_b64(pb); ka[0] = lds_b128(pk); v0[0] = lds_b128(pv); v1[0] = lds_b128(pv + 704);
  v1[1] = v1[0];
  float mA, mB;
  {
    sA = mfma32(ka[0], qaA, mfma32k8(kb[0], __builtin_bit_cast(bf16x4, qtA), zero));
    sB = mfma32(ka[0], qaB, mfma32k8(kb[0], __builtin_bit_cast(bf16x4, qtB), zero));
    const unsigned nA = pk2(-rowmax16(sA), 0.f), nB = pk2(-rowmax16(sB), 0.f);
    mA = -bflo(nA); mB = -bflo(nB);
    if (h) { qtA[0] = nA; qtB[0] = nB; }
  }
  const bf16x4 qbA = __builtin_bit_cast(bf16x4, qtA), qbB = __builtin_bit_cast(bf16x4, qtB);
  sA = mfma32(ka[0], qaA, mfma32k8(kb[0], qbA, zero));
  u32x4 pl = {0u, 0u, 0u, 0u};

  auto phase = [&](const f32x16& sC, f32x16& oC, f32x16& oP, bf16x8 vP, f32x16& sN, bf16x8 qa, bf16x4 qb4, bf16x8 kA, bf16x4 kB, bf16x8 vC, auto&& reads) {
    u32x4 p0;
    oP = mfma32(vP, __builtin_bit_cast(bf16x8, pl), oP); reads(); SBAR();
    p0[0] = exp_pk(sC[0], sC[1]); p0[1] = exp_pk(sC[2], sC[3]); SBAR();
    sN = mfma32k8(kB, qb4, zero); SBAR();
    p0[2] = exp_pk(sC[4], sC[5]); p0[3] = exp_pk(sC[6], sC[7]); SBAR();
    sN = mfma32(kA, qa, sN); SBAR();
    pl[0] = exp_pk(sC[8], sC[9]); pl[1] = exp_pk(sC[10], sC[11]); SBAR();
    oC = mfma32(vC, __builtin_bit_cast(bf16x8, p0), oC); SBAR();
    pl[2] = exp_pk(sC[12], sC[13]); pl[3] = exp_pk(sC[14], sC[15]); SBAR();
  };
#pragma unroll 1
  for (int sc = 0; sc < 4; ++sc) {
    const int dn = sc == 2 ? -4 * SC_BYTES : sc == 3 ? 0 : 2 * SC_BYTES;
#pragma unroll
    for (int t = 0; t < 8; ++t) {
      const int c = t & 1, n = c ^ 1, no = t < 7 ? (t + 1) * KV_TILE : dn;
      if (t == 2 && sc == 0) wait_lgkm_bar();
      if (t == 7 && !(sc & 1)) wait_lgkm_bar();
      phase(sA, oA, oB, v1[n], sB, qaB, qbB, ka[c], kb[c], v0[c], [&] { kb[n] = lds_b64(pb + no); ka[n] = lds_b128(pk + no); });
      phase(sB, oB, oA, v1[c], sA, qaA, qbA, ka[n], kb[n], v0[c], [&] { v0[n] = lds_b128(pv + no); v1[n] = lds_b128(pv + no + 704); });
    }
    pk += dn; pb += dn; pv += dn;
  }
  oB = mfma32(v1[1], __builtin_bit_cast(bf16x8, pl), oB);
  STAMP(2);

  {
    const float lA = h ? oA[8] : oA[12], lB = h ? oB[8] : oB[12];
    bool ok = lA < 1.0e18f && lA > 1.0e-18f && lB < 1.0e18f && lB > 1.0e-18f;
#pragma unroll
    for (int i = 0; i < 12; ++i) ok = ok && fabsf(oA[i]) < 3.0e38f && fabsf(oB[i]) < 3.0e38f;
    if (!__all(ok)) {
      const char* kvb = ws + OFF_KV + head * KV_HEAD + kh * 4 * SC_BYTES;
      const bf16x4 z4 = {0, 0, 0, 0}, tA = h ? z4 : qbA, tB = h ? z4 : qbB;
      mA = -INFINITY; mB = -INFINITY; oA = zero; oB = zero;
      auto upd = [&](f32x16 s, float& m, f32x16& o, bf16x8 w0, bf16x8 w1) {
        const float mn = fmaxf(m, rowmax16(s)), alpha = __builtin_amdgcn_exp2f(m - mn);
        m = mn; o *= alpha;
        u32x4 a, b;
#pragma unroll
        for (int i = 0; i < 4; ++i) { a[i] = exp_pk(s[2 * i] - mn, s[2 * i + 1] - mn); b[i] = exp_pk(s[8 + 2 * i] - mn, s[9 + 2 * i] - mn); }
        o = mfma32(w1, __builtin_bit_cast(bf16x8, b), mfma32(w0, __builtin_bit_cast(bf16x8, a), o));
      };
#pragma unroll 1
      for (int t = 0; t < 32; ++t) {
        const char* tb = kvb + t * KV_TILE;
        const bf16x8 k8 = *(const bf16x8*)(tb + kaO), w0 = *(const bf16x8*)(tb + vO), w1 = *(const bf16x8*)(tb + vO + 704);
        const bf16x4 k4 = *(const bf16x4*)(tb + kbO);
        upd(mfma32(k8, qaA, mfma32k8(k4, tA, zero)), mA, oA, w0, w1);
        upd(mfma32(k8, qaB, mfma32k8(k4, tB, zero)), mB, oB, w0, w1);
      }
    }
  }
  const f32x16 oS = kh ? oA : oB, oK = kh ? oB : oA;
  const float mS = kh ? mA : mB, mK = kh ? mB : mA;
  lds_ptr X = (lds_ptr)smem + 2 * SC_BYTES + lane * 16;
#pragma unroll
  for (int u = 0; u < 3; ++u) *(LDS_AS f32x4*)(X + wid * 4096 + u * 1024) = f32x4{oS[4 * u], oS[4 * u + 1], oS[4 * u + 2], oS[4 * u + 3]};
  *(LDS_AS f32x2*)(X + wid * 4096 + 3072) = f32x2{oS[12], mS};
  wait_lgkm_bar();
  f32x4 y[3];
#pragma unroll
  for (int u = 0; u < 3; ++u) y[u] = *(const LDS_AS f32x4*)(X + (wid ^ 4) * 4096 + u * 1024);
  const f32x2 y3 = *(const LDS_AS f32x2*)(X + (wid ^ 4) * 4096 + 3072);
  const float mx = fmaxf(mK, y3[1]);
  float fK = __builtin_amdgcn_exp2f(mK - mx), fO = __builtin_amdgcn_exp2f(y3[1] - mx);
  const float inv = 1.0f / (fK * (h ? oK[8] : oK[12]) + fO * (h ? y[2][0] : y3[0]));
  fK *= inv; fO *= inv;
  unsigned w[6];
#pragma unroll
  for (int u = 0; u < 3; ++u) {
    w[2 * u] = pk2(fK * oK[4 * u] + fO * y[u][0], fK * oK[4 * u + 1] + fO * y[u][1]);
    w[2 * u + 1] = pk2(fK * oK[4 * u + 2] + fO * y[u][2], fK * oK[4 * u + 3] + fO * y[u][3]);
  }
  const auto s0 = __builtin_amdgcn_permlane32_swap(w[0], w[2], false, false), s1 = __builtin_amdgcn_permlane32_swap(w[1], w[3], false, false);
  const int tok = qt * 256 + qp * 64 + kh * 32 + r;
  char* ob = ws + OFF_OF + (tok >> 4) * 20480 + (tok & 15) * 16;
  const int U = (head >> 1) * 5 + (head & 1) * 2 + h, UT = (head >> 1) * 5 + 4;
  *(u32x4*)(ob + U * 256) = u32x4{s0[0], s1[0], s0[1], s1[1]};
  if (!h) *(u32x2*)(ob + UT * 256 + (head & 1) * 8) = u32x2{w[4], w[5]};
  PROF_END(1);
}

constexpr int S_STAGE = 18 * 1024, S_SLOTS = 8;
constexpr int ring_issued(int s, int nst) { int a = 2 * s + 4 < s + S_SLOTS ? 2 * s + 4 : s + S_SLOTS; return a < nst ? a : nst; }

template <int NST, int CNT>
DI void small_loader(const char* __restrict__ ws, unsigned lds0, int first, int lane, unsigned abase, unsigned bbase PROF_ARG) {
  constexpr int NKS = 2 * NST;
  const char* src[CNT];
#pragma unroll
  for (int j = 0; j < CNT; ++j) {
    const int i = first + j;
    src[j] = ws + (i < 8 ? abase : bbase - 4 * NKS * 1024) + ((i >> 1) * NKS + (i & 1)) * 1024;
  }
  const unsigned vo = lane * 16;
  auto issue = [&](int st) {
    const unsigned base = lds0 + (st & (S_SLOTS - 1)) * S_STAGE + first * 1024;
#pragma unroll
    for (int j = 0; j < CNT; ++j) glds16(vo + st * 2048, src[j], base + j * 1024);
  };
  auto step = [&](auto S) {
    constexpr int s = S.value, n0 = ring_issued(s - 1, NST), n1 = ring_issued(s, NST);
    wait_vm_bar<(n0 - s - 1) * CNT>();
    if (s == 0) STAMP(3);
#pragma unroll
    for (int t = n0; t < n1; ++t) issue(t);
  };
  STAMP(1);
  issue(0); issue(1);
  STAMP(2);
  if constexpr (NST > 16) {
    static_for<0, 6>(step);
#pragma unroll 1
    for (int s = 6; s <= NST - 8; ++s) { wait_vm_bar<6 * CNT>(); issue(s + 7); }
    static_for<NST - 7, NST>(step);
  } else {
    static_for<0, NST>(step);
  }
}

template <int NST, int MODE, int ODD>
DI void small_compute(char* smem, const char* __restrict__ x, char* __restrict__ ws, char* __restrict__ out, int rt, int ct, int wid, int lane PROF_ARG) {
  lds_cptr L = (lds_cptr)smem;
  const int rh = wid >> 1, kp = wid & 1, t16 = lane & 15, g = lane >> 4;
  const int aO = (4 * rh + kp) * 1024 + lane * 16, bO = (8 + kp) * 1024 + lane * 16;
  const int mf = rt * 4 + 2 * rh + kp, tok = mf * 16 + t16;
  const int U0 = (ct >> 1) * 5 + 3 * ODD, UH = (ct >> 1) * 5 + 2;
  char* hp = ws + OFF_HF + (mf * 20 + U0) * 1024;
  char* hh = ws + OFF_HF + (mf * 20 + UH) * 1024 + ODD * 512;
  const unsigned fo = lane * 16, fh = (g >> 1) * 256 + t16 * 16 + (g & 1) * 8;
  const unsigned ro = tok * 1280 + U0 * 64 + g * 16, rhh = tok * 1280 + UH * 64 + ODD * 32 + g * 8;
  u32x4 res0, res1; u32x2 resh;
  if (MODE == 0) { res0 = *(const u32x4*)(x + ro); res1 = *(const u32x4*)(x + ro + 64); resh = *(const u32x2*)(x + rhh); }
  else { res0 = *(const u32x4*)(hp + fo); res1 = *(const u32x4*)(hp + 1024 + fo); resh = *(const u32x2*)(hh + fh); }

  f32x4 acc[2][5];
#pragma unroll
  for (int u = 0; u < 2; ++u)
#pragma unroll
    for (int v = 0; v < 5; ++v) acc[u][v] = f32x4{0.f, 0.f, 0.f, 0.f};
  bf16x8 a0[2], b0[5], a1[2], b1[5];
  auto rd = [&](bf16x8 (&a)[2], bf16x8 (&b)[5], int s) {
    if (GEMM_OFF) return;
    lds_cptr S = L + (s & (S_SLOTS - 1)) * S_STAGE;
#pragma unroll
    for (int u = 0; u < 2; ++u) a[u] = lds_b128(S + aO + u * 2048);
#pragma unroll
    for (int v = 0; v < 5; ++v) b[v] = lds_b128(S + bO + v * 2048);
  };
  auto mm = [&](bf16x8 (&a)[2], bf16x8 (&b)[5]) {
    if (GEMM_OFF) return;
#pragma unroll
    for (int u = 0; u < 2; ++u)
#pragma unroll
      for (int v = 0; v < 5; ++v) acc[u][v] = mfma16(b[v], a[u], acc[u][v]);
  };
  auto ilv = [&] {
#pragma unroll
    for (int i = 0; i < 3; ++i) { SGB(0x100, 2); SGB(0x8, 1); }
    SGB(0x100, 1); SGB(0x8, 7);
    SBAR();
  };
  wait_lgkm_bar(); STAMP(1); rd(a0, b0, 0);
#pragma unroll 1
  for (int s = 1; s + 1 < NST; s += 2) {
    wait_lgkm_bar(); rd(a1, b1, s); mm(a0, b0); ilv();
    wait_lgkm_bar(); rd(a0, b0, s + 1); mm(a1, b1); ilv();
  }
  if (NST % 2 == 0) { wait_lgkm_bar(); rd(a1, b1, NST - 1); mm(a0, b0); ilv(); mm(a1, b1); }
  else mm(a0, b0);
  STAMP(2);
  static_assert(((NST - 1) & (S_SLOTS - 1)) < 4, "scratch reuses ring slots 4 and 5, which are drained by now");
  lds_ptr scr = (lds_ptr)smem + 4 * S_STAGE + lane * 16;
#pragma unroll
  for (int v = 0; v < 5; ++v) *(LDS_AS f32x4*)(scr + wid * 5120 + v * 1024) = kp ? acc[0][v] : acc[1][v];
  wait_lgkm_bar();
  f32x4 y[5];
#pragma unroll
  for (int v = 0; v < 5; ++v) y[v] = (kp ? acc[1][v] : acc[0][v]) + *(const LDS_AS f32x4*)(scr + (wid ^ 1) * 5120 + v * 1024);
  auto fin = [&](f32x4 v, unsigned r0, unsigned r1, unsigned& o0, unsigned& o1) {
    const unsigned y0 = pk2(v[0], v[1]), y1 = pk2(v[2], v[3]);
    o0 = pk2(bflo(r0) + bflo(y0), bfhi(r0) + bfhi(y0)); o1 = pk2(bflo(r1) + bflo(y1), bfhi(r1) + bfhi(y1));
  };
  unsigned o[10];
  fin(y[ODD], res0[0], res0[1], o[0], o[1]); fin(y[ODD + 1], res0[2], res0[3], o[2], o[3]);
  fin(y[ODD + 2], res1[0], res1[1], o[4], o[5]); fin(y[ODD + 3], res1[2], res1[3], o[6], o[7]);
  fin(y[ODD ? 0 : 4], resh[0], resh[1], o[8], o[9]);
  if (MODE == 0) {
    *(u32x4*)(hp + fo) = u32x4{o[0], o[1], o[2], o[3]}; *(u32x4*)(hp + 1024 + fo) = u32x4{o[4], o[5], o[6], o[7]}; *(u32x2*)(hh + fh) = u32x2{o[8], o[9]};
    float ssum = 0.f, ssq = 0.f;
#pragma unroll
    for (int i = 0; i < 10; ++i) { const float e0 = bflo(o[i]), e1 = bfhi(o[i]); ssum += e0 + e1; ssq += e0 * e0 + e1 * e1; }
    ssum = xsum32(xsum16(ssum)); ssq = xsum32(xsum16(ssq));
    if (g == 0) *(f32x2*)(ws + OFF_STAT + tok * 64 + ct * 8) = f32x2{ssum, ssq};
  } else {
    *(u32x4*)(out + ro) = u32x4{o[0], o[1], o[2], o[3]}; *(u32x4*)(out + ro + 64) = u32x4{o[4], o[5], o[6], o[7]}; *(u32x2*)(out + rhh) = u32x2{o[8], o[9]};
  }
}

template <int NST, int MODE>
DI void small_gemm(char* smem, const char* __restrict__ x, char* __restrict__ ws, char* __restrict__ out) {
  constexpr int NKS = 2 * NST;
  const int bid = blockIdx.x, xcd = bid & 7, idx = bid >> 3;
  const int rt = (xcd >> 1) * 8 + (idx & 7), ct = (xcd & 1) * 4 + (idx >> 3);
  PROF_DECL STAMP(0);
  const int tid = threadIdx.x, lane = tid & 63, wid = __builtin_amdgcn_readfirstlane(tid >> 6);
  if (wid >= 4) {
    const unsigned lds0 = (unsigned)(uintptr_t)smem;
    const unsigned abase = (MODE == 0 ? OFF_OF : OFF_ACT) + rt * 4 * NKS * 1024, bbase = (MODE == 0 ? OFF_WOF : OFF_WDF) + ct * 5 * NKS * 1024;
    if (wid < 6) small_loader<NST, 5>(ws, lds0, (wid - 4) * 5, lane, abase, bbase PROF_PASS);
    else small_loader<NST, 4>(ws, lds0, 10 + (wid - 6) * 4, lane, abase, bbase PROF_PASS);
  } else if (ct & 1) small_compute<NST, MODE, 1>(smem, x, ws, out, rt, ct, wid, lane PROF_PASS);
  else small_compute<NST, MODE, 0>(smem, x, ws, out, rt, ct, wid, lane PROF_PASS);
  PROF_END(MODE ? 4 : 2);
}
__global__ __launch_bounds__(512, 2) void k_wo(const char* __restrict__ x, char* __restrict__ ws) {
  __shared__ __attribute__((aligned(16))) char smem[S_SLOTS * S_STAGE];
  small_gemm<10, 0>(smem, x, ws, nullptr);
}
__global__ __launch_bounds__(512, 2) void k_down(char* __restrict__ ws, char* __restrict__ out) {
  __shared__ __attribute__((aligned(16))) char smem[S_SLOTS * S_STAGE];
  small_gemm<27, 1>(smem, nullptr, ws, out);
}

constexpr int G_STAGE = 44 * 1024;
__global__ __launch_bounds__(512, 2) void k_gateup(char* __restrict__ ws) {
  __shared__ __attribute__((aligned(16))) char smem[3 * G_STAGE + 2048];
  const int bid = blockIdx.x, xcd = bid & 7, idx = bid >> 3;
  const int rt = (xcd >> 2) * 8 + (idx & 7), ct = (xcd & 3) * 4 + (idx >> 3);
  const int c8 = ct & 7, UB = (ct >> 3) * 27 + 3 * c8, UL = (ct >> 3) * 27 + 24 + min(c8 >> 1, 2);
  auto slot_nf = [&](int s) { const int wc = s >= 7, v = s - 7 * wc; return v < 4 ? 4 * UB + 8 * wc + v : v < 6 ? 4 * UB + 2 * wc + v : 4 * UL + 2 * (c8 & 1) + wc; };
  PROF_DECL STAMP(0);
  const int tid = threadIdx.x, lane = tid & 63, wid = __builtin_amdgcn_readfirstlane(tid >> 6);
  if (wid >= 4) {
    const int first = (wid - 4) * 11;
    const unsigned lds0 = (unsigned)(uintptr_t)smem + first * 1024, vo = lane * 16;
    const char* src[11];
#pragma unroll
    for (int j = 0; j < 11; ++j) {
      const int i = first + j;
      src[j] = ws + (i < 16 ? OFF_HF + (rt * 8 + (i >> 1)) * 20480 : OFF_WGUF + slot_nf((i - 16) >> 1) * 20480) + (i & 1) * 1024;
    }
    auto issue = [&](int st, int slot) {
#pragma unroll
      for (int j = 0; j < 11; ++j) glds16(vo + st * 2048, src[j], lds0 + slot * G_STAGE + j * 1024);
    };
    STAMP(1); issue(0, 0); issue(1, 1); STAMP(2);
    wait_vm_bar<11>(); STAMP(3);
    issue(2, 2);
    int slot = 0;
#pragma unroll 1
    for (int s = 1; s < 8; ++s) { wait_vm_bar<11>(); issue(s + 2, slot); slot = slot == 2 ? 0 : slot + 1; }
    wait_vm_bar<11>(); wait_vm_bar<0>();
    PROF_END(3);
    return;
  }
  lds_cptr L = (lds_cptr)smem;
  LDS_AS float* sst = (LDS_AS float*)((lds_ptr)smem + 3 * G_STAGE);
  LDS_AS float* scs = sst + 256;
  if (tid < 128) {
    const f32x4* sp = (const f32x4*)(ws + OFF_STAT + (rt * 128 + tid) * 64);
    const f32x4 v = (sp[0] + sp[1]) + (sp[2] + sp[3]);
    const float mean = (v[0] + v[2]) * (1.0f / DM);
    *(LDS_AS f32x2*)(sst + tid * 2) = f32x2{-mean, fmaxf((v[1] + v[3]) * (1.0f / DM) - mean * mean, 0.f) + LN_EPS};
  }
  if (tid < 224) {
    const float* cp = (const float*)(ws + OFF_CSUM) + slot_nf(tid >> 4) * 16 + (tid & 15);
    float c[20];
#pragma unroll
    for (int k = 0; k < 20; ++k) c[k] = cp[k * 3456];
#pragma unroll
    for (int k = 0; k < 10; ++k) c[k] += c[k + 10];
    scs[tid] = ((c[0] + c[1]) + (c[2] + c[3])) + ((c[4] + c[5]) + (c[6] + c[7])) + (c[8] + c[9]);
  }
  const int wr = wid >> 1, wc = wid & 1, t16 = lane & 15, g = lane >> 4;
  const int aO = wr * 8192 + lane * 16, bO = (16 + 14 * wc) * 1024 + lane * 16;
  f32x4 acc[4][7];
  bf16x8 a0[4], b0[7], a1[4], b1[7];
  auto rd = [&](bf16x8 (&a)[4], bf16x8 (&b)[7], int so) {
    if (GEMM_OFF) return;
    a[0] = lds_b128(L + so + aO);
#pragma unroll
    for (int v = 0; v < 7; ++v) b[v] = lds_b128(L + so + bO + v * 2048);
#pragma unroll
    for (int u = 1; u < 4; ++u) a[u] = lds_b128(L + so + aO + u * 2048);
  };
  auto mm = [&](bf16x8 (&a)[4], bf16x8 (&b)[7]) {
    if (GEMM_OFF) return;
#pragma unroll
    for (int u = 0; u < 4; ++u)
#pragma unroll
      for (int v = 0; v < 7; ++v) acc[u][v] = mfma16(b[v], a[u], acc[u][v]);
  };
  auto ilv = [&] {
#pragma unroll
    for (int i = 0; i < 11; ++i) { SGB(0x8, 2); SGB(0x100, 1); }
    SGB(0x8, 6);
    SBAR();
  };
  wait_lgkm_bar();
  STAMP(1);
  rd(a0, b0, 0);
  float vk[4];
  {
    f32x4 c4[7];
#pragma unroll
    for (int v = 0; v < 7; ++v) c4[v] = *(const LDS_AS f32x4*)(scs + (7 * wc + v) * 16 + 4 * g);
#pragma unroll
    for (int u = 0; u < 4; ++u) {
      const f32x2 st = *(const LDS_AS f32x2*)(sst + (64 * wr + 16 * u + t16) * 2);
      vk[u] = st[1];
#pragma unroll
      for (int v = 0; v < 7; ++v) acc[u][v] = c4[v] * st[0];
    }
  }
  SBAR();
  int so = 0;
#pragma unroll 1
  for (int kt = 0; kt < 9; ++kt) {
    rd(a1, b1, so + 1024); mm(a0, b0); ilv();
    so = so == 2 * G_STAGE ? 0 : so + G_STAGE;
    wait_lgkm_bar();
    rd(a0, b0, so); mm(a1, b1); ilv();
  }
  rd(a1, b1, so + 1024); mm(a0, b0); ilv();
  mm(a1, b1);
  SBAR();
  STAMP(2);
  auto swi = [&](f32x4 x0, f32x4 x1, float c1, float k) {
    float a[4];
#pragma unroll
    for (int i = 0; i < 4; ++i) {
      const auto rr = __builtin_amdgcn_permlane32_swap(__float_as_uint(x0[i]), __float_as_uint(x1[i]), false, false);
      const float gt = __uint_as_float(rr[0]), up = __uint_as_float(rr[1]);
      a[i] = gt * up * __builtin_amdgcn_rcpf(__builtin_amdgcn_exp2f(gt * c1) * k + k);
    }
    return u32x2{pk2(a[0], a[1]), pk2(a[2], a[3])};
  };
  float c1[4];
#pragma unroll
  for (int u = 0; u < 4; ++u) c1[u] = -LOG2E * __builtin_amdgcn_rsqf(vk[u]);
  char* ab = ws + OFF_ACT + ((rt * 8 + 4 * wr) * 54 + UB) * 1024;
#pragma unroll
  for (int u = 0; u < 4; ++u) {
    const u32x2 pa = swi(acc[u][0], acc[u][1], c1[u], vk[u]), pb = swi(acc[u][2], acc[u][3], c1[u], vk[u]);
    *(u32x4*)(ab + (u * 54 + 2 * wc) * 1024 + lane * 16u) = u32x4{pa[0], pa[1], pb[0], pb[1]};
    *(u32x2*)(ab + (u * 54 + 1) * 1024 + wc * 8 + lane * 16u) = swi(acc[u][4], acc[u][5], c1[u], vk[u]);
  }
  if (c8 < 6) {
    char* al = ws + OFF_ACT + ((rt * 8 + 4 * wr) * 54 + UL) * 1024 + wc * 512 + (c8 & 1) * 8;
    const unsigned lo = (g >> 1) * 54 * 1024 + (g & 1) * 256 + t16 * 16;
#pragma unroll
    for (int u = 0; u < 4; u += 2)
      *(u32x2*)(al + u * 54 * 1024 + lo) = swi(acc[u][6], acc[u + 1][6], lane < 32 ? c1[u] : c1[u + 1], lane < 32 ? vk[u] : vk[u + 1]);
  }
  PROF_END(3);
}

extern "C" void kernel_launch(void* const* d_in, const int* in_sizes, int n_in, void* d_out, int out_size, void* d_ws, size_t ws_size,
                              hipStream_t stream) {
  const char* x = (const char*)d_in[0]; const char* wqkv = (const char*)d_in[1]; const char* wo = (const char*)d_in[2];
  const char* wg = (const char*)d_in[4]; const char* wu = (const char*)d_in[5]; const char* wd = (const char*)d_in[6];
  char* ws = (char*)d_ws;
  k_qkv<<<256, 512, 0, stream>>>(x, wqkv, ws);
  k_attn<<<256, 768, 0, stream>>>(wo, wg, wu, wd, ws);
  k_wo<<<256, 512, 0, stream>>>(x, ws);
  k_gateup<<<256, 512, 0, stream>>>(ws);
  k_down<<<256, 512, 0, stream>>>(ws, (char*)d_out);
}
```

```cpp
#include <hip/hip_runtime.h>
#include <hip/hip_bf16.h>
#include <stdint.h>

typedef __attribute__((ext_vector_type(8))) short bf16x8;
typedef __attribute__((ext_vector_type(4))) short bf16x4;
typedef __attribute__((ext_vector_type(2))) float f32x2;
typedef __attribute__((ext_vector_type(4))) float f32x4;
typedef __attribute__((ext_vector_type(16))) float f32x16;
typedef __attribute__((ext_vector_type(4))) unsigned u32x4;
typedef __attribute__((ext_vector_type(2))) unsigned u32x2;
typedef __attribute__((ext_vector_type(2))) __bf16 bf2;
typedef __attribute__((address_space(3))) const char* lds_cptr;
typedef __attribute__((address_space(3))) char* lds_ptr;
#define LDS_AS __attribute__((address_space(3)))
#define DI __device__ __forceinline__

constexpr int SEQ = 2048, DM = 640, NH = 32, DH = 20, FF = 1728;
constexpr float LN_EPS = 1e-5f;
constexpr float LOG2E = 1.4426950408889634f;
constexpr float QSCALE = 0.22360679774997896f * LOG2E;

constexpr int Q_TILE = 1280;
constexpr int Q_HEAD = 64 * Q_TILE;
constexpr int KV_TILE = 2688;
constexpr int KV_HEAD = 64 * KV_TILE;
constexpr unsigned OFF_Q    = 0;
constexpr unsigned OFF_KV   = OFF_Q + NH * Q_HEAD;
constexpr unsigned OFF_OF   = OFF_KV + NH * KV_HEAD;
constexpr unsigned OFF_HF   = OFF_OF + SEQ * DM * 2;
constexpr unsigned OFF_ACT  = OFF_HF + SEQ * DM * 2;
constexpr unsigned OFF_WOF  = OFF_ACT + SEQ * FF * 2;
constexpr unsigned OFF_WGUF = OFF_WOF + DM * DM * 2;
constexpr unsigned OFF_WDF  = OFF_WGUF + DM * FF * 4;
constexpr unsigned OFF_STAT = OFF_WDF + FF * DM * 2;
constexpr unsigned OFF_CSUM = OFF_STAT + SEQ * 64;

#define PROF_DECL
#define STAMP(i)
#define PROF_END(k)
#define PROF_ARG
#define PROF_PASS
DI void glds16(unsigned voff, const void* sbase, unsigned lds) {
  unsigned keep;
  asm volatile("s_mov_b32 %0, m0\n\ts_mov_b32 m0, %3\n\ts_nop 0\n\tglobal_load_lds_dwordx4 %1, %2\n\ts_mov_b32 m0, %0"
               : "=&s"(keep) : "v"(voff), "s"(sbase), "s"(lds) : "memory");
}
#define CODE_TOUCH(first, last, part, sink) do { unsigned long long pc_; int a_, b_; \
  asm volatile("s_getpc_b64 %0\n.Lct%=:\n\ts_mov_b32 %1, " #first "-.Lct%=\n\ts_mov_b32 %2, " #last "-.Lct%=" : "=s"(pc_), "=s"(a_), "=s"(b_)); \
  const int o_ = min(max(a_, 0) + (int)((threadIdx.x & 63) * 128 + (part) * 8192), max(b_, 0)); \
  asm volatile("global_load_dword %0, %1, %2" : "=v"(sink) : "v"(o_), "s"(pc_) : "memory"); } while (0)
#define CODE_DONE(sink) asm volatile("s_waitcnt vmcnt(0)" :: "v"(sink) : "memory")
template <int N> DI void wait_vm_bar() { asm volatile("s_waitcnt vmcnt(%0)\n\ts_barrier" :: "n"(N) : "memory"); }
template <int N> DI void wait_vm() { asm volatile("s_waitcnt vmcnt(%0)" :: "n"(N) : "memory"); }
#define SBAR() __builtin_amdgcn_sched_barrier(0)
#define SGB(mask, n) __builtin_amdgcn_sched_group_barrier(mask, n, 0)
DI void wait_lgkm_bar() { SBAR(); __builtin_amdgcn_s_waitcnt(0xC07F); asm volatile("s_barrier" ::: "memory"); SBAR(); }
#define GEMM_OFF 0
template <int I> struct IC { static constexpr int value = I; };
template <int B, int E, class F> DI void static_for(F&& f) { if constexpr (B < E) { f(IC<B>{}); static_for<B + 1, E>(f); } }
DI bf16x8 lds_b128(lds_cptr p) { return *(const LDS_AS bf16x8*)p; }
DI bf16x4 lds_b64(lds_cptr p) { return *(const LDS_AS bf16x4*)p; }
DI bf16x4 lds_tr(lds_cptr p) { return __builtin_amdgcn_ds_read_tr16_b64_v4i16((LDS_AS bf16x4*)p); }
DI f32x4 mfma16(bf16x8 a, bf16x8 b, f32x4 c) { return __builtin_amdgcn_mfma_f32_16x16x32_bf16(a, b, c, 0, 0, 0); }
DI f32x16 mfma32(bf16x8 a, bf16x8 b, f32x16 c) { return __builtin_amdgcn_mfma_f32_32x32x16_bf16(a, b, c, 0, 0, 0); }
DI f32x16 mfma32k8(bf16x4 a, bf16x4 b, f32x16 c) { return __builtin_amdgcn_mfma_f32_32x32x8bf16_1k(a, b, c, 0, 0, 0); }
DI unsigned pk2(float lo, float hi) { f32x2 v = {lo, hi}; return __builtin_bit_cast(unsigned, __builtin_convertvector(v, bf2)); }
DI float bflo(unsigned u) { return __uint_as_float(u << 16); }
DI float bfhi(unsigned u) { return __uint_as_float(u & 0xffff0000u); }
DI int div20(int c) { return (c * 3277) >> 16; }
DI float xsum32(float v) { auto r = __builtin_amdgcn_permlane32_swap(__float_as_uint(v), __float_as_uint(v), false, false); return __uint_as_float(r[0]) + __uint_as_float(r[1]); }
DI float xsum16(float v) { auto r = __builtin_amdgcn_permlane16_swap(__float_as_uint(v), __float_as_uint(v), false, false); return __uint_as_float(r[0]) + __uint_as_float(r[1]); }
constexpr bf16x8 ONES8 = {0x3f80, 0x3f80, 0x3f80, 0x3f80, 0x3f80, 0x3f80, 0x3f80, 0x3f80};

template <int MODE>
DI void qkv_compute(lds_cptr L, lds_ptr scr, int wid, int lane, int row0, int col0, char* __restrict__ ws PROF_ARG) {
  const int wr = wid >> 1, wc = wid & 1, r16 = lane & 15, g = lane >> 4, q = r16 >> 2, p = lane & 3;
  const int f = q | ((g & 1) << 2);
  const int rp = MODE == 2 ? 16 * (r16 >> 3) + (r16 & 7) : r16;
  constexpr int RSTEP = MODE == 2 ? 8 : 16;
  int aoff[2][2], boff[4];
#pragma unroll
  for (int e = 0; e < 2; ++e)
#pragma unroll
    for (int ks = 0; ks < 2; ++ks) { const int r = rp + e * RSTEP; aoff[e][ks] = (64 * wr + r) * 128 + (((4 * ks + g) ^ ((r >> 1) & 7) ^ ((r >> 2) & 4)) << 4); }
#pragma unroll
  for (int jj = 0; jj < 4; ++jj) { const int j = (jj + 2 * wr) & 3; boff[jj] = 16384 + (8 * g + q) * 256 + ((((4 * wc + j) ^ f) * 2 + (p >> 1)) << 4) + (p & 1) * 8; }
  const int pr0 = wc * 4096, pr1 = 4096 - pr0;

  f32x4 acc[4][4], rs[2], gr[2], cs[2];
#pragma unroll
  for (int i = 0; i < 4; ++i)
#pragma unroll
    for (int j = 0; j < 4; ++j) acc[i][j] = f32x4{0.f, 0.f, 0.f, 0.f};
#pragma unroll
  for (int i = 0; i < 2; ++i) { rs[i] = f32x4{0.f, 0.f, 0.f, 0.f}; gr[i] = rs[i]; cs[i] = rs[i]; }
  bf16x8 a0[4], b0[4], a1[4], b1[4];

  auto rd = [&](bf16x8 (&a)[4], bf16x8 (&b)[4], int slot, int ks) {
    if (GEMM_OFF && slot + ks > 0) return;
    lds_cptr S = L + slot;
#pragma unroll
    for (int jj = 0; jj < 4; ++jj) {
      bf16x4 lo = lds_tr(S + boff[jj] + ks * 8192), hi = lds_tr(S + boff[jj] + ks * 8192 + 1024);
      b[jj] = __builtin_shufflevector(lo, hi, 0, 1, 2, 3, 4, 5, 6, 7);
      a[jj] = lds_b128(S + aoff[jj & 1][ks] + (jj < 2 ? pr0 : pr1));
    }
  };
  auto mm = [&](bf16x8 (&a)[4], bf16x8 (&b)[4]) {
    if (GEMM_OFF) return;
#pragma unroll
    for (int ii = 0; ii < 4; ++ii)
#pragma unroll
      for (int jj = 0; jj < 4; ++jj)
        acc[ii][jj] = (MODE == 2) ? mfma16(a[ii], b[jj], acc[ii][jj]) : mfma16(b[jj], a[ii], acc[ii][jj]);
#pragma unroll
    for (int u = 0; u < 2; ++u) {
      rs[u] = mfma16(a[u], ONES8, rs[u]);
      gr[u] = mfma16(a[u], a[u], gr[u]);
      cs[u] = mfma16(ONES8, b[u], cs[u]);
    }
  };
  auto ilv = [&] {
#pragma unroll
    for (int i = 0; i < 12; ++i) { SGB(0x8, 1); SGB(0x100, 1); }
    SGB(0x8, 10);
    SBAR();
  };

  wait_lgkm_bar();
  STAMP(1);
  rd(a0, b0, 0, 0);
  SBAR();
#pragma unroll 1
  for (int kt = 0; kt < 9; ++kt) {
    rd(a1, b1, (kt & 3) * 32768, 1); mm(a0, b0); ilv();
    wait_lgkm_bar();
    rd(a0, b0, ((kt + 1) & 3) * 32768, 0); mm(a1, b1); ilv();
  }
  rd(a1, b1, 32768, 1); mm(a0, b0); ilv();
  mm(a1, b1);
  SBAR();
  STAMP(2);
  LDS_AS float* rsum = (LDS_AS float*)scr; LDS_AS float* rsq = rsum + 128; LDS_AS float* csum = rsum + 256;
#pragma unroll
  for (int u = 0; u < 2; ++u) {
    const int rb = 64 * wr + 32 * wc + 16 * u;
    if (r16 == 0) *(LDS_AS f32x4*)(rsum + rb + 4 * g) = rs[u];
    if (q == g) { const float d = p == 0 ? gr[u][0] : p == 1 ? gr[u][1] : p == 2 ? gr[u][2] : gr[u][3]; rsq[rb + r16] = d; }
    if (g == 0) csum[64 * wc + 32 * wr + 16 * u + r16] = cs[u][0];
  }
  constexpr float invD = 1.0f / DM;
  if (MODE != 2) {
    constexpr int TS = MODE == 0 ? Q_TILE : KV_TILE, HS = MODE == 0 ? Q_HEAD : KV_HEAD;
    unsigned off[4][2];
#pragma unroll
    for (int jj = 0; jj < 4; ++jj) {
      const int ck = col0 - MODE * DM + 64 * wc + 16 * ((jj + 2 * wr) & 3) + 4 * g, head = div20(ck), d = ck - head * DH;
      off[jj][0] = head * HS + (d < 16 ? (d >> 3) * 512 + (d & 7) * 2 + r16 * 16 : 1024 + r16 * 8);
      off[jj][1] = off[jj][0] + (d < 16 ? 256 : 128);
    }
    char* base = ws + (MODE == 0 ? OFF_Q : OFF_KV) + ((row0 >> 5) + 2 * wr) * TS;
    wait_lgkm_bar();
    f32x4 c4[4];
#pragma unroll
    for (int jj = 0; jj < 4; ++jj) c4[jj] = *(const LDS_AS f32x4*)(csum + 64 * wc + 16 * ((jj + 2 * wr) & 3) + 4 * g);
#pragma unroll
    for (int ii = 0; ii < 4; ++ii) {
      const int half = ((ii >> 1) + wc) & 1, rl = 64 * wr + 32 * half + 16 * (ii & 1) + r16;
      const float mean = rsum[rl] * invD, var = fmaxf(rsq[rl] * invD - mean * mean, 0.f);
      const float rstd = __builtin_amdgcn_rsqf(var + LN_EPS) * (MODE == 0 ? QSCALE : 1.0f), nb = -mean * rstd;
      char* tb = base + half * TS;
#pragma unroll
      for (int jj = 0; jj < 4; ++jj) {
        const f32x4 v = acc[ii][jj] * rstd + c4[jj] * nb;
        *(u32x2*)(tb + off[jj][ii & 1]) = u32x2{pk2(v[0], v[1]), pk2(v[2], v[3])};
      }
    }
  } else {
    unsigned off[4];
#pragma unroll
    for (int jj = 0; jj < 4; ++jj) {
      const int cv = col0 - 2 * DM + 64 * wc + 16 * ((jj + 2 * wr) & 3) + r16, head = div20(cv), d = cv - head * DH;
      off[jj] = head * KV_HEAD + 1280 + g * 352 + d * 16;
    }
    char* base = ws + OFF_KV + ((row0 >> 5) + 2 * wr) * KV_TILE;
    { const int head = div20(col0 - 2 * DM + 19) + (lane >> 3), t = lane & 7;
      if (wc == 0 && head * DH < col0 - 2 * DM + 128) *(bf16x8*)(base + head * KV_HEAD + (t >> 2) * KV_TILE + 1280 + (t & 3) * 352 + 320) = ONES8; }
    wait_lgkm_bar();
    float c1[4];
#pragma unroll
    for (int jj = 0; jj < 4; ++jj) c1[jj] = csum[64 * wc + 16 * ((jj + 2 * wr) & 3) + r16];
#pragma unroll
    for (int ip = 0; ip < 2; ++ip) {
      const int half = (ip + wc) & 1;
      f32x4 rstd[2], nb[2];
#pragma unroll
      for (int e = 0; e < 2; ++e) {
        const int rl = 64 * wr + 32 * half + 16 * e + 4 * g;
        const f32x4 mean = *(const LDS_AS f32x4*)(rsum + rl) * invD, q4 = *(const LDS_AS f32x4*)(rsq + rl);
#pragma unroll
        for (int i = 0; i < 4; ++i) rstd[e][i] = __builtin_amdgcn_rsqf(fmaxf(q4[i] * invD - mean[i] * mean[i], 0.f) + LN_EPS);
        nb[e] = -mean * rstd[e];
      }
      char* tb = base + half * KV_TILE;
#pragma unroll
      for (int jj = 0; jj < 4; ++jj) {
        const f32x4 v0 = acc[2 * ip][jj] * rstd[0] + nb[0] * c1[jj], v1 = acc[2 * ip + 1][jj] * rstd[1] + nb[1] * c1[jj];
        *(u32x4*)(tb + off[jj]) = u32x4{pk2(v0[0], v0[1]), pk2(v0[2], v0[3]), pk2(v1[0], v1[1]), pk2(v1[2], v1[3])};
      }
    }
  }
}

extern "C" __global__ __launch_bounds__(512, 2) void k_qkv(const char* __restrict__ x, const char* __restrict__ w, char* __restrict__ ws) {
  __shared__ __attribute__((aligned(16))) char smem[4 * 32768 + 2048];
  const int bid = blockIdx.x, xcd = bid & 7, idx = bid >> 3;
  const int rt = (xcd >> 1) * 4 + (idx & 3), ct = (xcd & 1) * 8 + (idx >> 2);
  if (ct >= 15) return;
  PROF_DECL STAMP(0);
  const int tid = threadIdx.x, lane = tid & 63, wid = __builtin_amdgcn_readfirstlane(tid >> 6);
  if (wid >= 4) {
    const int lw = wid - 4;
    const unsigned lds0 = (unsigned)(uintptr_t)smem + lw * 1024;
    const int ra = 8 * lw + (lane >> 3), rb = 4 * lw + (lane >> 4), f = (rb & 3) | ((rb >> 1) & 4);
    const unsigned voA = ra * 1280 + (((lane & 7) ^ ((ra >> 1) & 7) ^ ((ra >> 2) & 4)) << 4), voB = rb * 3840 + (((lane & 15) ^ (f << 1)) << 4);
    const char* pa = x + (size_t)rt * 128 * 1280; const char* pb = w + ct * 256;
    auto issue = [&](int st) {
      const unsigned base = lds0 + (st & 3) * 32768;
#pragma unroll
      for (int j = 0; j < 4; ++j) { glds16(voA, pa + st * 128 + j * 32 * 1280, base + j * 4096); glds16(voB, pb + (st * 64 + j * 16) * 3840, base + 16384 + j * 4096); }
    };
    STAMP(1); issue(0); issue(1); STAMP(2);
    wait_vm_bar<8>(); STAMP(3);
    issue(2); issue(3);
#pragma unroll 1
    for (int s = 1; s < 7; ++s) { wait_vm_bar<16>(); issue(s + 3); }
    wait_vm_bar<16>(); wait_vm_bar<8>(); wait_vm_bar<0>();
    PROF_END(0);
    return;
  }
  lds_cptr L = (lds_cptr)smem; lds_ptr scr = (lds_ptr)smem + 4 * 32768;
  unsigned cs_; CODE_TOUCH(k_qkv, k_wo, wid, cs_);
  if (ct < 5) qkv_compute<0>(L, scr, wid, lane, rt * 128, ct * 128, ws PROF_PASS);
  else if (ct < 10) qkv_compute<1>(L, scr, wid, lane, rt * 128, ct * 128, ws PROF_PASS);
  else qkv_compute<2>(L, scr, wid, lane, rt * 128, ct * 128, ws PROF_PASS);
  CODE_DONE(cs_);
  PROF_END(0);
}

constexpr int SC_BYTES = 8 * KV_TILE;
constexpr int STG_OFF = 6 * SC_BYTES;

DI float rowmax16(const f32x16& s) {
  float a = fmaxf(fmaxf(s[0], s[1]), s[2]), b = fmaxf(fmaxf(s[3], s[4]), s[5]);
  a = fmaxf(fmaxf(a, s[6]), s[7]); b = fmaxf(fmaxf(b, s[8]), s[9]);
  a = fmaxf(fmaxf(a, s[10]), s[11]); b = fmaxf(fmaxf(b, s[12]), s[13]);
  a = fmaxf(fmaxf(a, s[14]), s[15]);
  const float m = fmaxf(a, b);
  auto rr = __builtin_amdgcn_permlane32_swap(__float_as_uint(m), __float_as_uint(m), false, false);
  return fmaxf(__uint_as_float(rr[0]), __uint_as_float(rr[1]));
}
DI unsigned exp_pk(float a, float b) { return pk2(__builtin_amdgcn_exp2f(a), __builtin_amdgcn_exp2f(b)); }

template <int KIND>
DI void retile_issue(const char* __restrict__ w0, const char* __restrict__ w1, int tile, int ks, int lane, unsigned stg) {
  if (KIND == 0) {
    const unsigned vo = (ks * 32 + (lane >> 3)) * (FF * 2) + tile * 128 + (lane & 7) * 16;
#pragma unroll
    for (int j = 0; j < 4; ++j) { glds16(vo, w0 + j * 8 * (FF * 2), stg + j * 1024); glds16(vo, w1 + j * 8 * (FF * 2), stg + 4096 + j * 1024); }
  } else {
#pragma unroll
    for (int j = 0; j < 8; ++j) {
      int k = ks * 32 + 4 * j + (lane >> 4);
      if (KIND == 2) { const int pp = div20(k) >> 1, e = k - 40 * pp; k = 40 * pp + (e < 16 ? e : e < 32 ? e + 4 : e < 36 ? e - 16 : e); }
      glds16(k * (DM * 2) + tile * 256 + (lane & 15) * 16, w0, stg + j * 1024);
    }
  }
}
template <int KIND>
DI void retile_store(char* __restrict__ ws, lds_cptr S, int tile, int ks, int lane) {
  const int g = lane >> 4, q = (lane & 15) >> 2, p = lane & 3;
#pragma unroll
  for (int f = 0; f < 8; ++f) {
    lds_cptr a; int hi; unsigned dst;
    if (KIND == 0) {
      a = S + (p >> 1) * 4096 + (8 * g + q) * 128 + (p & 1) * 16 + (32 * (f >> 2) + 16 * (f & 1) + 4 * ((f >> 1) & 1)) * 2; hi = 512;
      dst = OFF_WGUF + ((8 * tile + f) * 20 + ks) * 1024;
    } else {
      const int U = 4 * tile + (f >> 1), j = f & 1;
      a = S + (8 * g + q) * 256 + (32 * (f >> 1) + (U % 5 == 2 ? 16 * j + 4 * p : 8 * p + 4 * j)) * 2; hi = 1024;
      dst = (KIND == 1 ? OFF_WDF : OFF_WOF) + ((2 * U + j) * (KIND == 1 ? 54 : 20) + ks) * 1024;
    }
    const bf16x8 fr = __builtin_shufflevector(lds_tr(a), lds_tr(a + hi), 0, 1, 2, 3, 4, 5, 6, 7);
    *(bf16x8*)(ws + dst + lane * 16) = fr;
    if (KIND == 0) {
      const f32x4 c = mfma16(fr, ONES8, f32x4{0.f, 0.f, 0.f, 0.f});
      if ((lane & 15) == 0) *(f32x4*)(ws + OFF_CSUM + ((ks * 216 + 8 * tile + f) * 16 + 4 * g) * 4) = c;
    }
  }
}

extern "C" __global__ __launch_bounds__(768, 3) void k_attn(const char* __restrict__ wo, const char* __restrict__ wg, const char* __restrict__ wu,
                                                 const char* __restrict__ wd, char* __restrict__ ws) {
  __shared__ __attribute__((aligned(16))) char smem[6 * SC_BYTES + 4 * 8192];
  const int bid = blockIdx.x, xcd = bid & 7, idx = bid >> 3, head = xcd * 4 + (idx >> 3), qt = idx & 7;
  const int tid = threadIdx.x, lane = tid & 63, wid = __builtin_amdgcn_readfirstlane(tid >> 6);
  PROF_DECL STAMP(0);
  lds_cptr L = (lds_cptr)smem;
  if (wid >= 8) {
    const int hw = wid - 8, kh = hw >> 1, sub = hw & 1;
    const unsigned lds0 = (unsigned)(uintptr_t)smem, vo = lane * 16;
    const char* src = ws + OFF_KV + head * KV_HEAD + kh * 4 * SC_BYTES + sub * 1024;
    const unsigned dst = lds0 + kh * SC_BYTES + sub * 1024;
    auto piece = [&](int sc, int slot, int i) { const int o = (i == 10 ? (sub ? 9 : 10) : i) * 2048; glds16(vo, src + sc * SC_BYTES + o, dst + slot * 2 * SC_BYTES + o); };
#pragma unroll
    for (int i = 0; i < 4; ++i) piece(0, 0, i);
    STAMP(1);
    wait_vm_bar<0>();
    STAMP(2);
#pragma unroll
    for (int i = 4; i < 11; ++i) piece(0, 0, i);
    wait_vm_bar<0>();
#pragma unroll
    for (int i = 0; i < 11; ++i) piece(1, 1, i);
#pragma unroll
    for (int i = 0; i < 11; ++i) piece(2, 2, i);
    const int job = bid * 4 + hw;
    const unsigned stg = lds0 + STG_OFF + hw * 8192;
    int tile = 0, ks = 0;
    if (job < 540)      { ks = job / 27; tile = job - ks * 27; retile_issue<0>(wg, wu, tile, ks, lane, stg); }
    else if (job < 810) { ks = (job - 540) / 5; tile = job - 540 - ks * 5; retile_issue<1>(wd, wd, tile, ks, lane, stg); }
    else                { ks = min(job - 810, 99) / 5; tile = min(job - 810, 99) - ks * 5; retile_issue<2>(wo, wo, tile, ks, lane, stg); }
    wait_vm_bar<8>();
    STAMP(3);
#pragma unroll
    for (int i = 0; i < 11; ++i) piece(3, 0, i);
    wait_vm<11>();
    lds_cptr S = L + STG_OFF + hw * 8192;
    if (job < 540) retile_store<0>(ws, S, tile, ks, lane);
    else if (job < 810) retile_store<1>(ws, S, tile, ks, lane);
    else if (job < 910) retile_store<2>(ws, S, tile, ks, lane);
    STAMP(4);
    unsigned cs_; CODE_TOUCH(k_wo, k_down, hw, cs_);
    wait_vm_bar<0>();
    CODE_DONE(cs_);
    PROF_END(1);
    return;
  }
  const int r = lane & 31, h = lane >> 5, qp = wid & 3, kh = wid >> 2;
  const char* qb = ws + OFF_Q + head * Q_HEAD + (qt * 8 + qp * 2) * Q_TILE;
  const bf16x8 qaA = *(const bf16x8*)(qb + lane * 16), qaB = *(const bf16x8*)(qb + Q_TILE + lane * 16);
  u32x2 qtA = *(const u32x2*)(qb + 1024 + r * 8), qtB = *(const u32x2*)(qb + Q_TILE + 1024 + r * 8);
  if (h) { qtA = u32x2{0u, 0u}; qtB = qtA; }

  const int rm = r <= 20 ? r : (r < 28 ? 20 : r - 16);
  const int kaO = lane * 16, kbO = h ? 1280 + 320 : 1024 + r * 8, vO = 1280 + h * 352 + rm * 16;
  f32x16 zero, oA, oB, sA, sB;
#pragma unroll
  for (int i = 0; i < 16; ++i) { zero[i] = 0.f; oA[i] = 0.f; oB[i] = 0.f; }
  bf16x8 ka[2], v0[2], v1[2]; bf16x4 kb[2];
  lds_cptr pk = L + kh * SC_BYTES + kaO, pb = L + kh * SC_BYTES + kbO, pv = L + kh * SC_BYTES + vO;
  wait_lgkm_bar();
  STAMP(1);
  kb[0] = lds_b64(pb); ka[0] = lds_b128(pk); v0[0] = lds_b128(pv); v1[0] = lds_b128(pv + 704);
  v1[1] = v1[0];
  float mA, mB;
  {
    sA = mfma32(ka[0], qaA, mfma32k8(kb[0], __builtin_bit_cast(bf16x4, qtA), zero));
    sB = mfma32(ka[0], qaB, mfma32k8(kb[0], __builtin_bit_cast(bf16x4, qtB), zero));
    const unsigned nA = pk2(-rowmax16(sA), 0.f), nB = pk2(-rowmax16(sB), 0.f);
    mA = -bflo(nA); mB = -bflo(nB);
    if (h) { qtA[0] = nA; qtB[0] = nB; }
  }
  const bf16x4 qbA = __builtin_bit_cast(bf16x4, qtA), qbB = __builtin_bit_cast(bf16x4, qtB);
  sA = mfma32(ka[0], qaA, mfma32k8(kb[0], qbA, zero));
  u32x4 pl = {0u, 0u, 0u, 0u};

  auto phase = [&](const f32x16& sC, f32x16& oC, f32x16& oP, bf16x8 vP, f32x16& sN, bf16x8 qa, bf16x4 qb4, bf16x8 kA, bf16x4 kB, bf16x8 vC, auto&& reads) {
    u32x4 p0;
    oP = mfma32(vP, __builtin_bit_cast(bf16x8, pl), oP); reads(); SBAR();
    p0[0] = exp_pk(sC[0], sC[1]); p0[1] = exp_pk(sC[2], sC[3]); SBAR();
    sN = mfma32k8(kB, qb4, zero); SBAR();
    p0[2] = exp_pk(sC[4], sC[5]); p0[3] = exp_pk(sC[6], sC[7]); SBAR();
    sN = mfma32(kA, qa, sN); SBAR();
    pl[0] = exp_pk(sC[8], sC[9]); pl[1] = exp_pk(sC[10], sC[11]); SBAR();
    oC = mfma32(vC, __builtin_bit_cast(bf16x8, p0), oC); SBAR();
    pl[2] = exp_pk(sC[12], sC[13]); pl[3] = exp_pk(sC[14], sC[15]); SBAR();
  };
#pragma unroll 1
  for (int sc = 0; sc < 4; ++sc) {
    const int dn = sc == 2 ? -4 * SC_BYTES : sc == 3 ? 0 : 2 * SC_BYTES;
#pragma unroll
    for (int t = 0; t < 8; ++t) {
      const int c = t & 1, n = c ^ 1, no = t < 7 ? (t + 1) * KV_TILE : dn;
      if (t == 2 && sc == 0) wait_lgkm_bar();
      if (t == 7 && !(sc & 1)) wait_lgkm_bar();
      phase(sA, oA, oB, v1[n], sB, qaB, qbB, ka[c], kb[c], v0[c], [&] { kb[n] = lds_b64(pb + no); ka[n] = lds_b128(pk + no); });
      phase(sB, oB, oA, v1[c], sA, qaA, qbA, ka[n], kb[n], v0[c], [&] { v0[n] = lds_b128(pv + no); v1[n] = lds_b128(pv + no + 704); });
    }
    pk += dn; pb += dn; pv += dn;
  }
  oB = mfma32(v1[1], __builtin_bit_cast(bf16x8, pl), oB);
  STAMP(2);

  {
    const float lA = h ? oA[8] : oA[12], lB = h ? oB[8] : oB[12];
    bool ok = lA < 1.0e18f && lA > 1.0e-18f && lB < 1.0e18f && lB > 1.0e-18f;
#pragma unroll
    for (int i = 0; i < 12; ++i) ok = ok && fabsf(oA[i]) < 3.0e38f && fabsf(oB[i]) < 3.0e38f;
    if (!__all(ok)) {
      const char* kvb = ws + OFF_KV + head * KV_HEAD + kh * 4 * SC_BYTES;
      const bf16x4 z4 = {0, 0, 0, 0}, tA = h ? z4 : qbA, tB = h ? z4 : qbB;
      mA = -INFINITY; mB = -INFINITY; oA = zero; oB = zero;
      auto upd = [&](f32x16 s, float& m, f32x16& o, bf16x8 w0, bf16x8 w1) {
        const float mn = fmaxf(m, rowmax16(s)), alpha = __builtin_amdgcn_exp2f(m - mn);
        m = mn; o *= alpha;
        u32x4 a, b;
#pragma unroll
        for (int i = 0; i < 4; ++i) { a[i] = exp_pk(s[2 * i] - mn, s[2 * i + 1] - mn); b[i] = exp_pk(s[8 + 2 * i] - mn, s[9 + 2 * i] - mn); }
        o = mfma32(w1, __builtin_bit_cast(bf16x8, b), mfma32(w0, __builtin_bit_cast(bf16x8, a), o));
      };
#pragma unroll 1
      for (int t = 0; t < 32; ++t) {
        const char* tb = kvb + t * KV_TILE;
        const bf16x8 k8 = *(const bf16x8*)(tb + kaO), w0 = *(const bf16x8*)(tb + vO), w1 = *(const bf16x8*)(tb + vO + 704);
        const bf16x4 k4 = *(const bf16x4*)(tb + kbO);
        upd(mfma32(k8, qaA, mfma32k8(k4, tA, zero)), mA, oA, w0, w1);
        upd(mfma32(k8, qaB, mfma32k8(k4, tB, zero)), mB, oB, w0, w1);
      }
    }
  }
  const f32x16 oS = kh ? oA : oB, oK = kh ? oB : oA;
  const float mS = kh ? mA : mB, mK = kh ? mB : mA;
  lds_ptr X = (lds_ptr)smem + 2 * SC_BYTES + lane * 16;
#pragma unroll
  for (int u = 0; u < 3; ++u) *(LDS_AS f32x4*)(X + wid * 4096 + u * 1024) = f32x4{oS[4 * u], oS[4 * u + 1], oS[4 * u + 2], oS[4 * u + 3]};
  *(LDS_AS f32x2*)(X + wid * 4096 + 3072) = f32x2{oS[12], mS};
  wait_lgkm_bar();
  f32x4 y[3];
#pragma unroll
  for (int u = 0; u < 3; ++u) y[u] = *(const LDS_AS f32x4*)(X + (wid ^ 4) * 4096 + u * 1024);
  const f32x2 y3 = *(const LDS_AS f32x2*)(X + (wid ^ 4) * 4096 + 3072);
  const float mx = fmaxf(mK, y3[1]);
  float fK = __builtin_amdgcn_exp2f(mK - mx), fO = __builtin_amdgcn_exp2f(y3[1] - mx);
  const float inv = 1.0f / (fK * (h ? oK[8] : oK[12]) + fO * (h ? y[2][0] : y3[0]));
  fK *= inv; fO *= inv;
  unsigned w[6];
#pragma unroll
  for (int u = 0; u < 3; ++u) {
    w[2 * u] = pk2(fK * oK[4 * u] + fO * y[u][0], fK * oK[4 * u + 1] + fO * y[u][1]);
    w[2 * u + 1] = pk2(fK * oK[4 * u + 2] + fO * y[u][2], fK * oK[4 * u + 3] + fO * y[u][3]);
  }
  const auto s0 = __builtin_amdgcn_permlane32_swap(w[0], w[2], false, false), s1 = __builtin_amdgcn_permlane32_swap(w[1], w[3], false, false);
  const int tok = qt * 256 + qp * 64 + kh * 32 + r;
  char* ob = ws + OFF_OF + (tok >> 4) * 20480 + (tok & 15) * 16;
  const int U = (head >> 1) * 5 + (head & 1) * 2 + h, UT = (head >> 1) * 5 + 4;
  *(u32x4*)(ob + U * 256) = u32x4{s0[0], s1[0], s0[1], s1[1]};
  if (!h) *(u32x2*)(ob + UT * 256 + (head & 1) * 8) = u32x2{w[4], w[5]};
  PROF_END(1);
}

constexpr int S_STAGE = 18 * 1024, S_SLOTS = 8;
constexpr int ring_issued(int s, int nst) { int a = 2 * s + 4 < s + S_SLOTS ? 2 * s + 4 : s + S_SLOTS; return a < nst ? a : nst; }

template <int NST, int CNT>
DI void small_loader(const char* __restrict__ ws, unsigned lds0, int first, int lane, unsigned abase, unsigned bbase PROF_ARG) {
  constexpr int NKS = 2 * NST;
  const char* src[CNT];
#pragma unroll
  for (int j = 0; j < CNT; ++j) {
    const int i = first + j;
    src[j] = ws + (i < 8 ? abase : bbase - 4 * NKS * 1024) + ((i >> 1) * NKS + (i & 1)) * 1024;
  }
  const unsigned vo = lane * 16;
  auto issue = [&](int st) {
    const unsigned base = lds0 + (st & (S_SLOTS - 1)) * S_STAGE + first * 1024;
#pragma unroll
    for (int j = 0; j < CNT; ++j) glds16(vo + st * 2048, src[j], base + j * 1024);
  };
  auto step = [&](auto S) {
    constexpr int s = S.value, n0 = ring_issued(s - 1, NST), n1 = ring_issued(s, NST);
    wait_vm_bar<(n0 - s - 1) * CNT>();
    if (s == 0) STAMP(3);
#pragma unroll
    for (int t = n0; t < n1; ++t) issue(t);
  };
  STAMP(1);
  issue(0); issue(1);
  STAMP(2);
  if constexpr (NST > 16) {
    static_for<0, 6>(step);
#pragma unroll 1
    for (int s = 6; s <= NST - 8; ++s) { wait_vm_bar<6 * CNT>(); issue(s + 7); }
    static_for<NST - 7, NST>(step);
  } else {
    static_for<0, NST>(step);
  }
}

template <int NST, int MODE, int ODD>
DI void small_compute(char* smem, const char* __restrict__ x, char* __restrict__ ws, char* __restrict__ out, int rt, int ct, int wid, int lane PROF_ARG) {
  lds_cptr L = (lds_cptr)smem;
  const int rh = wid >> 1, kp = wid & 1, t16 = lane & 15, g = lane >> 4;
  const int aO = (4 * rh + kp) * 1024 + lane * 16, bO = (8 + kp) * 1024 + lane * 16;
  const int mf = rt * 4 + 2 * rh + kp, tok = mf * 16 + t16;
  const int U0 = (ct >> 1) * 5 + 3 * ODD, UH = (ct >> 1) * 5 + 2;
  char* hp = ws + OFF_HF + (mf * 20 + U0) * 1024;
  char* hh = ws + OFF_HF + (mf * 20 + UH) * 1024 + ODD * 512;
  const unsigned fo = lane * 16, fh = (g >> 1) * 256 + t16 * 16 + (g & 1) * 8;
  const unsigned ro = tok * 1280 + U0 * 64 + g * 16, rhh = tok * 1280 + UH * 64 + ODD * 32 + g * 8;
  u32x4 res0, res1; u32x2 resh;
  if (MODE == 0) { res0 = *(const u32x4*)(x + ro); res1 = *(const u32x4*)(x + ro + 64); resh = *(const u32x2*)(x + rhh); }
  else { res0 = *(const u32x4*)(hp + fo); res1 = *(const u32x4*)(hp + 1024 + fo); resh = *(const u32x2*)(hh + fh); }

  f32x4 acc[2][5];
#pragma unroll
  for (int u = 0; u < 2; ++u)
#pragma unroll
    for (int v = 0; v < 5; ++v) acc[u][v] = f32x4{0.f, 0.f, 0.f, 0.f};
  bf16x8 a0[2], b0[5], a1[2], b1[5];
  auto rd = [&](bf16x8 (&a)[2], bf16x8 (&b)[5], int s) {
    if (GEMM_OFF) return;
    lds_cptr S = L + (s & (S_SLOTS - 1)) * S_STAGE;
#pragma unroll
    for (int u = 0; u < 2; ++u) a[u] = lds_b128(S + aO + u * 2048);
#pragma unroll
    for (int v = 0; v < 5; ++v) b[v] = lds_b128(S + bO + v * 2048);
  };
  auto mm = [&](bf16x8 (&a)[2], bf16x8 (&b)[5]) {
    if (GEMM_OFF) return;
#pragma unroll
    for (int u = 0; u < 2; ++u)
#pragma unroll
      for (int v = 0; v < 5; ++v) acc[u][v] = mfma16(b[v], a[u], acc[u][v]);
  };
  auto ilv = [&] {
#pragma unroll
    for (int i = 0; i < 3; ++i) { SGB(0x100, 2); SGB(0x8, 1); }
    SGB(0x100, 1); SGB(0x8, 7);
    SBAR();
  };
  wait_lgkm_bar(); STAMP(1); rd(a0, b0, 0);
#pragma unroll 1
  for (int s = 1; s + 1 < NST; s += 2) {
    wait_lgkm_bar(); rd(a1, b1, s); mm(a0, b0); ilv();
    wait_lgkm_bar(); rd(a0, b0, s + 1); mm(a1, b1); ilv();
  }
  if (NST % 2 == 0) { wait_lgkm_bar(); rd(a1, b1, NST - 1); mm(a0, b0); ilv(); mm(a1, b1); }
  else mm(a0, b0);
  STAMP(2);
  static_assert(((NST - 1) & (S_SLOTS - 1)) < 4, "scratch reuses ring slots 4 and 5, which are drained by now");
  lds_ptr scr = (lds_ptr)smem + 4 * S_STAGE + lane * 16;
#pragma unroll
  for (int v = 0; v < 5; ++v) *(LDS_AS f32x4*)(scr + wid * 5120 + v * 1024) = kp ? acc[0][v] : acc[1][v];
  wait_lgkm_bar();
  f32x4 y[5];
#pragma unroll
  for (int v = 0; v < 5; ++v) y[v] = (kp ? acc[1][v] : acc[0][v]) + *(const LDS_AS f32x4*)(scr + (wid ^ 1) * 5120 + v * 1024);
  auto fin = [&](f32x4 v, unsigned r0, unsigned r1, unsigned& o0, unsigned& o1) {
    const unsigned y0 = pk2(v[0], v[1]), y1 = pk2(v[2], v[3]);
    o0 = pk2(bflo(r0) + bflo(y0), bfhi(r0) + bfhi(y0)); o1 = pk2(bflo(r1) + bflo(y1), bfhi(r1) + bfhi(y1));
  };
  unsigned o[10];
  fin(y[ODD], res0[0], res0[1], o[0], o[1]); fin(y[ODD + 1], res0[2], res0[3], o[2], o[3]);
  fin(y[ODD + 2], res1[0], res1[1], o[4], o[5]); fin(y[ODD + 3], res1[2], res1[3], o[6], o[7]);
  fin(y[ODD ? 0 : 4], resh[0], resh[1], o[8], o[9]);
  if (MODE == 0) {
    *(u32x4*)(hp + fo) = u32x4{o[0], o[1], o[2], o[3]}; *(u32x4*)(hp + 1024 + fo) = u32x4{o[4], o[5], o[6], o[7]}; *(u32x2*)(hh + fh) = u32x2{o[8], o[9]};
    float ssum = 0.f, ssq = 0.f;
#pragma unroll
    for (int i = 0; i < 10; ++i) { const float e0 = bflo(o[i]), e1 = bfhi(o[i]); ssum += e0 + e1; ssq += e0 * e0 + e1 * e1; }
    ssum = xsum32(xsum16(ssum)); ssq = xsum32(xsum16(ssq));
    if (g == 0) *(f32x2*)(ws + OFF_STAT + tok * 64 + ct * 8) = f32x2{ssum, ssq};
  } else {
    *(u32x4*)(out + ro) = u32x4{o[0], o[1], o[2], o[3]}; *(u32x4*)(out + ro + 64) = u32x4{o[4], o[5], o[6], o[7]}; *(u32x2*)(out + rhh) = u32x2{o[8], o[9]};
  }
}

template <int NST, int MODE>
DI void small_gemm(char* smem, const char* __restrict__ x, char* __restrict__ ws, char* __restrict__ out) {
  constexpr int NKS = 2 * NST;
  const int bid = blockIdx.x, xcd = bid & 7, idx = bid >> 3;
  const int rt = (xcd >> 1) * 8 + (idx & 7), ct = (xcd & 1) * 4 + (idx >> 3);
  PROF_DECL STAMP(0);
  const int tid = threadIdx.x, lane = tid & 63, wid = __builtin_amdgcn_readfirstlane(tid >> 6);
  if (wid >= 4) {
    const unsigned lds0 = (unsigned)(uintptr_t)smem;
    const unsigned abase = (MODE == 0 ? OFF_OF : OFF_ACT) + rt * 4 * NKS * 1024, bbase = (MODE == 0 ? OFF_WOF : OFF_WDF) + ct * 5 * NKS * 1024;
    if (wid < 6) small_loader<NST, 5>(ws, lds0, (wid - 4) * 5, lane, abase, bbase PROF_PASS);
    else small_loader<NST, 4>(ws, lds0, 10 + (wid - 6) * 4, lane, abase, bbase PROF_PASS);
  } else if (ct & 1) small_compute<NST, MODE, 1>(smem, x, ws, out, rt, ct, wid, lane PROF_PASS);
  else small_compute<NST, MODE, 0>(smem, x, ws, out, rt, ct, wid, lane PROF_PASS);
  PROF_END(MODE ? 4 : 2);
}
extern "C" __global__ __launch_bounds__(512, 2) void k_wo(const char* __restrict__ x, char* __restrict__ ws) {
  __shared__ __attribute__((aligned(16))) char smem[S_SLOTS * S_STAGE];
  small_gemm<10, 0>(smem, x, ws, nullptr);
}

constexpr int G_STAGE = 44 * 1024;
extern "C" __global__ __launch_bounds__(512, 2) void k_gateup(char* __restrict__ ws) {
  __shared__ __attribute__((aligned(16))) char smem[3 * G_STAGE + 2048];
  const int bid = blockIdx.x, xcd = bid & 7, idx = bid >> 3;
  const int rt = (xcd >> 2) * 8 + (idx & 7), ct = (xcd & 3) * 4 + (idx >> 3);
  const int c8 = ct & 7, UB = (ct >> 3) * 27 + 3 * c8, UL = (ct >> 3) * 27 + 24 + min(c8 >> 1, 2);
  auto slot_nf = [&](int s) { const int wc = s >= 7, v = s - 7 * wc; return v < 4 ? 4 * UB + 8 * wc + v : v < 6 ? 4 * UB + 2 * wc + v : 4 * UL + 2 * (c8 & 1) + wc; };
  PROF_DECL STAMP(0);
  const int tid = threadIdx.x, lane = tid & 63, wid = __builtin_amdgcn_readfirstlane(tid >> 6);
  if (wid >= 4) {
    const int first = (wid - 4) * 11;
    const unsigned lds0 = (unsigned)(uintptr_t)smem + first * 1024, vo = lane * 16;
    const char* src[11];
#pragma unroll
    for (int j = 0; j < 11; ++j) {
      const int i = first + j;
      src[j] = ws + (i < 16 ? OFF_HF + (rt * 8 + (i >> 1)) * 20480 : OFF_WGUF + slot_nf((i - 16) >> 1) * 20480) + (i & 1) * 1024;
    }
    auto issue = [&](int st, int slot) {
#pragma unroll
      for (int j = 0; j < 11; ++j) glds16(vo + st * 2048, src[j], lds0 + slot * G_STAGE + j * 1024);
    };
    STAMP(1); issue(0, 0); issue(1, 1); STAMP(2);
    wait_vm_bar<11>(); STAMP(3);
    issue(2, 2);
    int slot = 0;
#pragma unroll 1
    for (int s = 1; s < 8; ++s) { wait_vm_bar<11>(); issue(s + 2, slot); slot = slot == 2 ? 0 : slot + 1; }
    wait_vm_bar<11>(); wait_vm_bar<0>();
    unsigned cs_; CODE_TOUCH(k_down, k_end, (wid & 1), cs_); CODE_DONE(cs_);
    PROF_END(3);
    return;
  }
  lds_cptr L = (lds_cptr)smem;
  LDS_AS float* sst = (LDS_AS float*)((lds_ptr)smem + 3 * G_STAGE);
  LDS_AS float* scs = sst + 256;
  if (tid < 128) {
    const f32x4* sp = (const f32x4*)(ws + OFF_STAT + (rt * 128 + tid) * 64);
    const f32x4 v = (sp[0] + sp[1]) + (sp[2] + sp[3]);
    const float mean = (v[0] + v[2]) * (1.0f / DM);
    *(LDS_AS f32x2*)(sst + tid * 2) = f32x2{-mean, fmaxf((v[1] + v[3]) * (1.0f / DM) - mean * mean, 0.f) + LN_EPS};
  }
  if (tid < 224) {
    const float* cp = (const float*)(ws + OFF_CSUM) + slot_nf(tid >> 4) * 16 + (tid & 15);
    float c[20];
#pragma unroll
    for (int k = 0; k < 20; ++k) c[k] = cp[k * 3456];
#pragma unroll
    for (int k = 0; k < 10; ++k) c[k] += c[k + 10];
    scs[tid] = ((c[0] + c[1]) + (c[2] + c[3])) + ((c[4] + c[5]) + (c[6] + c[7])) + (c[8] + c[9]);
  }
  const int wr = wid >> 1, wc = wid & 1, t16 = lane & 15, g = lane >> 4;
  const int aO = wr * 8192 + lane * 16, bO = (16 + 14 * wc) * 1024 + lane * 16;
  f32x4 acc[4][7];
  bf16x8 a0[4], b0[7], a1[4], b1[7];
  auto rd = [&](bf16x8 (&a)[4], bf16x8 (&b)[7], int so) {
    if (GEMM_OFF) return;
    a[0] = lds_b128(L + so + aO);
#pragma unroll
    for (int v = 0; v < 7; ++v) b[v] = lds_b128(L + so + bO + v * 2048);
#pragma unroll
    for (int u = 1; u < 4; ++u) a[u] = lds_b128(L + so + aO + u * 2048);
  };
  auto mm = [&](bf16x8 (&a)[4], bf16x8 (&b)[7]) {
    if (GEMM_OFF) return;
#pragma unroll
    for (int u = 0; u < 4; ++u)
#pragma unroll
      for (int v = 0; v < 7; ++v) acc[u][v] = mfma16(b[v], a[u], acc[u][v]);
  };
  auto ilv = [&] {
#pragma unroll
    for (int i = 0; i < 11; ++i) { SGB(0x8, 2); SGB(0x100, 1); }
    SGB(0x8, 6);
    SBAR();
  };
  wait_lgkm_bar();
  STAMP(1);
  rd(a0, b0, 0);
  float vk[4];
  {
    f32x4 c4[7];
#pragma unroll
    for (int v = 0; v < 7; ++v) c4[v] = *(const LDS_AS f32x4*)(scs + (7 * wc + v) * 16 + 4 * g);
#pragma unroll
    for (int u = 0; u < 4; ++u) {
      const f32x2 st = *(const LDS_AS f32x2*)(sst + (64 * wr + 16 * u + t16) * 2);
      vk[u] = st[1];
#pragma unroll
      for (int v = 0; v < 7; ++v) acc[u][v] = c4[v] * st[0];
    }
  }
  SBAR();
  int so = 0;
#pragma unroll 1
  for (int kt = 0; kt < 9; ++kt) {
    rd(a1, b1, so + 1024); mm(a0, b0); ilv();
    so = so == 2 * G_STAGE ? 0 : so + G_STAGE;
    wait_lgkm_bar();
    rd(a0, b0, so); mm(a1, b1); ilv();
  }
  rd(a1, b1, so + 1024); mm(a0, b0); ilv();
  mm(a1, b1);
  SBAR();
  STAMP(2);
  auto swi = [&](f32x4 x0, f32x4 x1, float c1, float k) {
    float a[4];
#pragma unroll
    for (int i = 0; i < 4; ++i) {
      const auto rr = __builtin_amdgcn_permlane32_swap(__float_as_uint(x0[i]), __float_as_uint(x1[i]), false, false);
      const float gt = __uint_as_float(rr[0]), up = __uint_as_float(rr[1]);
      a[i] = gt * up * __builtin_amdgcn_rcpf(__builtin_amdgcn_exp2f(gt * c1) * k + k);
    }
    return u32x2{pk2(a[0], a[1]), pk2(a[2], a[3])};
  };
  float c1[4];
#pragma unroll
  for (int u = 0; u < 4; ++u) c1[u] = -LOG2E * __builtin_amdgcn_rsqf(vk[u]);
  char* ab = ws + OFF_ACT + ((rt * 8 + 4 * wr) * 54 + UB) * 1024;
#pragma unroll
  for (int u = 0; u < 4; ++u) {
    const u32x2 pa = swi(acc[u][0], acc[u][1], c1[u], vk[u]), pb = swi(acc[u][2], acc[u][3], c1[u], vk[u]);
    *(u32x4*)(ab + (u * 54 + 2 * wc) * 1024 + lane * 16u) = u32x4{pa[0], pa[1], pb[0], pb[1]};
    *(u32x2*)(ab + (u * 54 + 1) * 1024 + wc * 8 + lane * 16u) = swi(acc[u][4], acc[u][5], c1[u], vk[u]);
  }
  if (c8 < 6) {
    char* al = ws + OFF_ACT + ((rt * 8 + 4 * wr) * 54 + UL) * 1024 + wc * 512 + (c8 & 1) * 8;
    const unsigned lo = (g >> 1) * 54 * 1024 + (g & 1) * 256 + t16 * 16;
#pragma unroll
    for (int u = 0; u < 4; u += 2)
      *(u32x2*)(al + u * 54 * 1024 + lo) = swi(acc[u][6], acc[u + 1][6], lane < 32 ? c1[u] : c1[u + 1], lane < 32 ? vk[u] : vk[u + 1]);
  }
  PROF_END(3);
}

extern "C" __global__ __launch_bounds__(512, 2) void k_down(char* __restrict__ ws, char* __restrict__ out) {
  __shared__ __attribute__((aligned(16))) char smem[S_SLOTS * S_STAGE];
  small_gemm<27, 1>(smem, nullptr, ws, out);
}
extern "C" __global__ void k_end() {}
extern "C" void kernel_launch(void* const* d_in, const int* in_sizes, int n_in, void* d_out, int out_size, void* d_ws, size_t ws_size,
                              hipStream_t stream) {
  const char* x = (const char*)d_in[0]; const char* wqkv = (const char*)d_in[1]; const char* wo = (const char*)d_in[2];
  const char* wg = (const char*)d_in[4]; const char* wu = (const char*)d_in[5]; const char* wd = (const char*)d_in[6];
  char* ws = (char*)d_ws;
  k_qkv<<<256, 512, 0, stream>>>(x, wqkv, ws);
  k_attn<<<256, 768, 0, stream>>>(wo, wg, wu, wd, ws);
  k_wo<<<256, 512, 0, stream>>>(x, ws);
  k_gateup<<<256, 512, 0, stream>>>(ws);
  k_down<<<256, 512, 0, stream>>>(ws, (char*)d_out);
}
```

```cpp
#define PROF
#define MEAS 0
#define FACT 4
#define NKERN 5
#include <hip/hip_runtime.h>
#include <hip/hip_bf16.h>
#include <stdint.h>

typedef __attribute__((ext_vector_type(8))) short bf16x8;
typedef __attribute__((ext_vector_type(4))) short bf16x4;
typedef __attribute__((ext_vector_type(2))) float f32x2;
typedef __attribute__((ext_vector_type(4))) float f32x4;
typedef __attribute__((ext_vector_type(16))) float f32x16;
typedef __attribute__((ext_vector_type(4))) unsigned u32x4;
typedef __attribute__((ext_vector_type(2))) unsigned u32x2;
typedef __attribute__((ext_vector_type(2))) __bf16 bf2;
typedef __attribute__((address_space(3))) const char* lds_cptr;
typedef __attribute__((address_space(3))) char* lds_ptr;
#define LDS_AS __attribute__((address_space(3)))
#define DI __device__ __forceinline__

constexpr int SEQ = 2048, DM = 640, NH = 32, DH = 20, FF = 1728;
constexpr float LN_EPS = 1e-5f;
constexpr float LOG2E = 1.4426950408889634f;
constexpr float QSCALE = 0.22360679774997896f * LOG2E;

constexpr int Q_TILE = 1280;
constexpr int Q_HEAD = 64 * Q_TILE;
constexpr int KV_TILE = 2688;
constexpr int KV_HEAD = 64 * KV_TILE;
constexpr unsigned OFF_Q    = 0;
constexpr unsigned OFF_KV   = OFF_Q + NH * Q_HEAD;
constexpr unsigned OFF_OF   = OFF_KV + NH * KV_HEAD;
constexpr unsigned OFF_HF   = OFF_OF + SEQ * DM * 2;
constexpr unsigned OFF_ACT  = OFF_HF + SEQ * DM * 2;
constexpr unsigned OFF_WOF  = OFF_ACT + SEQ * FF * 2;
constexpr unsigned OFF_WGUF = OFF_WOF + DM * DM * 2;
constexpr unsigned OFF_WDF  = OFF_WGUF + DM * FF * 4;
constexpr unsigned OFF_STAT = OFF_WDF + FF * DM * 2;
constexpr unsigned OFF_CSUM = OFF_STAT + SEQ * 64;

#define PROF_DECL unsigned long long pt_[8] = {0,0,0,0,0,0,0,0}; asm volatile("s_memrealtime %0\n\ts_waitcnt lgkmcnt(0)" : "=s"(pt_[6]) :: "memory");
#define STAMP(i) do { __builtin_amdgcn_sched_barrier(0); asm volatile("s_memtime %0\n\ts_waitcnt lgkmcnt(0)" : "=s"(pt_[i]) :: "memory"); __builtin_amdgcn_sched_barrier(0); } while (0)
#define PROF_END(k) do { asm volatile("s_waitcnt vmcnt(0)" ::: "memory"); STAMP(5); asm volatile("s_memrealtime %0\n\ts_waitcnt lgkmcnt(0)" : "=s"(pt_[7]) :: "memory"); \
  if ((threadIdx.x & 63) == 0) { unsigned long long* pp_ = (unsigned long long*)(ws + (128ull << 20)) + (((k) * 256 + blockIdx.x) * 12 + (threadIdx.x >> 6)) * 8; for (int i_ = 0; i_ < 8; ++i_) pp_[i_] = pt_[i_]; } } while (0)
#define PROF_ARG , unsigned long long (&pt_)[8]
#define PROF_PASS , pt_
DI void glds16(unsigned voff, const void* sbase, unsigned lds) {
  unsigned keep;
  asm volatile("s_mov_b32 %0, m0\n\ts_mov_b32 m0, %3\n\ts_nop 0\n\tglobal_load_lds_dwordx4 %1, %2\n\ts_mov_b32 m0, %0"
               : "=&s"(keep) : "v"(voff), "s"(sbase), "s"(lds) : "memory");
}
#define CODE_TOUCH(first, last, part, sink) do { unsigned long long pc_; int a_, b_; \
  asm volatile("s_getpc_b64 %0\n.Lct%=:\n\ts_mov_b32 %1, " #first "-.Lct%=\n\ts_mov_b32 %2, " #last "-.Lct%=" : "=s"(pc_), "=s"(a_), "=s"(b_)); \
  const int o_ = min(max(a_, 0) + (int)((threadIdx.x & 63) * 128 + (part) * 8192), max(b_, 0)); \
  asm volatile("global_load_dword %0, %1, %2" : "=v"(sink) : "v"(o_), "s"(pc_) : "memory"); } while (0)
#define CODE_DONE(sink) asm volatile("s_waitcnt vmcnt(0)" :: "v"(sink) : "memory")
template <int N> DI void wait_vm_bar() { asm volatile("s_waitcnt vmcnt(%0)\n\ts_barrier" :: "n"(N) : "memory"); }
template <int N> DI void wait_vm() { asm volatile("s_waitcnt vmcnt(%0)" :: "n"(N) : "memory"); }
#define SBAR() __builtin_amdgcn_sched_barrier(0)
#define SGB(mask, n) __builtin_amdgcn_sched_group_barrier(mask, n, 0)
DI void wait_lgkm_bar() { SBAR(); __builtin_amdgcn_s_waitcnt(0xC07F); asm volatile("s_barrier" ::: "memory"); SBAR(); }
#define GEMM_OFF 0
template <int I> struct IC { static constexpr int value = I; };
template <int B, int E, class F> DI void static_for(F&& f) { if constexpr (B < E) { f(IC<B>{}); static_for<B + 1, E>(f); } }
DI bf16x8 lds_b128(lds_cptr p) { return *(const LDS_AS bf16x8*)p; }
DI bf16x4 lds_b64(lds_cptr p) { return *(const LDS_AS bf16x4*)p; }
DI bf16x4 lds_tr(lds_cptr p) { return __builtin_amdgcn_ds_read_tr16_b64_v4i16((LDS_AS bf16x4*)p); }
DI f32x4 mfma16(bf16x8 a, bf16x8 b, f32x4 c) { return __builtin_amdgcn_mfma_f32_16x16x32_bf16(a, b, c, 0, 0, 0); }
DI f32x16 mfma32(bf16x8 a, bf16x8 b, f32x16 c) { return __builtin_amdgcn_mfma_f32_32x32x16_bf16(a, b, c, 0, 0, 0); }
DI f32x16 mfma32k8(bf16x4 a, bf16x4 b, f32x16 c) { return __builtin_amdgcn_mfma_f32_32x32x8bf16_1k(a, b, c, 0, 0, 0); }
DI unsigned pk2(float lo, float hi) { f32x2 v = {lo, hi}; return __builtin_bit_cast(unsigned, __builtin_convertvector(v, bf2)); }
DI float bflo(unsigned u) { return __uint_as_float(u << 16); }
DI float bfhi(unsigned u) { return __uint_as_float(u & 0xffff0000u); }
DI int div20(int c) { return (c * 3277) >> 16; }
DI float xsum32(float v) { auto r = __builtin_amdgcn_permlane32_swap(__float_as_uint(v), __float_as_uint(v), false, false); return __uint_as_float(r[0]) + __uint_as_float(r[1]); }
DI float xsum16(float v) { auto r = __builtin_amdgcn_permlane16_swap(__float_as_uint(v), __float_as_uint(v), false, false); return __uint_as_float(r[0]) + __uint_as_float(r[1]); }
constexpr bf16x8 ONES8 = {0x3f80, 0x3f80, 0x3f80, 0x3f80, 0x3f80, 0x3f80, 0x3f80, 0x3f80};

template <int MODE>
DI void qkv_compute(lds_cptr L, lds_ptr scr, int wid, int lane, int row0, int col0, char* __restrict__ ws PROF_ARG) {
  const int wr = wid >> 1, wc = wid & 1, r16 = lane & 15, g = lane >> 4, q = r16 >> 2, p = lane & 3;
  const int f = q | ((g & 1) << 2);
  const int rp = MODE == 2 ? 16 * (r16 >> 3) + (r16 & 7) : r16;
  constexpr int RSTEP = MODE == 2 ? 8 : 16;
  int aoff[2][2], boff[4];
#pragma unroll
  for (int e = 0; e < 2; ++e)
#pragma unroll
    for (int ks = 0; ks < 2; ++ks) { const int r = rp + e * RSTEP; aoff[e][ks] = (64 * wr + r) * 128 + (((4 * ks + g) ^ ((r >> 1) & 7) ^ ((r >> 2) & 4)) << 4); }
#pragma unroll
  for (int jj = 0; jj < 4; ++jj) { const int j = (jj + 2 * wr) & 3; boff[jj] = 16384 + (8 * g + q) * 256 + ((((4 * wc + j) ^ f) * 2 + (p >> 1)) << 4) + (p & 1) * 8; }
  const int pr0 = wc * 4096, pr1 = 4096 - pr0;

  f32x4 acc[4][4], rs[2], gr[2], cs[2];
#pragma unroll
  for (int i = 0; i < 4; ++i)
#pragma unroll
    for (int j = 0; j < 4; ++j) acc[i][j] = f32x4{0.f, 0.f, 0.f, 0.f};
#pragma unroll
  for (int i = 0; i < 2; ++i) { rs[i] = f32x4{0.f, 0.f, 0.f, 0.f}; gr[i] = rs[i]; cs[i] = rs[i]; }
  bf16x8 a0[4], b0[4], a1[4], b1[4];

  auto rd = [&](bf16x8 (&a)[4], bf16x8 (&b)[4], int slot, int ks) {
    if (GEMM_OFF && slot + ks > 0) return;
    lds_cptr S = L + slot;
#pragma unroll
    for (int jj = 0; jj < 4; ++jj) {
      bf16x4 lo = lds_tr(S + boff[jj] + ks * 8192), hi = lds_tr(S + boff[jj] + ks * 8192 + 1024);
      b[jj] = __builtin_shufflevector(lo, hi, 0, 1, 2, 3, 4, 5, 6, 7);
      a[jj] = lds_b128(S + aoff[jj & 1][ks] + (jj < 2 ? pr0 : pr1));
    }
  };
  auto mm = [&](bf16x8 (&a)[4], bf16x8 (&b)[4]) {
    if (GEMM_OFF) return;
#pragma unroll
    for (int ii = 0; ii < 4; ++ii)
#pragma unroll
      for (int jj = 0; jj < 4; ++jj)
        acc[ii][jj] = (MODE == 2) ? mfma16(a[ii], b[jj], acc[ii][jj]) : mfma16(b[jj], a[ii], acc[ii][jj]);
#pragma unroll
    for (int u = 0; u < 2; ++u) {
      rs[u] = mfma16(a[u], ONES8, rs[u]);
      gr[u] = mfma16(a[u], a[u], gr[u]);
      cs[u] = mfma16(ONES8, b[u], cs[u]);
    }
  };
  auto ilv = [&] {
#pragma unroll
    for (int i = 0; i < 12; ++i) { SGB(0x8, 1); SGB(0x100, 1); }
    SGB(0x8, 10);
    SBAR();
  };

  wait_lgkm_bar();
  STAMP(1);
  rd(a0, b0, 0, 0);
  SBAR();
#pragma unroll 1
  for (int kt = 0; kt < 9; ++kt) {
    rd(a1, b1, (kt & 3) * 32768, 1); mm(a0, b0); ilv();
    wait_lgkm_bar();
    rd(a0, b0, ((kt + 1) & 3) * 32768, 0); mm(a1, b1); ilv();
  }
  rd(a1, b1, 32768, 1); mm(a0, b0); ilv();
  mm(a1, b1);
  SBAR();
  STAMP(2);
  LDS_AS float* rsum = (LDS_AS float*)scr; LDS_AS float* rsq = rsum + 128; LDS_AS float* csum = rsum + 256;
#pragma unroll
  for (int u = 0; u < 2; ++u) {
    const int rb = 64 * wr + 32 * wc + 16 * u;
    if (r16 == 0) *(LDS_AS f32x4*)(rsum + rb + 4 * g) = rs[u];
    if (q == g) { const float d = p == 0 ? gr[u][0] : p == 1 ? gr[u][1] : p == 2 ? gr[u][2] : gr[u][3]; rsq[rb + r16] = d; }
    if (g == 0) csum[64 * wc + 32 * wr + 16 * u + r16] = cs[u][0];
  }
  constexpr float invD = 1.0f / DM;
  if (MODE != 2) {
    constexpr int TS = MODE == 0 ? Q_TILE : KV_TILE, HS = MODE == 0 ? Q_HEAD : KV_HEAD;
    unsigned off[4][2];
#pragma unroll
    for (int jj = 0; jj < 4; ++jj) {
      const int ck = col0 - MODE * DM + 64 * wc + 16 * ((jj + 2 * wr) & 3) + 4 * g, head = div20(ck), d = ck - head * DH;
      off[jj][0] = head * HS + (d < 16 ? (d >> 3) * 512 + (d & 7) * 2 + r16 * 16 : 1024 + r16 * 8);
      off[jj][1] = off[jj][0] + (d < 16 ? 256 : 128);
    }
    char* base = ws + (MODE == 0 ? OFF_Q : OFF_KV) + ((row0 >> 5) + 2 * wr) * TS;
    wait_lgkm_bar();
    f32x4 c4[4];
#pragma unroll
    for (int jj = 0; jj < 4; ++jj) c4[jj] = *(const LDS_AS f32x4*)(csum + 64 * wc + 16 * ((jj + 2 * wr) & 3) + 4 * g);
#pragma unroll
    for (int ii = 0; ii < 4; ++ii) {
      const int half = ((ii >> 1) + wc) & 1, rl = 64 * wr + 32 * half + 16 * (ii & 1) + r16;
      const float mean = rsum[rl] * invD, var = fmaxf(rsq[rl] * invD - mean * mean, 0.f);
      const float rstd = __builtin_amdgcn_rsqf(var + LN_EPS) * (MODE == 0 ? QSCALE : 1.0f), nb = -mean * rstd;
      char* tb = base + half * TS;
#pragma unroll
      for (int jj = 0; jj < 4; ++jj) {
        const f32x4 v = acc[ii][jj] * rstd + c4[jj] * nb;
        *(u32x2*)(tb + off[jj][ii & 1]) = u32x2{pk2(v[0], v[1]), pk2(v[2], v[3])};
      }
    }
  } else {
    unsigned off[4];
#pragma unroll
    for (int jj = 0; jj < 4; ++jj) {
      const int cv = col0 - 2 * DM + 64 * wc + 16 * ((jj + 2 * wr) & 3) + r16, head = div20(cv), d = cv - head * DH;
      off[jj] = head * KV_HEAD + 1280 + g * 352 + d * 16;
    }
    char* base = ws + OFF_KV + ((row0 >> 5) + 2 * wr) * KV_TILE;
    { const int head = div20(col0 - 2 * DM + 19) + (lane >> 3), t = lane & 7;
      if (wc == 0 && head * DH < col0 - 2 * DM + 128) *(bf16x8*)(base + head * KV_HEAD + (t >> 2) * KV_TILE + 1280 + (t & 3) * 352 + 320) = ONES8; }
    wait_lgkm_bar();
    float c1[4];
#pragma unroll
    for (int jj = 0; jj < 4; ++jj) c1[jj] = csum[64 * wc + 16 * ((jj + 2 * wr) & 3) + r16];
#pragma unroll
    for (int ip = 0; ip < 2; ++ip) {
      const int half = (ip + wc) & 1;
      f32x4 rstd[2], nb[2];
#pragma unroll
      for (int e = 0; e < 2; ++e) {
        const int rl = 64 * wr + 32 * half + 16 * e + 4 * g;
        const f32x4 mean = *(const LDS_AS f32x4*)(rsum + rl) * invD, q4 = *(const LDS_AS f32x4*)(rsq + rl);
#pragma unroll
        for (int i = 0; i < 4; ++i) rstd[e][i] = __builtin_amdgcn_rsqf(fmaxf(q4[i] * invD - mean[i] * mean[i], 0.f) + LN_EPS);
        nb[e] = -mean * rstd[e];
      }
      char* tb = base + half * KV_TILE;
#pragma unroll
      for (int jj = 0; jj < 4; ++jj) {
        const f32x4 v0 = acc[2 * ip][jj] * rstd[0] + nb[0] * c1[jj], v1 = acc[2 * ip + 1][jj] * rstd[1] + nb[1] * c1[jj];
        *(u32x4*)(tb + off[jj]) = u32x4{pk2(v0[0], v0[1]), pk2(v0[2], v0[3]), pk2(v1[0], v1[1]), pk2(v1[2], v1[3])};
      }
    }
  }
}

extern "C" __global__ __launch_bounds__(512, 2) void k_qkv(const char* __restrict__ x, const char* __restrict__ w, char* __restrict__ ws) {
  __shared__ __attribute__((aligned(16))) char smem[4 * 32768 + 2048];
  const int bid = blockIdx.x, xcd = bid & 7, idx = bid >> 3;
  const int rt = (xcd >> 1) * 4 + (idx & 3), ct = (xcd & 1) * 8 + (idx >> 2);
  if (ct >= 15) return;
  PROF_DECL STAMP(0);
  const int tid = threadIdx.x, lane = tid & 63, wid = __builtin_amdgcn_readfirstlane(tid >> 6);
  if (wid >= 4) {
    const int lw = wid - 4;
    const unsigned lds0 = (unsigned)(uintptr_t)smem + lw * 1024;
    const int ra = 8 * lw + (lane >> 3), rb = 4 * lw + (lane >> 4), f = (rb & 3) | ((rb >> 1) & 4);
    const unsigned voA = ra * 1280 + (((lane & 7) ^ ((ra >> 1) & 7) ^ ((ra >> 2) & 4)) << 4), voB = rb * 3840 + (((lane & 15) ^ (f << 1)) << 4);
    const char* pa = x + (size_t)rt * 128 * 1280; const char* pb = w + ct * 256;
    auto issue = [&](int st) {
      const unsigned base = lds0 + (st & 3) * 32768;
#pragma unroll
      for (int j = 0; j < 4; ++j) { glds16(voA, pa + st * 128 + j * 32 * 1280, base + j * 4096); glds16(voB, pb + (st * 64 + j * 16) * 3840, base + 16384 + j * 4096); }
    };
    STAMP(1); issue(0); issue(1); STAMP(2);
    wait_vm_bar<8>(); STAMP(3);
    issue(2); issue(3);
#pragma unroll 1
    for (int s = 1; s < 7; ++s) { wait_vm_bar<16>(); issue(s + 3); }
    wait_vm_bar<16>(); wait_vm_bar<8>(); wait_vm_bar<0>();
    PROF_END(0);
    return;
  }
  lds_cptr L = (lds_cptr)smem; lds_ptr scr = (lds_ptr)smem + 4 * 32768;
  unsigned cs_; CODE_TOUCH(k_qkv, k_wo, wid, cs_);
  if (ct < 5) qkv_compute<0>(L, scr, wid, lane, rt * 128, ct * 128, ws PROF_PASS);
  else if (ct < 10) qkv_compute<1>(L, scr, wid, lane, rt * 128, ct * 128, ws PROF_PASS);
  else qkv_compute<2>(L, scr, wid, lane, rt * 128, ct * 128, ws PROF_PASS);
  CODE_DONE(cs_);
  PROF_END(0);
}

constexpr int SC_BYTES = 8 * KV_TILE;
constexpr int STG_OFF = 6 * SC_BYTES;

DI float rowmax16(const f32x16& s) {
  float a = fmaxf(fmaxf(s[0], s[1]), s[2]), b = fmaxf(fmaxf(s[3], s[4]), s[5]);
  a = fmaxf(fmaxf(a, s[6]), s[7]); b = fmaxf(fmaxf(b, s[8]), s[9]);
  a = fmaxf(fmaxf(a, s[10]), s[11]); b = fmaxf(fmaxf(b, s[12]), s[13]);
  a = fmaxf(fmaxf(a, s[14]), s[15]);
  const float m = fmaxf(a, b);
  auto rr = __builtin_amdgcn_permlane32_swap(__float_as_uint(m), __float_as_uint(m), false, false);
  return fmaxf(__uint_as_float(rr[0]), __uint_as_float(rr[1]));
}
DI unsigned exp_pk(float a, float b) { return pk2(__builtin_amdgcn_exp2f(a), __builtin_amdgcn_exp2f(b)); }

template <int KIND>
DI void retile_issue(const char* __restrict__ w0, const char* __restrict__ w1, int tile, int ks, int lane, unsigned stg) {
  if (KIND == 0) {
    const unsigned vo = (ks * 32 + (lane >> 3)) * (FF * 2) + tile * 128 + (lane & 7) * 16;
#pragma unroll
    for (int j = 0; j < 4; ++j) { glds16(vo, w0 + j * 8 * (FF * 2), stg + j * 1024); glds16(vo, w1 + j * 8 * (FF * 2), stg + 4096 + j * 1024); }
  } else {
#pragma unroll
    for (int j = 0; j < 8; ++j) {
      int k = ks * 32 + 4 * j + (lane >> 4);
      if (KIND == 2) { const int pp = div20(k) >> 1, e = k - 40 * pp; k = 40 * pp + (e < 16 ? e : e < 32 ? e + 4 : e < 36 ? e - 16 : e); }
      glds16(k * (DM * 2) + tile * 256 + (lane & 15) * 16, w0, stg + j * 1024);
    }
  }
}
template <int KIND>
DI void retile_store(char* __restrict__ ws, lds_cptr S, int tile, int ks, int lane) {
  const int g = lane >> 4, q = (lane & 15) >> 2, p = lane & 3;
#pragma unroll
  for (int f = 0; f < 8; ++f) {
    lds_cptr a; int hi; unsigned dst;
    if (KIND == 0) {
      a = S + (p >> 1) * 4096 + (8 * g + q) * 128 + (p & 1) * 16 + (32 * (f >> 2) + 16 * (f & 1) + 4 * ((f >> 1) & 1)) * 2; hi = 512;
      dst = OFF_WGUF + ((8 * tile + f) * 20 + ks) * 1024;
    } else {
      const int U = 4 * tile + (f >> 1), j = f & 1;
      a = S + (8 * g + q) * 256 + (32 * (f >> 1) + (U % 5 == 2 ? 16 * j + 4 * p : 8 * p + 4 * j)) * 2; hi = 1024;
      dst = (KIND == 1 ? OFF_WDF : OFF_WOF) + ((2 * U + j) * (KIND == 1 ? 54 : 20) + ks) * 1024;
    }
    const bf16x8 fr = __builtin_shufflevector(lds_tr(a), lds_tr(a + hi), 0, 1, 2, 3, 4, 5, 6, 7);
    *(bf16x8*)(ws + dst + lane * 16) = fr;
    if (KIND == 0) {
      const f32x4 c = mfma16(fr, ONES8, f32x4{0.f, 0.f, 0.f, 0.f});
      if ((lane & 15) == 0) *(f32x4*)(ws + OFF_CSUM + ((ks * 216 + 8 * tile + f) * 16 + 4 * g) * 4) = c;
    }
  }
}

extern "C" __global__ __launch_bounds__(768, 3) void k_attn(const char* __restrict__ wo, const char* __restrict__ wg, const char* __restrict__ wu,
                                                 const char* __restrict__ wd, char* __restrict__ ws) {
  __shared__ __attribute__((aligned(16))) char smem[6 * SC_BYTES + 4 * 8192];
  const int bid = blockIdx.x, xcd = bid & 7, idx = bid >> 3, head = xcd * 4 + (idx >> 3), qt = idx & 7;
  const int tid = threadIdx.x, lane = tid & 63, wid = __builtin_amdgcn_readfirstlane(tid >> 6);
  PROF_DECL STAMP(0);
  lds_cptr L = (lds_cptr)smem;
  if (wid >= 8) {
    const int hw = wid - 8, kh = hw >> 1, sub = hw & 1;
    const unsigned lds0 = (unsigned)(uintptr_t)smem, vo = lane * 16;
    const char* src = ws + OFF_KV + head * KV_HEAD + kh * 4 * SC_BYTES + sub * 1024;
    const unsigned dst = lds0 + kh * SC_BYTES + sub * 1024;
    auto piece = [&](int sc, int slot, int i) { const int o = (i == 10 ? (sub ? 9 : 10) : i) * 2048; glds16(vo, src + sc * SC_BYTES + o, dst + slot * 2 * SC_BYTES + o); };
#pragma unroll
    for (int i = 0; i < 4; ++i) piece(0, 0, i);
    STAMP(1);
    wait_vm_bar<0>();
    STAMP(2);
#pragma unroll
    for (int i = 4; i < 11; ++i) piece(0, 0, i);
    wait_vm_bar<0>();
#pragma unroll
    for (int i = 0; i < 11; ++i) piece(1, 1, i);
#pragma unroll
    for (int i = 0; i < 11; ++i) piece(2, 2, i);
    const int job = bid * 4 + hw;
    const unsigned stg = lds0 + STG_OFF + hw * 8192;
    int tile = 0, ks = 0;
    if (job < 540)      { ks = job / 27; tile = job - ks * 27; retile_issue<0>(wg, wu, tile, ks, lane, stg); }
    else if (job < 810) { ks = (job - 540) / 5; tile = job - 540 - ks * 5; retile_issue<1>(wd, wd, tile, ks, lane, stg); }
    else                { ks = min(job - 810, 99) / 5; tile = min(job - 810, 99) - ks * 5; retile_issue<2>(wo, wo, tile, ks, lane, stg); }
    wait_vm_bar<8>();
    STAMP(3);
#pragma unroll
    for (int i = 0; i < 11; ++i) piece(3, 0, i);
    wait_vm<11>();
    lds_cptr S = L + STG_OFF + hw * 8192;
    if (job < 540) retile_store<0>(ws, S, tile, ks, lane);
    else if (job < 810) retile_store<1>(ws, S, tile, ks, lane);
    else if (job < 910) retile_store<2>(ws, S, tile, ks, lane);
    STAMP(4);
    unsigned cs_; CODE_TOUCH(k_wo, k_down, hw, cs_);
    wait_vm_bar<0>();
    CODE_DONE(cs_);
    PROF_END(1);
    return;
  }
  const int r = lane & 31, h = lane >> 5, qp = wid & 3, kh = wid >> 2;
  const char* qb = ws + OFF_Q + head * Q_HEAD + (qt * 8 + qp * 2) * Q_TILE;
  const bf16x8 qaA = *(const bf16x8*)(qb + lane * 16), qaB = *(const bf16x8*)(qb + Q_TILE + lane * 16);
  u32x2 qtA = *(const u32x2*)(qb + 1024 + r * 8), qtB = *(const u32x2*)(qb + Q_TILE + 1024 + r * 8);
  if (h) { qtA = u32x2{0u, 0u}; qtB = qtA; }

  const int rm = r <= 20 ? r : (r < 28 ? 20 : r - 16);
  const int kaO = lane * 16, kbO = h ? 1280 + 320 : 1024 + r * 8, vO = 1280 + h * 352 + rm * 16;
  f32x16 zero, oA, oB, sA, sB;
#pragma unroll
  for (int i = 0; i < 16; ++i) { zero[i] = 0.f; oA[i] = 0.f; oB[i] = 0.f; }
  bf16x8 ka[2], v0[2], v1[2]; bf16x4 kb[2];
  lds_cptr pk = L + kh * SC_BYTES + kaO, pb = L + kh * SC_BYTES + kbO, pv = L + kh * SC_BYTES + vO;
  wait_lgkm_bar();
  STAMP(1);
  kb[0] = lds_b64(pb); ka[0] = lds_b128(pk); v0[0] = lds_b128(pv); v1[0] = lds_b128(pv + 704);
  v1[1] = v1[0];
  float mA, mB;
  {
    sA = mfma32(ka[0], qaA, mfma32k8(kb[0], __builtin_bit_cast(bf16x4, qtA), zero));
    sB = mfma32(ka[0], qaB, mfma32k8(kb[0], __builtin_bit_cast(bf16x4, qtB), zero));
    const unsigned nA = pk2(-rowmax16(sA), 0.f), nB = pk2(-rowmax16(sB), 0.f);
    mA = -bflo(nA); mB = -bflo(nB);
    if (h) { qtA[0] = nA; qtB[0] = nB; }
  }
  const bf16x4 qbA = __builtin_bit_cast(bf16x4, qtA), qbB = __builtin_bit_cast(bf16x4, qtB);
  sA = mfma32(ka[0], qaA, mfma32k8(kb[0], qbA, zero));
  u32x4 pl = {0u, 0u, 0u, 0u};

  auto phase = [&](const f32x16& sC, f32x16& oC, f32x16& oP, bf16x8 vP, f32x16& sN, bf16x8 qa, bf16x4 qb4, bf16x8 kA, bf16x4 kB, bf16x8 vC, auto&& reads) {
    u32x4 p0;
    oP = mfma32(vP, __builtin_bit_cast(bf16x8, pl), oP); reads(); SBAR();
    p0[0] = exp_pk(sC[0], sC[1]); p0[1] = exp_pk(sC[2], sC[3]); SBAR();
    sN = mfma32k8(kB, qb4, zero); SBAR();
    p0[2] = exp_pk(sC[4], sC[5]); p0[3] = exp_pk(sC[6], sC[7]); SBAR();
    sN = mfma32(kA, qa, sN); SBAR();
    pl[0] = exp_pk(sC[8], sC[9]); pl[1] = exp_pk(sC[10], sC[11]); SBAR();
    oC = mfma32(vC, __builtin_bit_cast(bf16x8, p0), oC); SBAR();
    pl[2] = exp_pk(sC[12], sC[13]); pl[3] = exp_pk(sC[14], sC[15]); SBAR();
  };
#pragma unroll 1
  for (int sc = 0; sc < 4; ++sc) {
    const int dn = sc == 2 ? -4 * SC_BYTES : sc == 3 ? 0 : 2 * SC_BYTES;
#pragma unroll
    for (int t = 0; t < 8; ++t) {
      const int c = t & 1, n = c ^ 1, no = t < 7 ? (t + 1) * KV_TILE : dn;
      if (t == 2 && sc == 0) wait_lgkm_bar();
      if (t == 7 && !(sc & 1)) wait_lgkm_bar();
      phase(sA, oA, oB, v1[n], sB, qaB, qbB, ka[c], kb[c], v0[c], [&] { kb[n] = lds_b64(pb + no); ka[n] = lds_b128(pk + no); });
      phase(sB, oB, oA, v1[c], sA, qaA, qbA, ka[n], kb[n], v0[c], [&] { v0[n] = lds_b128(pv + no); v1[n] = lds_b128(pv + no + 704); });
    }
    pk += dn; pb += dn; pv += dn;
  }
  oB = mfma32(v1[1], __builtin_bit_cast(bf16x8, pl), oB);
  STAMP(2);

  {
    const float lA = h ? oA[8] : oA[12], lB = h ? oB[8] : oB[12];
    bool ok = lA < 1.0e18f && lA > 1.0e-18f && lB < 1.0e18f && lB > 1.0e-18f;
#pragma unroll
    for (int i = 0; i < 12; ++i) ok = ok && fabsf(oA[i]) < 3.0e38f && fabsf(oB[i]) < 3.0e38f;
    if (!__all(ok)) {
      const char* kvb = ws + OFF_KV + head * KV_HEAD + kh * 4 * SC_BYTES;
      const bf16x4 z4 = {0, 0, 0, 0}, tA = h ? z4 : qbA, tB = h ? z4 : qbB;
      mA = -INFINITY; mB = -INFINITY; oA = zero; oB = zero;
      auto upd = [&](f32x16 s, float& m, f32x16& o, bf16x8 w0, bf16x8 w1) {
        const float mn = fmaxf(m, rowmax16(s)), alpha = __builtin_amdgcn_exp2f(m - mn);
        m = mn; o *= alpha;
        u32x4 a, b;
#pragma unroll
        for (int i = 0; i < 4; ++i) { a[i] = exp_pk(s[2 * i] - mn, s[2 * i + 1] - mn); b[i] = exp_pk(s[8 + 2 * i] - mn, s[9 + 2 * i] - mn); }
        o = mfma32(w1, __builtin_bit_cast(bf16x8, b), mfma32(w0, __builtin_bit_cast(bf16x8, a), o));
      };
#pragma unroll 1
      for (int t = 0; t < 32; ++t) {
        const char* tb = kvb + t * KV_TILE;
        const bf16x8 k8 = *(const bf16x8*)(tb + kaO), w0 = *(const bf16x8*)(tb + vO), w1 = *(const bf16x8*)(tb + vO + 704);
        const bf16x4 k4 = *(const bf16x4*)(tb + kbO);
        upd(mfma32(k8, qaA, mfma32k8(k4, tA, zero)), mA, oA, w0, w1);
        upd(mfma32(k8, qaB, mfma32k8(k4, tB, zero)), mB, oB, w0, w1);
      }
    }
  }
  const f32x16 oS = kh ? oA : oB, oK = kh ? oB : oA;
  const float mS = kh ? mA : mB, mK = kh ? mB : mA;
  lds_ptr X = (lds_ptr)smem + 2 * SC_BYTES + lane * 16;
#pragma unroll
  for (int u = 0; u < 3; ++u) *(LDS_AS f32x4*)(X + wid * 4096 + u * 1024) = f32x4{oS[4 * u], oS[4 * u + 1], oS[4 * u + 2], oS[4 * u + 3]};
  *(LDS_AS f32x2*)(X + wid * 4096 + 3072) = f32x2{oS[12], mS};
  wait_lgkm_bar();
  f32x4 y[3];
#pragma unroll
  for (int u = 0; u < 3; ++u) y[u] = *(const LDS_AS f32x4*)(X + (wid ^ 4) * 4096 + u * 1024);
  const f32x2 y3 = *(const LDS_AS f32x2*)(X + (wid ^ 4) * 4096 + 3072);
  const float mx = fmaxf(mK, y3[1]);
  float fK = __builtin_amdgcn_exp2f(mK - mx), fO = __builtin_amdgcn_exp2f(y3[1] - mx);
  const float inv = 1.0f / (fK * (h ? oK[8] : oK[12]) + fO * (h ? y[2][0] : y3[0]));
  fK *= inv; fO *= inv;
  unsigned w[6];
#pragma unroll
  for (int u = 0; u < 3; ++u) {
    w[2 * u] = pk2(fK * oK[4 * u] + fO * y[u][0], fK * oK[4 * u + 1] + fO * y[u][1]);
    w[2 * u + 1] = pk2(fK * oK[4 * u + 2] + fO * y[u][2], fK * oK[4 * u + 3] + fO * y[u][3]);
  }
  const auto s0 = __builtin_amdgcn_permlane32_swap(w[0], w[2], false, false), s1 = __builtin_amdgcn_permlane32_swap(w[1], w[3], false, false);
  const int tok = qt * 256 + qp * 64 + kh * 32 + r;
  char* ob = ws + OFF_OF + (tok >> 4) * 20480 + (tok & 15) * 16;
  const int U = (head >> 1) * 5 + (head & 1) * 2 + h, UT = (head >> 1) * 5 + 4;
  *(u32x4*)(ob + U * 256) = u32x4{s0[0], s1[0], s0[1], s1[1]};
  if (!h) *(u32x2*)(ob + UT * 256 + (head & 1) * 8) = u32x2{w[4], w[5]};
  PROF_END(1);
}

constexpr int S_STAGE = 18 * 1024, S_SLOTS = 8;
constexpr int ring_issued(int s, int nst) { int a = 2 * s + 4 < s + S_SLOTS ? 2 * s + 4 : s + S_SLOTS; return a < nst ? a : nst; }

template <int NST, int CNT>
DI void small_loader(const char* __restrict__ ws, unsigned lds0, int first, int lane, unsigned abase, unsigned bbase PROF_ARG) {
  constexpr int NKS = 2 * NST;
  const char* src[CNT];
#pragma unroll
  for (int j = 0; j < CNT; ++j) {
    const int i = first + j;
    src[j] = ws + (i < 8 ? abase : bbase - 4 * NKS * 1024) + ((i >> 1) * NKS + (i & 1)) * 1024;
  }
  const unsigned vo = lane * 16;
  auto issue = [&](int st) {
    const unsigned base = lds0 + (st & (S_SLOTS - 1)) * S_STAGE + first * 1024;
#pragma unroll
    for (int j = 0; j < CNT; ++j) glds16(vo + st * 2048, src[j], base + j * 1024);
  };
  auto step = [&](auto S) {
    constexpr int s = S.value, n0 = ring_issued(s - 1, NST), n1 = ring_issued(s, NST);
    wait_vm_bar<(n0 - s - 1) * CNT>();
    if (s == 0) STAMP(3);
#pragma unroll
    for (int t = n0; t < n1; ++t) issue(t);
  };
  STAMP(1);
  issue(0); issue(1);
  STAMP(2);
  if constexpr (NST > 16) {
    static_for<0, 6>(step);
#pragma unroll 1
    for (int s = 6; s <= NST - 8; ++s) { wait_vm_bar<6 * CNT>(); issue(s + 7); }
    static_for<NST - 7, NST>(step);
  } else {
    static_for<0, NST>(step);
  }
}

template <int NST, int MODE, int ODD>
DI void small_compute(char* smem, const char* __restrict__ x, char* __restrict__ ws, char* __restrict__ out, int rt, int ct, int wid, int lane PROF_ARG) {
  lds_cptr L = (lds_cptr)smem;
  const int rh = wid >> 1, kp = wid & 1, t16 = lane & 15, g = lane >> 4;
  const int aO = (4 * rh + kp) * 1024 + lane * 16, bO = (8 + kp) * 1024 + lane * 16;
  const int mf = rt * 4 + 2 * rh + kp, tok = mf * 16 + t16;
  const int U0 = (ct >> 1) * 5 + 3 * ODD, UH = (ct >> 1) * 5 + 2;
  char* hp = ws + OFF_HF + (mf * 20 + U0) * 1024;
  char* hh = ws + OFF_HF + (mf * 20 + UH) * 1024 + ODD * 512;
  const unsigned fo = lane * 16, fh = (g >> 1) * 256 + t16 * 16 + (g & 1) * 8;
  const unsigned ro = tok * 1280 + U0 * 64 + g * 16, rhh = tok * 1280 + UH * 64 + ODD * 32 + g * 8;
  u32x4 res0, res1; u32x2 resh;
  if (MODE == 0) { res0 = *(const u32x4*)(x + ro); res1 = *(const u32x4*)(x + ro + 64); resh = *(const u32x2*)(x + rhh); }
  else { res0 = *(const u32x4*)(hp + fo); res1 = *(const u32x4*)(hp + 1024 + fo); resh = *(const u32x2*)(hh + fh); }

  f32x4 acc[2][5];
#pragma unroll
  for (int u = 0; u < 2; ++u)
#pragma unroll
    for (int v = 0; v < 5; ++v) acc[u][v] = f32x4{0.f, 0.f, 0.f, 0.f};
  bf16x8 a0[2], b0[5], a1[2], b1[5];
  auto rd = [&](bf16x8 (&a)[2], bf16x8 (&b)[5], int s) {
    if (GEMM_OFF) return;
    lds_cptr S = L + (s & (S_SLOTS - 1)) * S_STAGE;
#pragma unroll
    for (int u = 0; u < 2; ++u) a[u] = lds_b128(S + aO + u * 2048);
#pragma unroll
    for (int v = 0; v < 5; ++v) b[v] = lds_b128(S + bO + v * 2048);
  };
  auto mm = [&](bf16x8 (&a)[2], bf16x8 (&b)[5]) {
    if (GEMM_OFF) return;
#pragma unroll
    for (int u = 0; u < 2; ++u)
#pragma unroll
      for (int v = 0; v < 5; ++v) acc[u][v] = mfma16(b[v], a[u], acc[u][v]);
  };
  auto ilv = [&] {
#pragma unroll
    for (int i = 0; i < 3; ++i) { SGB(0x100, 2); SGB(0x8, 1); }
    SGB(0x100, 1); SGB(0x8, 7);
    SBAR();
  };
  wait_lgkm_bar(); STAMP(1); rd(a0, b0, 0);
#pragma unroll 1
  for (int s = 1; s + 1 < NST; s += 2) {
    wait_lgkm_bar(); rd(a1, b1, s); mm(a0, b0); ilv();
    wait_lgkm_bar(); rd(a0, b0, s + 1); mm(a1, b1); ilv();
  }
  if (NST % 2 == 0) { wait_lgkm_bar(); rd(a1, b1, NST - 1); mm(a0, b0); ilv(); mm(a1, b1); }
  else mm(a0, b0);
  STAMP(2);
  static_assert(((NST - 1) & (S_SLOTS - 1)) < 4, "scratch reuses ring slots 4 and 5, which are drained by now");
  lds_ptr scr = (lds_ptr)smem + 4 * S_STAGE + lane * 16;
#pragma unroll
  for (int v = 0; v < 5; ++v) *(LDS_AS f32x4*)(scr + wid * 5120 + v * 1024) = kp ? acc[0][v] : acc[1][v];
  wait_lgkm_bar();
  f32x4 y[5];
#pragma unroll
  for (int v = 0; v < 5; ++v) y[v] = (kp ? acc[1][v] : acc[0][v]) + *(const LDS_AS f32x4*)(scr + (wid ^ 1) * 5120 + v * 1024);
  auto fin = [&](f32x4 v, unsigned r0, unsigned r1, unsigned& o0, unsigned& o1) {
    const unsigned y0 = pk2(v[0], v[1]), y1 = pk2(v[2], v[3]);
    o0 = pk2(bflo(r0) + bflo(y0), bfhi(r0) + bfhi(y0)); o1 = pk2(bflo(r1) + bflo(y1), bfhi(r1) + bfhi(y1));
  };
  unsigned o[10];
  fin(y[ODD], res0[0], res0[1], o[0], o[1]); fin(y[ODD + 1], res0[2], res0[3], o[2], o[3]);
  fin(y[ODD + 2], res1[0], res1[1], o[4], o[5]); fin(y[ODD + 3], res1[2], res1[3], o[6], o[7]);
  fin(y[ODD ? 0 : 4], resh[0], resh[1], o[8], o[9]);
  if (MODE == 0) {
    *(u32x4*)(hp + fo) = u32x4{o[0], o[1], o[2], o[3]}; *(u32x4*)(hp + 1024 + fo) = u32x4{o[4], o[5], o[6], o[7]}; *(u32x2*)(hh + fh) = u32x2{o[8], o[9]};
    float ssum = 0.f, ssq = 0.f;
#pragma unroll
    for (int i = 0; i < 10; ++i) { const float e0 = bflo(o[i]), e1 = bfhi(o[i]); ssum += e0 + e1; ssq += e0 * e0 + e1 * e1; }
    ssum = xsum32(xsum16(ssum)); ssq = xsum32(xsum16(ssq));
    if (g == 0) *(f32x2*)(ws + OFF_STAT + tok * 64 + ct * 8) = f32x2{ssum, ssq};
  } else {
    *(u32x4*)(out + ro) = u32x4{o[0], o[1], o[2], o[3]}; *(u32x4*)(out + ro + 64) = u32x4{o[4], o[5], o[6], o[7]}; *(u32x2*)(out + rhh) = u32x2{o[8], o[9]};
  }
}

template <int NST, int MODE>
DI void small_gemm(char* smem, const char* __restrict__ x, char* __restrict__ ws, char* __restrict__ out) {
  constexpr int NKS = 2 * NST;
  const int bid = blockIdx.x, xcd = bid & 7, idx = bid >> 3;
  const int rt = (xcd >> 1) * 8 + (idx & 7), ct = (xcd & 1) * 4 + (idx >> 3);
  PROF_DECL STAMP(0);
  const int tid = threadIdx.x, lane = tid & 63, wid = __builtin_amdgcn_readfirstlane(tid >> 6);
  if (wid >= 4) {
    const unsigned lds0 = (unsigned)(uintptr_t)smem;
    const unsigned abase = (MODE == 0 ? OFF_OF : OFF_ACT) + rt * 4 * NKS * 1024, bbase = (MODE == 0 ? OFF_WOF : OFF_WDF) + ct * 5 * NKS * 1024;
    if (wid < 6) small_loader<NST, 5>(ws, lds0, (wid - 4) * 5, lane, abase, bbase PROF_PASS);
    else small_loader<NST, 4>(ws, lds0, 10 + (wid - 6) * 4, lane, abase, bbase PROF_PASS);
  } else if (ct & 1) small_compute<NST, MODE, 1>(smem, x, ws, out, rt, ct, wid, lane PROF_PASS);
  else small_compute<NST, MODE, 0>(smem, x, ws, out, rt, ct, wid, lane PROF_PASS);
  PROF_END(MODE ? 4 : 2);
}
extern "C" __global__ __launch_bounds__(512, 2) void k_wo(const char* __restrict__ x, char* __restrict__ ws) {
  __shared__ __attribute__((aligned(16))) char smem[S_SLOTS * S_STAGE];
  small_gemm<10, 0>(smem, x, ws, nullptr);
}

constexpr int G_STAGE = 44 * 1024;
extern "C" __global__ __launch_bounds__(512, 2) void k_gateup(char* __restrict__ ws) {
  __shared__ __attribute__((aligned(16))) char smem[3 * G_STAGE + 2048];
  const int bid = blockIdx.x, xcd = bid & 7, idx = bid >> 3;
  const int rt = (xcd >> 2) * 8 + (idx & 7), ct = (xcd & 3) * 4 + (idx >> 3);
  const int c8 = ct & 7, UB = (ct >> 3) * 27 + 3 * c8, UL = (ct >> 3) * 27 + 24 + min(c8 >> 1, 2);
  auto slot_nf = [&](int s) { const int wc = s >= 7, v = s - 7 * wc; return v < 4 ? 4 * UB + 8 * wc + v : v < 6 ? 4 * UB + 2 * wc + v : 4 * UL + 2 * (c8 & 1) + wc; };
  PROF_DECL STAMP(0);
  const int tid = threadIdx.x, lane = tid & 63, wid = __builtin_amdgcn_readfirstlane(tid >> 6);
  if (wid >= 4) {
    const int first = (wid - 4) * 11;
    const unsigned lds0 = (unsigned)(uintptr_t)smem + first * 1024, vo = lane * 16;
    const char* src[11];
#pragma unroll
    for (int j = 0; j < 11; ++j) {
      const int i = first + j;
      src[j] = ws + (i < 16 ? OFF_HF + (rt * 8 + (i >> 1)) * 20480 : OFF_WGUF + slot_nf((i - 16) >> 1) * 20480) + (i & 1) * 1024;
    }
    auto issue = [&](int st, int slot) {
#pragma unroll
      for (int j = 0; j < 11; ++j) glds16(vo + st * 2048, src[j], lds0 + slot * G_STAGE + j * 1024);
    };
    STAMP(1); issue(0, 0); issue(1, 1); STAMP(2);
    wait_vm_bar<11>(); STAMP(3);
    issue(2, 2);
    int slot = 0;
#pragma unroll 1
    for (int s = 1; s < 8; ++s) { wait_vm_bar<11>(); issue(s + 2, slot); slot = slot == 2 ? 0 : slot + 1; }
    wait_vm_bar<11>(); wait_vm_bar<0>();
    unsigned cs_; CODE_TOUCH(k_down, k_end, (wid & 1), cs_); CODE_DONE(cs_);
    PROF_END(3);
    return;
  }
  lds_cptr L = (lds_cptr)smem;
  LDS_AS float* sst = (LDS_AS float*)((lds_ptr)smem + 3 * G_STAGE);
  LDS_AS float* scs = sst + 256;
  if (tid < 128) {
    const f32x4* sp = (const f32x4*)(ws + OFF_STAT + (rt * 128 + tid) * 64);
    const f32x4 v = (sp[0] + sp[1]) + (sp[2] + sp[3]);
    const float mean = (v[0] + v[2]) * (1.0f / DM);
    *(LDS_AS f32x2*)(sst + tid * 2) = f32x2{-mean, fmaxf((v[1] + v[3]) * (1.0f / DM) - mean * mean, 0.f) + LN_EPS};
  }
  if (tid < 224) {
    const float* cp = (const float*)(ws + OFF_CSUM) + slot_nf(tid >> 4) * 16 + (tid & 15);
    float c[20];
#pragma unroll
    for (int k = 0; k < 20; ++k) c[k] = cp[k * 3456];
#pragma unroll
    for (int k = 0; k < 10; ++k) c[k] += c[k + 10];
    scs[tid] = ((c[0] + c[1]) + (c[2] + c[3])) + ((c[4] + c[5]) + (c[6] + c[7])) + (c[8] + c[9]);
  }
  const int wr = wid >> 1, wc = wid & 1, t16 = lane & 15, g = lane >> 4;
  const int aO = wr * 8192 + lane * 16, bO = (16 + 14 * wc) * 1024 + lane * 16;
  f32x4 acc[4][7];
  bf16x8 a0[4], b0[7], a1[4], b1[7];
  auto rd = [&](bf16x8 (&a)[4], bf16x8 (&b)[7], int so) {
    if (GEMM_OFF) return;
    a[0] = lds_b128(L + so + aO);
#pragma unroll
    for (int v = 0; v < 7; ++v) b[v] = lds_b128(L + so + bO + v * 2048);
#pragma unroll
    for (int u = 1; u < 4; ++u) a[u] = lds_b128(L + so + aO + u * 2048);
  };
  auto mm = [&](bf16x8 (&a)[4], bf16x8 (&b)[7]) {
    if (GEMM_OFF) return;
#pragma unroll
    for (int u = 0; u < 4; ++u)
#pragma unroll
      for (int v = 0; v < 7; ++v) acc[u][v] = mfma16(b[v], a[u], acc[u][v]);
  };
  auto ilv = [&] {
#pragma unroll
    for (int i = 0; i < 11; ++i) { SGB(0x8, 2); SGB(0x100, 1); }
    SGB(0x8, 6);
    SBAR();
  };
  wait_lgkm_bar();
  STAMP(1);
  rd(a0, b0, 0);
  float vk[4];
  {
    f32x4 c4[7];
#pragma unroll
    for (int v = 0; v < 7; ++v) c4[v] = *(const LDS_AS f32x4*)(scs + (7 * wc + v) * 16 + 4 * g);
#pragma unroll
    for (int u = 0; u < 4; ++u) {
      const f32x2 st = *(const LDS_AS f32x2*)(sst + (64 * wr + 16 * u + t16) * 2);
      vk[u] = st[1];
#pragma unroll
      for (int v = 0; v < 7; ++v) acc[u][v] = c4[v] * st[0];
    }
  }
  SBAR();
  int so = 0;
#pragma unroll 1
  for (int kt = 0; kt < 9; ++kt) {
    rd(a1, b1, so + 1024); mm(a0, b0); ilv();
    so = so == 2 * G_STAGE ? 0 : so + G_STAGE;
    wait_lgkm_bar();
    rd(a0, b0, so); mm(a1, b1); ilv();
  }
  rd(a1, b1, so + 1024); mm(a0, b0); ilv();
  mm(a1, b1);
  SBAR();
  STAMP(2);
  auto swi = [&](f32x4 x0, f32x4 x1, float c1, float k) {
    float a[4];
#pragma unroll
    for (int i = 0; i < 4; ++i) {
      const auto rr = __builtin_amdgcn_permlane32_swap(__float_as_uint(x0[i]), __float_as_uint(x1[i]), false, false);
      const float gt = __uint_as_float(rr[0]), up = __uint_as_float(rr[1]);
      a[i] = gt * up * __builtin_amdgcn_rcpf(__builtin_amdgcn_exp2f(gt * c1) * k + k);
    }
    return u32x2{pk2(a[0], a[1]), pk2(a[2], a[3])};
  };
  float c1[4];
#pragma unroll
  for (int u = 0; u < 4; ++u) c1[u] = -LOG2E * __builtin_amdgcn_rsqf(vk[u]);
  char* ab = ws + OFF_ACT + ((rt * 8 + 4 * wr) * 54 + UB) * 1024;
#pragma unroll
  for (int u = 0; u < 4; ++u) {
    const u32x2 pa = swi(acc[u][0], acc[u][1], c1[u], vk[u]), pb = swi(acc[u][2], acc[u][3], c1[u], vk[u]);
    *(u32x4*)(ab + (u * 54 + 2 * wc) * 1024 + lane * 16u) = u32x4{pa[0], pa[1], pb[0], pb[1]};
    *(u32x2*)(ab + (u * 54 + 1) * 1024 + wc * 8 + lane * 16u) = swi(acc[u][4], acc[u][5], c1[u], vk[u]);
  }
  if (c8 < 6) {
    char* al = ws + OFF_ACT + ((rt * 8 + 4 * wr) * 54 + UL) * 1024 + wc * 512 + (c8 & 1) * 8;
    const unsigned lo = (g >> 1) * 54 * 1024 + (g & 1) * 256 + t16 * 16;
#pragma unroll
    for (int u = 0; u < 4; u += 2)
      *(u32x2*)(al + u * 54 * 1024 + lo) = swi(acc[u][6], acc[u + 1][6], lane < 32 ? c1[u] : c1[u + 1], lane < 32 ? vk[u] : vk[u + 1]);
  }
  PROF_END(3);
}

extern "C" __global__ __launch_bounds__(512, 2) void k_down(char* __restrict__ ws, char* __restrict__ out) {
  __shared__ __attribute__((aligned(16))) char smem[S_SLOTS * S_STAGE];
  small_gemm<27, 1>(smem, nullptr, ws, out);
}
extern "C" __global__ void k_end() {}

__global__ void k_report(char* ws) {
  __shared__ unsigned long long smin[5], smax[5];
  const int b = threadIdx.x;
  if (b < 5) { smin[b] = ~0ull; smax[b] = 0; }
  __syncthreads();
  const unsigned long long* pr = (const unsigned long long*)(ws + (128ull << 20));
  for (int k = 0; k < NKERN; ++k) {
    if (k == 0 && ((b & 1) * 8 + (b >> 5)) >= 15) continue;
    const unsigned long long* t = pr + ((k * 256 + b) * 12) * 8;
    atomicMin(&smin[k], t[6]); atomicMax(&smax[k], t[7]);
  }
  __syncthreads();
  long long x = 0;
  if (MEAS >= 1 && MEAS <= 9) x = (MEAS & 1) ? (long long)(smax[MEAS >> 1] - smin[MEAS >> 1]) : (long long)(smin[MEAS >> 1] - smax[(MEAS >> 1) - 1]);
  if (MEAS == 10) x = (long long)(smax[NKERN - 1] - smin[0]);
  if (MEAS >= 20) {
    const int k = (MEAS / 100) % 10, w = (MEAS / 1000), i = MEAS % 10;
    const unsigned long long* t = pr + ((k * 256 + 8) * 12 + w) * 8;
    x = (long long)(t[i] - t[0]) / 20;
  }
  if (x < 0) x = 0; if (x > 10000) x = 10000;
  unsigned long long t0, t1;
  asm volatile("s_memrealtime %0\n\ts_waitcnt lgkmcnt(0)" : "=s"(t0) :: "memory");
  do { asm volatile("s_memrealtime %0\n\ts_waitcnt lgkmcnt(0)" : "=s"(t1) :: "memory"); } while ((long long)(t1 - t0) < FACT * x);
}
extern "C" void kernel_launch(void* const* d_in, const int* in_sizes, int n_in, void* d_out, int out_size, void* d_ws, size_t ws_size,
                              hipStream_t stream) {
  const char* x = (const char*)d_in[0]; const char* wqkv = (const char*)d_in[1]; const char* wo = (const char*)d_in[2];
  const char* wg = (const char*)d_in[4]; const char* wu = (const char*)d_in[5]; const char* wd = (const char*)d_in[6];
  char* ws = (char*)d_ws;
  k_qkv<<<256, 512, 0, stream>>>(x, wqkv, ws);
  k_attn<<<256, 768, 0, stream>>>(wo, wg, wu, wd, ws);
  k_wo<<<256, 512, 0, stream>>>(x, ws);
  k_gateup<<<256, 512, 0, stream>>>(ws);
  k_down<<<256, 512, 0, stream>>>(ws, (char*)d_out);
  k_report<<<1, 256, 0, stream>>>(ws);
}
```

```cpp
#include <hip/hip_runtime.h>
#include <hip/hip_bf16.h>
#include <stdint.h>

typedef __attribute__((ext_vector_type(8))) short bf16x8;
typedef __attribute__((ext_vector_type(4))) short bf16x4;
typedef __attribute__((ext_vector_type(2))) float f32x2;
typedef __attribute__((ext_vector_type(4))) float f32x4;
typedef __attribute__((ext_vector_type(16))) float f32x16;
typedef __attribute__((ext_vector_type(4))) unsigned u32x4;
typedef __attribute__((ext_vector_type(2))) unsigned u32x2;
typedef __attribute__((ext_vector_type(2))) __bf16 bf2;
typedef __attribute__((address_space(3))) const char* lds_cptr;
typedef __attribute__((address_space(3))) char* lds_ptr;
#define LDS_AS __attribute__((address_space(3)))
#define DI __device__ __forceinline__

constexpr int SEQ = 2048, DM = 640, NH = 32, DH = 20, FF = 1728;
constexpr float LN_EPS = 1e-5f;
constexpr float LOG2E = 1.4426950408889634f;
constexpr float QSCALE = 0.22360679774997896f * LOG2E;

constexpr int Q_TILE = 1280;
constexpr int Q_HEAD = 64 * Q_TILE;
constexpr int KV_TILE = 2688;
constexpr int KV_HEAD = 64 * KV_TILE;
constexpr unsigned OFF_Q    = 0;
constexpr unsigned OFF_KV   = OFF_Q + NH * Q_HEAD;
constexpr unsigned OFF_OF   = OFF_KV + NH * KV_HEAD;
constexpr unsigned OFF_HF   = OFF_OF + SEQ * DM * 2;
constexpr unsigned OFF_ACT  = OFF_HF + SEQ * DM * 2;
constexpr unsigned OFF_WOF  = OFF_ACT + SEQ * FF * 2;
constexpr unsigned OFF_WGUF = OFF_WOF + DM * DM * 2;
constexpr unsigned OFF_WDF  = OFF_WGUF + DM * FF * 4;
constexpr unsigned OFF_STAT = OFF_WDF + FF * DM * 2;
constexpr unsigned OFF_CSUM = OFF_STAT + SEQ * 64;
constexpr unsigned OFF_FLAG = OFF_CSUM + 20 * 216 * 64;
constexpr unsigned FL_O = OFF_FLAG, FL_H = FL_O + 64 * 256, FL_ACT = FL_H + 128 * 256, FL_W = FL_ACT + 128 * 256;
constexpr int N_FLAGS = 384;
constexpr unsigned WS_BYTES = OFF_FLAG + N_FLAGS * 256;

#define PROF_DECL
#define STAMP(i)
#define PROF_END(k)
#define PROF_ARG
#define PROF_PASS
#define STXN 0
#define STX(n) do { if (n == STXN) STAMP(12); } while (0)
template <int N> DI void glds(unsigned voff, const void* sbase, unsigned lds, int imm) {
#define GL(o) "\n\tglobal_load_lds_dwordx4 %0, %1 offset:" #o
  if (N == 1) asm volatile("s_add_u32 m0, %2, %3\n\ts_nop 0" GL(0) :: "v"(voff), "s"(sbase), "s"(lds), "ns"(imm) : "memory", "scc");
  if (N == 2) asm volatile("s_add_u32 m0, %2, %3\n\ts_nop 0" GL(0) GL(1024) :: "v"(voff), "s"(sbase), "s"(lds), "ns"(imm) : "memory", "scc");
  if (N == 4) asm volatile("s_add_u32 m0, %2, %3\n\ts_nop 0" GL(0) GL(1024) GL(2048) GL(3072) :: "v"(voff), "s"(sbase), "s"(lds), "ns"(imm) : "memory", "scc");
#undef GL
}
#define CODE_TOUCH(first, last, part, sink) do { unsigned long long pc_; int a_, b_; \
  asm volatile("s_getpc_b64 %0\n.Lct%=:\n\ts_mov_b32 %1, " #first "-.Lct%=\n\ts_mov_b32 %2, " #last "-.Lct%=" : "=s"(pc_), "=s"(a_), "=s"(b_)); \
  const int o_ = min(max(a_, 0) + lane * 128 + (part) * 8192, max(b_, 0)); \
  asm volatile("global_load_dword %0, %1, %2" : "=v"(sink) : "v"(o_), "s"(pc_) : "memory"); } while (0)
#define CODE_DONE(sink) asm volatile("s_waitcnt vmcnt(0)" :: "v"(sink) : "memory")
typedef __attribute__((address_space(1))) unsigned gu32;
typedef __amdgpu_buffer_rsrc_t rsrc_t;
#define LDAUX 16
#define XK(k) (k)
DI u32x4 ld16c(rsrc_t rs, unsigned vo, unsigned so) { return __builtin_amdgcn_raw_buffer_load_b128(rs, vo, so, LDAUX); }
DI u32x2 ld8c(rsrc_t rs, unsigned vo, unsigned so) { return __builtin_amdgcn_raw_buffer_load_b64(rs, vo, so, 16); }
DI void st16c(rsrc_t rs, unsigned vo, unsigned so, u32x4 v) { __builtin_amdgcn_raw_buffer_store_b128(v, rs, vo + so, 0, 16); }
DI void st8c(rsrc_t rs, unsigned vo, unsigned so, u32x2 v) { __builtin_amdgcn_raw_buffer_store_b64(v, rs, vo, so, 16); }
DI void arrive(char* ws, unsigned off, int lane, int stride) {
  if (lane < 8) __hip_atomic_fetch_add((gu32*)(ws + off + lane * stride), 1u, __ATOMIC_RELAXED, __HIP_MEMORY_SCOPE_AGENT);
}
DI void await8(char* ws, unsigned off, int lane, unsigned want) {
#pragma unroll 1
  for (int n = 0; n < (1 << 16); ++n) {
    const unsigned v = __hip_atomic_load((gu32*)(ws + off + (lane & 7) * 256), __ATOMIC_RELAXED, __HIP_MEMORY_SCOPE_AGENT);
    if (__all(v >= want)) break;
    __builtin_amdgcn_s_sleep(1);
  }
}
DI void await(char* ws, unsigned off, unsigned want) {
#pragma unroll 1
  for (int n = 0; n < (1 << 16); ++n) {
    const unsigned v = __hip_atomic_load((gu32*)(ws + off), __ATOMIC_RELAXED, __HIP_MEMORY_SCOPE_AGENT);
    if ((unsigned)__builtin_amdgcn_readfirstlane(v) >= want) break;
    __builtin_amdgcn_s_sleep(1);
  }
  __builtin_amdgcn_fence(__ATOMIC_ACQUIRE, "wavefront");
}
template <int N> DI void wait_vm_bar() { asm volatile("s_waitcnt vmcnt(%0)\n\ts_barrier" :: "n"(N) : "memory"); }
template <int N> DI void wait_vm() { asm volatile("s_waitcnt vmcnt(%0)" :: "n"(N) : "memory"); }
#define SBAR() __builtin_amdgcn_sched_barrier(0)
#define SGB(mask, n) __builtin_amdgcn_sched_group_barrier(mask, n, 0)
DI void wait_lgkm_bar() { SBAR(); __builtin_amdgcn_s_waitcnt(0xC07F); asm volatile("s_barrier" ::: "memory"); SBAR(); }
#define GEMM_OFF 0
template <int I> struct IC { static constexpr int value = I; };
template <int B, int E, class F> DI void static_for(F&& f) { if constexpr (B < E) { f(IC<B>{}); static_for<B + 1, E>(f); } }
DI bf16x8 lds_b128(lds_cptr p) { return *(const LDS_AS bf16x8*)p; }
DI bf16x4 lds_b64(lds_cptr p) { return *(const LDS_AS bf16x4*)p; }
DI bf16x4 lds_tr(lds_cptr p) { return __builtin_amdgcn_ds_read_tr16_b64_v4i16((LDS_AS bf16x4*)p); }
DI f32x4 mfma16(bf16x8 a, bf16x8 b, f32x4 c) { return __builtin_amdgcn_mfma_f32_16x16x32_bf16(a, b, c, 0, 0, 0); }
DI void mfma16a(f32x4& c, bf16x8 a, u32x4 b) { asm volatile("v_mfma_f32_16x16x32_bf16 %0, %1, %2, %0" : "+v"(c) : "v"(a), "v"(b)); }
DI void mfma16z(f32x4& c, bf16x8 a, u32x4 b) { asm volatile("v_mfma_f32_16x16x32_bf16 %0, %1, %2, 0" : "=&v"(c) : "v"(a), "v"(b)); }
DI f32x16 mfma32(bf16x8 a, bf16x8 b, f32x16 c) { return __builtin_amdgcn_mfma_f32_32x32x16_bf16(a, b, c, 0, 0, 0); }
DI f32x16 mfma32k8(bf16x4 a, bf16x4 b, f32x16 c) { return __builtin_amdgcn_mfma_f32_32x32x8bf16_1k(a, b, c, 0, 0, 0); }
DI unsigned pk2(float lo, float hi) { f32x2 v = {lo, hi}; return __builtin_bit_cast(unsigned, __builtin_convertvector(v, bf2)); }
DI float bflo(unsigned u) { return __uint_as_float(u << 16); }
DI float bfhi(unsigned u) { return __uint_as_float(u & 0xffff0000u); }
DI int div20(int c) { return (c * 3277) >> 16; }
DI float xsum32(float v) { auto r = __builtin_amdgcn_permlane32_swap(__float_as_uint(v), __float_as_uint(v), false, false); return __uint_as_float(r[0]) + __uint_as_float(r[1]); }
DI float xsum16(float v) { auto r = __builtin_amdgcn_permlane16_swap(__float_as_uint(v), __float_as_uint(v), false, false); return __uint_as_float(r[0]) + __uint_as_float(r[1]); }
constexpr bf16x8 ONES8 = {0x3f80, 0x3f80, 0x3f80, 0x3f80, 0x3f80, 0x3f80, 0x3f80, 0x3f80};

template <int MODE>
DI void qkv_compute(lds_cptr L, lds_ptr scr, int wid, int lane, int row0, int col0, char* __restrict__ ws PROF_ARG) {
  const int wr = wid >> 1, wc = wid & 1, r16 = lane & 15, g = lane >> 4, q = r16 >> 2, p = lane & 3;
  const int f = q | ((g & 1) << 2);
  const int rp = MODE == 2 ? 16 * (r16 >> 3) + (r16 & 7) : r16;
  constexpr int RSTEP = MODE == 2 ? 8 : 16;
  int aoff[2][2], boff[4];
#pragma unroll
  for (int e = 0; e < 2; ++e)
#pragma unroll
    for (int ks = 0; ks < 2; ++ks) { const int r = rp + e * RSTEP; aoff[e][ks] = (64 * wr + r) * 128 + (((4 * ks + g) ^ ((r >> 1) & 7) ^ ((r >> 2) & 4)) << 4); }
#pragma unroll
  for (int jj = 0; jj < 4; ++jj) { const int j = (jj + 2 * wr) & 3; boff[jj] = 16384 + (8 * g + q) * 256 + ((((4 * wc + j) ^ f) * 2 + (p >> 1)) << 4) + (p & 1) * 8; }
  const int pr0 = wc * 4096, pr1 = 4096 - pr0;

  f32x4 acc[4][4], rs[2], gr[2], cs[2];
#pragma unroll
  for (int i = 0; i < 4; ++i)
#pragma unroll
    for (int j = 0; j < 4; ++j) acc[i][j] = f32x4{0.f, 0.f, 0.f, 0.f};
#pragma unroll
  for (int i = 0; i < 2; ++i) { rs[i] = f32x4{0.f, 0.f, 0.f, 0.f}; gr[i] = rs[i]; cs[i] = rs[i]; }
  bf16x8 a0[4], b0[4], a1[4], b1[4];

  auto rd = [&](bf16x8 (&a)[4], bf16x8 (&b)[4], int slot, int ks) {
    if (GEMM_OFF && slot + ks > 0) return;
    lds_cptr S = L + slot;
#pragma unroll
    for (int jj = 0; jj < 4; ++jj) {
      bf16x4 lo = lds_tr(S + boff[jj] + ks * 8192), hi = lds_tr(S + boff[jj] + ks * 8192 + 1024);
      b[jj] = __builtin_shufflevector(lo, hi, 0, 1, 2, 3, 4, 5, 6, 7);
      a[jj] = lds_b128(S + aoff[jj & 1][ks] + (jj < 2 ? pr0 : pr1));
    }
  };
  auto mm = [&](bf16x8 (&a)[4], bf16x8 (&b)[4]) {
    if (GEMM_OFF) return;
#pragma unroll
    for (int ii = 0; ii < 4; ++ii)
#pragma unroll
      for (int jj = 0; jj < 4; ++jj)
        acc[ii][jj] = (MODE == 2) ? mfma16(a[ii], b[jj], acc[ii][jj]) : mfma16(b[jj], a[ii], acc[ii][jj]);
#pragma unroll
    for (int u = 0; u < 2; ++u) {
      rs[u] = mfma16(a[u], ONES8, rs[u]);
      gr[u] = mfma16(a[u], a[u], gr[u]);
      cs[u] = mfma16(ONES8, b[u], cs[u]);
    }
  };
  auto ilv = [&] {
#pragma unroll
    for (int i = 0; i < 12; ++i) { SGB(0x8, 1); SGB(0x100, 1); }
    SGB(0x8, 10);
    SBAR();
  };

  wait_lgkm_bar();
  STAMP(1);
  rd(a0, b0, 0, 0);
  SBAR();
#pragma unroll 1
  for (int kt = 0; kt < 9; ++kt) {
    rd(a1, b1, (kt & 3) * 32768, 1); mm(a0, b0); ilv();
    wait_lgkm_bar();
    rd(a0, b0, ((kt + 1) & 3) * 32768, 0); mm(a1, b1); ilv();
  }
  rd(a1, b1, 32768, 1); mm(a0, b0); ilv();
  mm(a1, b1);
  SBAR();
  STAMP(2);
  LDS_AS float* rsum = (LDS_AS float*)scr; LDS_AS float* rsq = rsum + 128; LDS_AS float* csum = rsum + 256;
#pragma unroll
  for (int u = 0; u < 2; ++u) {
    const int rb = 64 * wr + 32 * wc + 16 * u;
    if (r16 == 0) *(LDS_AS f32x4*)(rsum + rb + 4 * g) = rs[u];
    if (q == g) { const float d = p == 0 ? gr[u][0] : p == 1 ? gr[u][1] : p == 2 ? gr[u][2] : gr[u][3]; rsq[rb + r16] = d; }
    if (g == 0) csum[64 * wc + 32 * wr + 16 * u + r16] = cs[u][0];
  }
  constexpr float invD = 1.0f / DM;
  if (MODE != 2) {
    constexpr int TS = MODE == 0 ? Q_TILE : KV_TILE, HS = MODE == 0 ? Q_HEAD : KV_HEAD;
    unsigned off[4][2];
#pragma unroll
    for (int jj = 0; jj < 4; ++jj) {
      const int ck = col0 - MODE * DM + 64 * wc + 16 * ((jj + 2 * wr) & 3) + 4 * g, head = div20(ck), d = ck - head * DH;
      off[jj][0] = head * HS + (d < 16 ? (d >> 3) * 512 + (d & 7) * 2 + r16 * 16 : 1024 + r16 * 8);
      off[jj][1] = off[jj][0] + (d < 16 ? 256 : 128);
    }
    char* base = ws + (MODE == 0 ? OFF_Q : OFF_KV) + ((row0 >> 5) + 2 * wr) * TS;
    wait_lgkm_bar();
    f32x4 c4[4];
#pragma unroll
    for (int jj = 0; jj < 4; ++jj) c4[jj] = *(const LDS_AS f32x4*)(csum + 64 * wc + 16 * ((jj + 2 * wr) & 3) + 4 * g);
#pragma unroll
    for (int ii = 0; ii < 4; ++ii) {
      const int half = ((ii >> 1) + wc) & 1, rl = 64 * wr + 32 * half + 16 * (ii & 1) + r16;
      const float mean = rsum[rl] * invD, var = fmaxf(rsq[rl] * invD - mean * mean, 0.f);
      const float rstd = __builtin_amdgcn_rsqf(var + LN_EPS) * (MODE == 0 ? QSCALE : 1.0f), nb = -mean * rstd;
      char* tb = base + half * TS;
#pragma unroll
      for (int jj = 0; jj < 4; ++jj) {
        const f32x4 v = acc[ii][jj] * rstd + c4[jj] * nb;
        *(u32x2*)(tb + off[jj][ii & 1]) = u32x2{pk2(v[0], v[1]), pk2(v[2], v[3])};
      }
    }
  } else {
    unsigned off[4];
#pragma unroll
    for (int jj = 0; jj < 4; ++jj) {
      const int cv = col0 - 2 * DM + 64 * wc + 16 * ((jj + 2 * wr) & 3) + r16, head = div20(cv), d = cv - head * DH;
      off[jj] = head * KV_HEAD + 1280 + g * 352 + d * 16;
    }
    char* base = ws + OFF_KV + ((row0 >> 5) + 2 * wr) * KV_TILE;
    { const int head = div20(col0 - 2 * DM + 19) + (lane >> 3), t = lane & 7;
      if (head * DH < col0 - 2 * DM + 128) *(u32x4*)(base + head * KV_HEAD + (t >> 2) * KV_TILE + 1280 + (t & 3) * 352 + 320 + wc * 16) = wc ? u32x4{0u, 0u, 0u, 0u} : __builtin_bit_cast(u32x4, ONES8); }
    wait_lgkm_bar();
    float c1[4];
#pragma unroll
    for (int jj = 0; jj < 4; ++jj) c1[jj] = csum[64 * wc + 16 * ((jj + 2 * wr) & 3) + r16];
#pragma unroll
    for (int ip = 0; ip < 2; ++ip) {
      const int half = (ip + wc) & 1;
      f32x4 rstd[2], nb[2];
#pragma unroll
      for (int e = 0; e < 2; ++e) {
        const int rl = 64 * wr + 32 * half + 16 * e + 4 * g;
        const f32x4 mean = *(const LDS_AS f32x4*)(rsum + rl) * invD, q4 = *(const LDS_AS f32x4*)(rsq + rl);
#pragma unroll
        for (int i = 0; i < 4; ++i) rstd[e][i] = __builtin_amdgcn_rsqf(fmaxf(q4[i] * invD - mean[i] * mean[i], 0.f) + LN_EPS);
        nb[e] = -mean * rstd[e];
      }
      char* tb = base + half * KV_TILE;
#pragma unroll
      for (int jj = 0; jj < 4; ++jj) {
        const f32x4 v0 = acc[2 * ip][jj] * rstd[0] + nb[0] * c1[jj], v1 = acc[2 * ip + 1][jj] * rstd[1] + nb[1] * c1[jj];
        *(u32x4*)(tb + off[jj]) = u32x4{pk2(v0[0], v0[1]), pk2(v0[2], v0[3]), pk2(v1[0], v1[1]), pk2(v1[2], v1[3])};
      }
    }
  }
}

extern "C" __global__ __launch_bounds__(512, 2) void k_qkv(const char* __restrict__ x, const char* __restrict__ w, char* __restrict__ ws) {
  __shared__ __attribute__((aligned(16))) char smem[4 * 32768 + 2048];
  const int bid = blockIdx.x, xcd = bid & 7, idx = bid >> 3;
  const int rt = (xcd >> 1) * 4 + (idx & 3), ct = (xcd & 1) * 8 + (idx >> 2);
  if (ct >= 15) {
    if (rt == 0 && threadIdx.x < N_FLAGS) *(unsigned*)(ws + OFF_FLAG + threadIdx.x * 256) = 0u;
    return;
  }
  PROF_DECL STAMP(0);
  const int tid = threadIdx.x, lane = tid & 63, wid = __builtin_amdgcn_readfirstlane(tid >> 6);
  if (wid >= 4) {
    const int lw = wid - 4;
    const unsigned lds0 = (unsigned)(uintptr_t)smem + lw * 1024;
    const int ra = 8 * lw + (lane >> 3), rb = 4 * lw + (lane >> 4), f = (rb & 3) | ((rb >> 1) & 4);
    const unsigned voA = ra * 1280 + (((lane & 7) ^ ((ra >> 1) & 7) ^ ((ra >> 2) & 4)) << 4), voB = rb * 3840 + (((lane & 15) ^ (f << 1)) << 4);
    const char* pa = x + (size_t)rt * 128 * 1280; const char* pb = w + ct * 256;
    auto issue = [&](int st) {
      const unsigned base = lds0 + (st & 3) * 32768;
#pragma unroll
      for (int j = 0; j < 4; ++j) { glds<1>(voA + j * 32 * 1280, pa + st * 128, base, j * 4096); glds<1>(voB + j * 16 * 3840, pb + st * 64 * 3840, base, 16384 + j * 4096); }
    };
    STAMP(1); issue(0); issue(1); STAMP(2);
    wait_vm_bar<8>(); STAMP(3);
    issue(2); issue(3);
#pragma unroll 1
    for (int s = 1; s < 7; ++s) { wait_vm_bar<16>(); issue(s + 3); }
    wait_vm_bar<16>(); wait_vm_bar<8>(); wait_vm_bar<0>();
    PROF_END(0);
    return;
  }
  lds_cptr L = (lds_cptr)smem; lds_ptr scr = (lds_ptr)smem + 4 * 32768;
  unsigned cs0, cs1; CODE_TOUCH(k_qkv, k_end, wid, cs0); CODE_TOUCH(k_qkv, k_end, wid + 4, cs1);
  if (ct < 5) qkv_compute<0>(L, scr, wid, lane, rt * 128, ct * 128, ws PROF_PASS);
  else if (ct < 10) qkv_compute<1>(L, scr, wid, lane, rt * 128, ct * 128, ws PROF_PASS);
  else qkv_compute<2>(L, scr, wid, lane, rt * 128, ct * 128, ws PROF_PASS);
  CODE_DONE(cs0); CODE_DONE(cs1);
  PROF_END(0);
}

constexpr int L_STG = 48 * KV_TILE;
constexpr int L_WO = 4 * KV_TILE;
constexpr int G_STAGE = 28 * 1024;
constexpr int L_RED = 5 * G_STAGE;
constexpr int D_STAGE = 10 * 1024;
constexpr int L_CS = 15 * D_STAGE;
constexpr int L_CNT = 163840 - 16;

DI float rowmax16(const f32x16& s) {
  float a = fmaxf(fmaxf(s[0], s[1]), s[2]), b = fmaxf(fmaxf(s[3], s[4]), s[5]);
  a = fmaxf(fmaxf(a, s[6]), s[7]); b = fmaxf(fmaxf(b, s[8]), s[9]);
  a = fmaxf(fmaxf(a, s[10]), s[11]); b = fmaxf(fmaxf(b, s[12]), s[13]);
  a = fmaxf(fmaxf(a, s[14]), s[15]);
  const float m = fmaxf(a, b);
  auto rr = __builtin_amdgcn_permlane32_swap(__float_as_uint(m), __float_as_uint(m), false, false);
  return fmaxf(__uint_as_float(rr[0]), __uint_as_float(rr[1]));
}
DI unsigned exp_pk(float a, float b) { return pk2(__builtin_amdgcn_exp2f(a), __builtin_amdgcn_exp2f(b)); }
DI int lane_id() { int v; asm volatile("v_mbcnt_lo_u32_b32 %0, -1, 0\n\tv_mbcnt_hi_u32_b32 %0, -1, %0" : "=v"(v)); __builtin_assume(v >= 0 && v < 64); return v; }
DI bf16x8 as_frag(u32x4 v) { return __builtin_bit_cast(bf16x8, v); }

template <int KIND>
DI void retile_issue(const char* __restrict__ w0, const char* __restrict__ w1, int tile, int ks, int lane, unsigned stg) {
  if (KIND == 0) {
    const unsigned vo = (ks * 32 + (lane >> 3)) * (FF * 2) + tile * 128 + (lane & 7) * 16;
#pragma unroll
    for (int j = 0; j < 4; ++j) { glds<1>(vo + j * 8 * (FF * 2), w0, stg, j * 1024); glds<1>(vo + j * 8 * (FF * 2), w1, stg, 4096 + j * 1024); }
  } else {
#pragma unroll
    for (int j = 0; j < 8; ++j) {
      int k = ks * 32 + 4 * j + (lane >> 4);
      if (KIND == 2) { const int pp = div20(k) >> 1, e = k - 40 * pp; k = 40 * pp + (e < 16 ? e : e < 32 ? e + 4 : e < 36 ? e - 16 : e); }
      glds<1>(k * (DM * 2) + tile * 256 + (lane & 15) * 16, w0, stg, j * 1024);
    }
  }
}
template <int KIND>
DI void retile_store(rsrc_t rs, lds_cptr S, int tile, int ks, int lane) {
  const int g = lane >> 4, q = (lane & 15) >> 2, p = lane & 3;
#pragma unroll
  for (int f = 0; f < 8; ++f) {
    lds_cptr a; int hi; unsigned dst;
    if (KIND == 0) {
      const int U = 2 * tile + (f >> 2), j = (f >> 1) & 1;
      a = S + (f & 1) * 4096 + (8 * g + q) * 128 + (32 * (f >> 2) + ((U >= 27 ? U - 27 : U) >= 24 ? 16 * j + 4 * p : 8 * p + 4 * j)) * 2; hi = 512;
      dst = OFF_WGUF + ((8 * tile + f) * 20 + ks) * 1024;
    } else {
      const int U = 4 * tile + (f >> 1), j = f & 1;
      a = S + (8 * g + q) * 256 + (32 * (f >> 1) + (U % 5 == 2 ? 16 * j + 4 * p : 8 * p + 4 * j)) * 2; hi = 1024;
      const int n10 = (2 * U + j) % 10, nf = 2 * U + j + (n10 == 5 ? 4 : n10 > 5 ? -1 : 0);
      dst = (KIND == 1 ? OFF_WDF : OFF_WOF) + (nf * (KIND == 1 ? 54 : 20) + ks) * 1024;
    }
    const bf16x8 fr = __builtin_shufflevector(lds_tr(a), lds_tr(a + hi), 0, 1, 2, 3, 4, 5, 6, 7);
    st16c(rs, lane * 16, dst, __builtin_bit_cast(u32x4, fr));
    if (KIND == 0) {
      const f32x4 c = mfma16(fr, ONES8, f32x4{0.f, 0.f, 0.f, 0.f});
      if ((lane & 15) == 0) st16c(rs, g * 16, OFF_CSUM + (ks * 216 + 8 * tile + f) * 64, __builtin_bit_cast(u32x4, c));
    }
  }
}

struct Roles {
  int head, qt, rt, ct, rg, cg, UB, UL;
  DI Roles(int bid) {
    const int xcd = bid & 7, idx = bid >> 3;
    head = xcd * 4 + (idx >> 3); qt = idx & 7;
    rt = (xcd >> 1) * 8 + (idx & 7); ct = (xcd & 1) * 4 + (idx >> 3);
    rg = (xcd >> 2) * 8 + (idx & 7); cg = (xcd & 3) * 4 + (idx >> 3);
    UB = (cg >> 3) * 27 + 3 * (cg & 7); UL = (cg >> 3) * 27 + 24 + min((cg & 7) >> 1, 2);
  }
  DI int gu_nf(int f) const { return f < 12 ? 4 * UB + f : 4 * UL + 2 * (cg & 1) + f - 12; }
};

DI void helper_waves(char* smem, const char* __restrict__ wo, const char* __restrict__ wg, const char* __restrict__ wu, const char* __restrict__ wd,
                     char* __restrict__ ws, rsrc_t rs, const Roles& R, int hw, int lane PROF_ARG) {
  const int bid = blockIdx.x;
  const unsigned lds0 = (unsigned)(uintptr_t)smem, vo = lane * 16;
  LDS_AS unsigned* cnt = (LDS_AS unsigned*)((lds_ptr)smem + L_CNT);
  const char* kv = ws + OFF_KV + R.head * KV_HEAD;
  const unsigned vk = vo + hw * 4096, lk = lds0 + hw * 4096;
  auto runs = [&](int i0, int i1) {
#pragma unroll
    for (int i = i0; i < i1; ++i) glds<4>(vk, kv + i * 16384, lk, i * 16384);
  };
  runs(0, 3);
  if (hw == 0) { cnt[0] = 0u; cnt[1] = 0u; }
  STAMP(1);
  asm volatile("s_waitcnt vmcnt(8) lgkmcnt(0)\n\ts_barrier" ::: "memory");
  runs(3, 6);
  const int job = bid * 4 + hw;
  const unsigned stg = lds0 + L_STG + hw * 8192;
  int tile = 0, ks = 0;
  if (job < 540)      { ks = job / 27; tile = job - ks * 27; retile_issue<0>(wg, wu, tile, ks, lane, stg); }
  else if (job < 810) { ks = (job - 540) / 5; tile = job - 540 - ks * 5; retile_issue<1>(wd, wd, tile, ks, lane, stg); }
  else                { ks = min(job - 810, 99) / 5; tile = min(job - 810, 99) - ks * 5; retile_issue<2>(wo, wo, tile, ks, lane, stg); }
  wait_vm_bar<20>();
  runs(6, 7);
  if (hw < 3) glds<4>(vk, kv + 7 * 16384, lk, 7 * 16384); else { glds<2>(vk, kv + 7 * 16384, lk, 7 * 16384); glds<2>(vk, kv + 7 * 16384, lk, 7 * 16384); }
  wait_vm_bar<16>();
  if (hw < 3) glds<4>(vk, kv + 60 * KV_TILE, lk, 0);
  if (hw < 3) wait_vm<4>(); else wait_vm<0>();
  STAMP(2);
  lds_cptr S = (lds_cptr)smem + L_STG + hw * 8192;
  if (job < 540) retile_store<0>(rs, S, tile, ks, lane);
  else if (job < 810) retile_store<1>(rs, S, tile, ks, lane);
  else if (job < 910) retile_store<2>(rs, S, tile, ks, lane);
  asm volatile("s_waitcnt lgkmcnt(0)" ::: "memory");
  glds<4>(vo + hw * 8192, kv + L_STG, stg, 0); glds<4>(vo + hw * 8192, kv + L_STG + 4096, stg, 4096);
  wait_vm<0>();
  STAMP(3);
  unsigned old = 0;
  if (lane == 0) old = __hip_atomic_fetch_add(cnt, 1u, __ATOMIC_RELAXED, __HIP_MEMORY_SCOPE_WORKGROUP);
  if (__builtin_amdgcn_readfirstlane(old) == 3) arrive(ws, FL_W + (bid & 7) * 256, lane, 2048);
  asm volatile("s_barrier" ::: "memory");
  if (hw == 0) {
    await8(ws, FL_W + ((bid >> 3) & 7) * 2048, lane, 32);
    __builtin_amdgcn_fence(__ATOMIC_ACQUIRE, "agent");
    wait_vm<0>();
    if (lane == 0) __hip_atomic_fetch_add(cnt + 1, 1u, __ATOMIC_RELAXED, __HIP_MEMORY_SCOPE_WORKGROUP);
  }
  STAMP(4);
  while (__builtin_amdgcn_readfirstlane(__hip_atomic_load(cnt + 1, __ATOMIC_RELAXED, __HIP_MEMORY_SCOPE_WORKGROUP)) < 5) __builtin_amdgcn_s_sleep(4);
  unsigned tw[5];
  CODE_TOUCH(k_main, k_end, hw, tw[2]); CODE_TOUCH(k_main, k_end, hw + 4, tw[3]);
  {
    const char* src = ws + OFF_WOF + R.ct * 102400;
#pragma unroll
    for (int i = 0; i < 6; ++i) glds<4>(vk, src + i * 16384, lk, L_WO + i * 16384);
    glds<1>(vo + hw * 1024, src + 98304, lds0 + hw * 1024, L_WO + 98304);
  }
  if (hw == 0) {
    f32x4 csum;
    {
      const int t = min(lane, 55);
      const f32x4* cp = (const f32x4*)(ws + OFF_CSUM) + R.gu_nf(t >> 2) * 4 + (t & 3);
      f32x4 c[20];
#pragma unroll
      for (int k = 0; k < 20; ++k) c[k] = cp[k * 864];
#pragma unroll
      for (int k = 0; k < 10; ++k) c[k] += c[k + 10];
      csum = ((c[0] + c[1]) + (c[2] + c[3])) + ((c[4] + c[5]) + (c[6] + c[7])) + (c[8] + c[9]);
    }
    wait_vm_bar<0>();
    asm volatile("" :: "v"(tw[2]), "v"(tw[3]));
    STAMP(5);
    arrive(ws, FL_O + R.qt * 2048, lane, 256);
    await(ws, FL_O + (R.rt >> 2) * 2048 + R.ct * 256, 32);
    STAMP(6);
    asm volatile("s_barrier\n\ts_barrier\n\ts_barrier" ::: "memory");
    STAMP(7);
    arrive(ws, FL_H + (R.rt >> 1) * 2048, lane, 256);
    if (lane < 56) *(LDS_AS f32x4*)((lds_ptr)smem + L_CS + lane * 16) = csum;
    await(ws, FL_H + R.rg * 2048 + (R.cg & 7) * 256, 16);
    STAMP(8);
    asm volatile("s_waitcnt lgkmcnt(0)\n\ts_barrier" ::: "memory");
#pragma unroll 1
    for (int s = 0; s < 12; ++s) asm volatile("s_barrier" ::: "memory");
    STAMP(9);
    arrive(ws, FL_ACT + R.rg * 2048, lane, 256);
    await(ws, FL_ACT + (R.rt >> 1) * 2048 + R.ct * 256, 16);
    STAMP(10);
    asm volatile("s_barrier" ::: "memory");
    return;
  }
  const int l = hw - 1;
  unsigned vg[5], vd[5];
#pragma unroll
  for (int i = 0; i < 5; ++i) { vg[i] = vo + R.gu_nf(min(l + 3 * i, 13)) * 20480; vd[i] = vo + i * 54 * 1024; }
  const char* gsrc = ws + OFF_WGUF; const char* dsrc = ws + OFF_WDF + (R.ct * 5 * 54 + 2 * l) * 1024;
  u32x4 wr[4][5][2];
#pragma unroll
  for (int m = 0; m < 4; ++m)
#pragma unroll
    for (int i = 0; i < 5; ++i)
      asm volatile("global_load_dwordx4 %0, %2, %3\n\tglobal_load_dwordx4 %1, %2, %3 offset:1024" : "=&v"(wr[m][i][0]), "=&v"(wr[m][i][1]) : "v"(vd[i]), "s"(dsrc + (m + 5) * 6144) : "memory");
#pragma unroll
  for (int r = 0; r < 2; ++r) {
    const int part = (((bid >> 3) & 7) * 3 + l) * 2 + r, n = min(part * 47 + min(lane, 46), 2239), f = (n * 6554) >> 20;
    const unsigned o0 = OFF_WGUF + R.gu_nf(f) * 20480 + (n - f * 160) * 128;
    asm volatile("global_load_dword %0, %1, %2" : "=&v"(tw[r]) : "v"(o0), "s"(ws) : "memory");
  }
  {
    const int n = (((bid >> 3) & 7) * 3 + l) * 50 + min(lane, 49), f = (n * 4370) >> 20;
    const unsigned o1 = OFF_WDF + (R.ct * 5 + f) * 55296 + (n - f * 240) * 128;
    asm volatile("global_load_dword %0, %1, %2" : "=&v"(tw[4]) : "v"(o1), "s"(ws) : "memory");
  }
  const int g4 = min(l + 12, 13) * 2048 - l * 2048;
  auto gstage = [&](int s) {
    const unsigned base = lds0 + (s >= 6 ? s - 6 : s >= 1 ? s - 1 : 4) * G_STAGE + l * 2048;
#pragma unroll
    for (int i = 0; i < 5; ++i) glds<2>(vg[i], gsrc + s * 2048, base, i < 4 ? i * 6144 : g4);
  };
  auto dstage = [&](int m) {
    const unsigned base = lds0 + ((m ? 3 * m - 3 : 12) + l) * D_STAGE;
#pragma unroll
    for (int i = 0; i < 5; ++i) glds<2>(vd[i], dsrc + m * 6144, base, i * 2048);
  };
  asm volatile("s_barrier" ::: "memory");
  gstage(0);
  wait_vm_bar<53>();
  asm volatile("s_barrier" ::: "memory");
  gstage(1);
  asm volatile("s_barrier" ::: "memory");
  gstage(2); gstage(3); gstage(4);
  wait_vm_bar<40>();
  asm volatile("" :: "v"(tw[0]), "v"(tw[1]), "v"(tw[2]), "v"(tw[3]), "v"(tw[4]));
  wait_vm_bar<30>();
#pragma unroll 1
  for (int s = 2; s < 7; ++s) { wait_vm_bar<20>(); gstage(s + 3); }
  wait_vm_bar<20>(); dstage(0);
  wait_vm_bar<20>();
  wait_vm_bar<10>(); dstage(1);
  asm volatile("s_barrier" ::: "memory"); dstage(2);
  asm volatile("s_barrier" ::: "memory");
  dstage(3); dstage(4);
  asm volatile("s_barrier" ::: "memory");
  wait_vm_bar<0>();
#pragma unroll
  for (int m = 0; m < 4; ++m) {
    asm volatile("s_barrier" ::: "memory");
    lds_ptr d = (lds_ptr)smem + ((m ? 3 * m - 3 : 12) + l) * D_STAGE + vo;
#pragma unroll
    for (int i = 0; i < 5; ++i) {
      asm volatile("" : "+v"(wr[m][i][0]), "+v"(wr[m][i][1]));
      *(LDS_AS u32x4*)(d + i * 2048) = wr[m][i][0]; *(LDS_AS u32x4*)(d + i * 2048 + 1024) = wr[m][i][1];
    }
  }
  asm volatile("s_waitcnt lgkmcnt(0)\n\ts_barrier" ::: "memory");
}

DI void attention(char* smem, char* __restrict__ ws, rsrc_t rs, const Roles& R, int wid, int lane PROF_ARG) {
  lds_cptr L = (lds_cptr)smem;
  const int r = lane & 31, h = lane >> 5;
  const char* qb = ws + OFF_Q + R.head * Q_HEAD + (R.qt * 8 + wid * 2) * Q_TILE;
  const bf16x8 qaA = *(const bf16x8*)(qb + lane * 16), qaB = *(const bf16x8*)(qb + Q_TILE + lane * 16);
  u32x2 qtA = *(const u32x2*)(qb + 1024 + r * 8), qtB = *(const u32x2*)(qb + Q_TILE + 1024 + r * 8);
  if (h) { qtA = u32x2{0u, 0u}; qtB = qtA; }

  const int rm = r <= 20 ? r : (r == 24 ? 20 : 21);
  const int kaO = lane * 16, kbO = h ? 1280 + 320 : 1024 + r * 8, vO = 1280 + h * 352 + rm * 16;
  f32x16 zero, oA, oB, sA, sB;
#pragma unroll
  for (int i = 0; i < 16; ++i) { zero[i] = 0.f; oA[i] = 0.f; oB[i] = 0.f; }
  bf16x8 ka[2], v0[2], v1[2]; bf16x4 kb[2];
  lds_cptr pk = L + kaO, pb = L + kbO, pv = L + vO;
  wait_lgkm_bar();
  STAMP(1);
  kb[0] = lds_b64(pb); ka[0] = lds_b128(pk); v0[0] = lds_b128(pv); v1[0] = lds_b128(pv + 704);
  v1[1] = v1[0];
  {
    sA = mfma32(ka[0], qaA, mfma32k8(kb[0], __builtin_bit_cast(bf16x4, qtA), zero));
    sB = mfma32(ka[0], qaB, mfma32k8(kb[0], __builtin_bit_cast(bf16x4, qtB), zero));
    const unsigned nA = pk2(-rowmax16(sA), 0.f), nB = pk2(-rowmax16(sB), 0.f);
    if (h) { qtA[0] = nA; qtB[0] = nB; }
  }
  const bf16x4 qbA = __builtin_bit_cast(bf16x4, qtA), qbB = __builtin_bit_cast(bf16x4, qtB);
  sA = mfma32(ka[0], qaA, mfma32k8(kb[0], qbA, zero));
  const unsigned pcnt = (unsigned)(uintptr_t)smem + L_CNT + 4, pone = lane == 0;
  u32x4 pl = {0u, 0u, 0u, 0u};
  float e14 = 0.f, e15 = 0.f;

#define EX(i) __builtin_amdgcn_exp2f(sC[i])
  auto phase = [&](const f32x16& sC, f32x16& oC, f32x16& oP, bf16x8 vP, f32x16& sN, bf16x8 qa, bf16x4 qb4, bf16x8 kA, bf16x4 kB, bf16x8 vC, auto&& rd1, auto&& rd2) {
    u32x4 p0; float a, b, c, d;
    sN = mfma32k8(kB, qb4, zero); SBAR(); rd1(); SBAR();
    a = EX(0); SBAR(); pl[3] = pk2(e14, e15); SBAR(); b = EX(1); SBAR(); c = EX(2); SBAR(); p0[0] = pk2(a, b); SBAR(); d = EX(3); SBAR();
    oP = mfma32(vP, as_frag(pl), oP); SBAR(); rd2(); SBAR();
    a = EX(4); SBAR(); p0[1] = pk2(c, d); SBAR(); b = EX(5); SBAR(); c = EX(6); SBAR(); p0[2] = pk2(a, b); SBAR(); d = EX(7); SBAR();
    sN = mfma32(kA, qa, sN); SBAR();
    a = EX(8); SBAR(); p0[3] = pk2(c, d); SBAR(); b = EX(9); SBAR(); c = EX(10); SBAR(); pl[0] = pk2(a, b); SBAR(); d = EX(11); SBAR();
    oC = mfma32(vC, as_frag(p0), oC); SBAR(); __builtin_amdgcn_s_waitcnt(0xC07F); SBAR();
    a = EX(12); SBAR(); pl[1] = pk2(c, d); SBAR(); b = EX(13); SBAR(); e14 = EX(14); SBAR(); pl[2] = pk2(a, b); SBAR(); e15 = EX(15); SBAR();
  };
#undef EX
#pragma unroll 1
  for (int it = 0; it < 16; ++it) {
    const int dn = it == 14 ? -56 * KV_TILE : 4 * KV_TILE;
#pragma unroll
    for (int t = 0; t < 4; ++t) {
      const int c = t & 1, n = c ^ 1, no = t < 3 ? (t + 1) * KV_TILE : 0;
      phase(sA, oA, oB, v1[n], sB, qaB, qbB, ka[c], kb[c], v0[c],
            [&] {
              if (t == 0) asm volatile("s_bitcmp1_b32 %0, %1\n\ts_cbranch_scc0 .Lsk%=\n\ts_cmp_eq_u32 %1, 11\n\ts_cbranch_scc1 .Lsp%=\n\ts_barrier\n\ts_branch .Lsk%=\n"
                                       ".Lsp%=:\n\tds_add_u32 %2, %3\n.Lsk%=:" :: "s"(0x90A), "s"(it), "v"(pcnt), "v"(pone) : "scc", "memory");
              if (t == 3) { pk += dn; pb += dn; pv += dn; }
              kb[n] = lds_b64(pb + no); },
            [&] { ka[n] = lds_b128(pk + no); });
      phase(sB, oB, oA, v1[c], sA, qaA, qbA, ka[n], kb[n], v0[c], [&] { v0[n] = lds_b128(pv + no); }, [&] { v1[n] = lds_b128(pv + no + 704); });
    }
  }
  pl[3] = pk2(e14, e15);
  oB = mfma32(v1[1], as_frag(pl), oB);
  STAMP(2);

  float lA = h ? oA[8] : oA[12], lB = h ? oB[8] : oB[12];
  {
    f32x2 z0 = {0.f, 0.f}, z1 = z0;
#pragma unroll
    for (int i = 0; i < 7; ++i) { z0 = __builtin_elementwise_fma(f32x2{oA[2 * i], oA[2 * i + 1]}, f32x2{0.f, 0.f}, z0); z1 = __builtin_elementwise_fma(f32x2{oB[2 * i], oB[2 * i + 1]}, f32x2{0.f, 0.f}, z1); }
    z0 += z1;
    const bool ok = fmaxf(lA, lB) + (z0[0] + z0[1]) < 1.0e18f && fminf(lA, lB) > 1.0e-18f;
    if (__builtin_expect(!__all(ok), 0)) {
      const char* kvb = ws + OFF_KV + R.head * KV_HEAD;
      const bf16x4 z4 = {0, 0, 0, 0}, tA = h ? z4 : qbA, tB = h ? z4 : qbB;
      float mA = -INFINITY, mB = -INFINITY; oA = zero; oB = zero;
      auto upd = [&](f32x16 s, float& m, f32x16& o, bf16x8 w0, bf16x8 w1) {
        const float mn = fmaxf(m, rowmax16(s)), alpha = __builtin_amdgcn_exp2f(m - mn);
        m = mn; o *= alpha;
        u32x4 a, b;
#pragma unroll
        for (int i = 0; i < 4; ++i) { a[i] = exp_pk(s[2 * i] - mn, s[2 * i + 1] - mn); b[i] = exp_pk(s[8 + 2 * i] - mn, s[9 + 2 * i] - mn); }
        o = mfma32(w1, as_frag(b), mfma32(w0, as_frag(a), o));
      };
#pragma unroll 1
      for (int t = 0; t < 64; ++t) {
        const char* tb = kvb + t * KV_TILE;
        const bf16x8 k8 = *(const bf16x8*)(tb + kaO), w0 = *(const bf16x8*)(tb + vO), w1 = *(const bf16x8*)(tb + vO + 704);
        const bf16x4 k4 = *(const bf16x4*)(tb + kbO);
        upd(mfma32(k8, qaA, mfma32k8(k4, tA, zero)), mA, oA, w0, w1);
        upd(mfma32(k8, qaB, mfma32k8(k4, tB, zero)), mB, oB, w0, w1);
      }
      lA = h ? oA[8] : oA[12]; lB = h ? oB[8] : oB[12];
    }
  }
  const unsigned ob = OFF_OF + (R.qt * 16 + wid * 4) * 20480 + (R.head >> 1) * 1280;
  const unsigned lo = (r >> 4) * 20480 + (r & 15) * 16;
  auto fin = [&](const f32x16& o, float l, int blk) {
    const float inv = __builtin_amdgcn_rcpf(l);
    unsigned w[6];
#pragma unroll
    for (int u = 0; u < 6; ++u) w[u] = pk2(o[2 * u] * inv, o[2 * u + 1] * inv);
    const auto s0 = __builtin_amdgcn_permlane32_swap(w[0], w[2], false, false), s1 = __builtin_amdgcn_permlane32_swap(w[1], w[3], false, false);
    st16c(rs, lo + h * 256, ob + blk * 40960 + (R.head & 1) * 512, u32x4{s0[0], s1[0], s0[1], s1[1]});
    if (!h) st8c(rs, lo, ob + blk * 40960 + 1024 + (R.head & 1) * 8, u32x2{w[4], w[5]});
  };
  fin(oA, lA, 0); fin(oB, lB, 1);
}

#define P_DEPTH 14
#define P_PRO 4
#define P_RAMP 3
constexpr int ld_pos(int s, int pro, int ramp, int depth, int n) {
  int j = 2 * pro;
  for (int i = 0; i < s; ++i) { j += ramp; if (j > 2 * (i + 1 + depth)) j = 2 * (i + 1 + depth); if (j > 2 * n) j = 2 * n; }
  return j;
}
template <int MODE>
DI void proj(char* smem, const char* __restrict__ x, rsrc_t rs, char* __restrict__ out, const Roles& R, int wid, int lane PROF_ARG) {
  constexpr int NST = MODE ? 27 : 10, NKS = 2 * NST, DEPTH = MODE ? P_DEPTH : 10, PRO = P_PRO, RAMP = P_RAMP;
  lds_cptr L = (lds_cptr)smem;
  const int rh = wid >> 1, kp = wid & 1, t16 = lane & 15, g = lane >> 4;
  const int mf = R.rt * 4 + 2 * rh + kp, tok = mf * 16 + t16;
  const int ODD = R.ct & 1, U0 = (R.ct >> 1) * 5 + 3 * ODD, UH = (R.ct >> 1) * 5 + 2;
  const unsigned hp = OFF_HF + (mf * 20 + U0) * 1024, hh = OFF_HF + (mf * 20 + UH) * 1024 + ODD * 512;
  const unsigned fo = lane * 16, fh = (g >> 1) * 256 + t16 * 16 + (g & 1) * 8;
  const unsigned ro = tok * 1280 + U0 * 64 + g * 16, rhh = tok * 1280 + UH * 64 + ODD * 32 + g * 8;
  const unsigned ab = (MODE ? OFF_ACT : OFF_OF) + (mf * NKS + kp) * 1024, ab1 = ab + (1 - 2 * kp) * NKS * 1024;
  lds_cptr B = L + (MODE ? 0 : L_WO) + kp * 1024 + lane * 16, B1 = B + 6 * D_STAGE, B2 = B + 12 * D_STAGE;
  asm volatile("" : "+v"(B1), "+v"(B2));
  u32x4 res0, res1; u32x2 resh;
  if (MODE == 0) { res0 = *(const u32x4*)(x + ro); res1 = *(const u32x4*)(x + ro + 64); resh = *(const u32x2*)(x + rhh); }
  else { res0 = ld16c(rs, fo, hp); res1 = ld16c(rs, fo, hp + 1024); resh = ld8c(rs, fh, hh); }
  f32x4 acc[2][5];
  acc[0][0] = f32x4{bflo(res0[0]), bfhi(res0[0]), bflo(res0[1]), bfhi(res0[1])}; acc[0][1] = f32x4{bflo(res0[2]), bfhi(res0[2]), bflo(res0[3]), bfhi(res0[3])};
  acc[0][2] = f32x4{bflo(res1[0]), bfhi(res1[0]), bflo(res1[1]), bfhi(res1[1])}; acc[0][3] = f32x4{bflo(res1[2]), bfhi(res1[2]), bflo(res1[3]), bfhi(res1[3])};
  acc[0][4] = f32x4{bflo(resh[0]), bfhi(resh[0]), bflo(resh[1]), bfhi(resh[1])};
  asm volatile("" : "+v"(acc[0][0]), "+v"(acc[0][1]), "+v"(acc[0][2]), "+v"(acc[0][3]), "+v"(acc[0][4]));
  SBAR(); asm volatile("s_barrier" ::: "memory"); SBAR();
  STAMP(MODE ? 10 : 4);
  u32x4 a[DEPTH + 1][2];
  bf16x8 b[2][5];
  auto lda = [&](int j) { a[(j >> 1) % (DEPTH + 1)][j & 1] = ld16c(rs, fo, (j & 1 ? ab1 : ab) + (j >> 1) * 2048); };
  auto ldb = [&](int s, int v) { constexpr int W = 6 * D_STAGE; const int o = MODE ? ((s + 12) % 15) * D_STAGE + v * 2048 : s * 2048 + v * 20480; b[s & 1][v] = lds_b128((o < W ? B : o < 2 * W ? B1 : B2) + o % W); };
#pragma unroll
  for (int j = 0; j < 2 * PRO; ++j) lda(j);
#pragma unroll
  for (int v = 0; v < 5; ++v) ldb(0, v);
  SBAR();
  static_for<0, NST>([&](auto S) {
    constexpr int s = S.value, c = s % (DEPTH + 1);
    constexpr int j0 = ld_pos(s, PRO, RAMP, DEPTH, NST), j1 = ld_pos(s + 1, PRO, RAMP, DEPTH, NST);
    if (MODE && (s + 1) % 3 == 0 && s + 1 < 16) wait_lgkm_bar();
#pragma unroll
    for (int u = 0; u < 2; ++u)
#pragma unroll
      for (int v = 0; v < 5; ++v) {
        if (s || !u) mfma16a(acc[u][v], b[s & 1][v], a[c][u]); else mfma16z(acc[u][v], b[0][v], a[0][u]);
        if (u == 0 && s + 1 < NST) ldb(s + 1, v);
        if (u == 1 && j0 + v < j1) lda(j0 + v);
        SBAR();
      }
  });
  asm volatile("s_nop 7" ::: "memory");
  SBAR();
  STAMP(MODE ? 11 : 5);
  lds_ptr scr = (lds_ptr)smem + (MODE ? 9 * D_STAGE : L_RED) + lane * 16;
#pragma unroll
  for (int v = 0; v < 5; ++v) *(LDS_AS f32x4*)(scr + wid * 5120 + v * 1024) = acc[1][v];
  wait_lgkm_bar();
  f32x4 y[5];
#pragma unroll
  for (int v = 0; v < 5; ++v) y[v] = *(const LDS_AS f32x4*)(scr + (wid ^ 1) * 5120 + v * 1024);
  SBAR();
  unsigned o[10];
#pragma unroll
  for (int v = 0; v < 5; ++v) { y[v] += acc[0][v]; o[2 * v] = pk2(y[v][0], y[v][1]); o[2 * v + 1] = pk2(y[v][2], y[v][3]); }
  if (MODE == 0) {
    st16c(rs, fo, hp, u32x4{o[0], o[1], o[2], o[3]}); st16c(rs, fo, hp + 1024, u32x4{o[4], o[5], o[6], o[7]}); st8c(rs, fh, hh, u32x2{o[8], o[9]});
    float s0 = 0.f, s1 = 0.f, q0 = 0.f, q1 = 0.f;
    const bf2 one2 = __builtin_bit_cast(bf2, 0x3f803f80u);
#pragma unroll
    for (int i = 0; i < 5; ++i) {
      const bf2 e0 = __builtin_bit_cast(bf2, o[2 * i]), e1 = __builtin_bit_cast(bf2, o[2 * i + 1]);
      s0 = __builtin_amdgcn_fdot2_f32_bf16(e0, one2, s0, false); s1 = __builtin_amdgcn_fdot2_f32_bf16(e1, one2, s1, false);
      q0 = __builtin_amdgcn_fdot2_f32_bf16(e0, e0, q0, false); q1 = __builtin_amdgcn_fdot2_f32_bf16(e1, e1, q1, false);
    }
    const float ssum = xsum32(xsum16(s0 + s1)), ssq = xsum32(xsum16(q0 + q1));
    if (g == 0) st8c(rs, tok * 64, OFF_STAT + R.ct * 8, u32x2{__float_as_uint(ssum), __float_as_uint(ssq)});
  } else {
    *(u32x4*)(out + ro) = u32x4{o[0], o[1], o[2], o[3]}; *(u32x4*)(out + ro + 64) = u32x4{o[4], o[5], o[6], o[7]}; *(u32x2*)(out + rhh) = u32x2{o[8], o[9]};
  }
}

DI void gateup(char* smem, rsrc_t rs, const Roles& R, int wid, int lane PROF_ARG) {
  constexpr int DEPTH = 6;
  const int t16 = lane & 15, g = lane >> 4, mf = R.rg * 8 + 2 * wid;
  lds_cptr B0 = (lds_cptr)smem + lane * 16, B1 = B0 + 2 * G_STAGE, B2 = B0 + 4 * G_STAGE;
  asm volatile("" : "+v"(B1), "+v"(B2));
  const unsigned fo = lane * 16, ab = OFF_HF + mf * 20480;
  SBAR(); asm volatile("s_barrier" ::: "memory"); SBAR();
  STAMP(7);
  u32x4 a[DEPTH][2];
  bf16x8 b[14];
  f32x4 acc[2][14];
  auto lda = [&](int k, int u) { a[k % DEPTH][u] = ld16c(rs, fo, ab + XK(k) * 1024 + u * 20480); };
  auto ldb = [&](int k, int v) {
    const int slot = ((k >> 1) + 4) % 5;
    b[v] = lds_b128((slot < 2 ? B0 : slot < 4 ? B1 : B2) + (slot & 1 & (slot < 4)) * G_STAGE + (k & 1) * 1024 + v * 2048);
  };
#pragma unroll
  for (int k = 0; k < DEPTH - 1; ++k) { lda(k, 0); lda(k, 1); }
  u32x4 st0, st1;
#pragma unroll
  for (int v = 0; v < 14; ++v) ldb(0, v);
  SBAR();
  static_for<0, 20>([&](auto S) {
    constexpr int k = S.value;
    if (k & 1) { asm volatile("s_barrier" ::: "memory"); SBAR(); }
#pragma unroll
    for (int v = 0; v < 14; ++v) {
      if (k) { mfma16a(acc[0][v], b[v], a[k % DEPTH][0]); mfma16a(acc[1][v], b[v], a[k % DEPTH][1]); }
      else   { mfma16z(acc[0][v], b[v], a[0][0]); mfma16z(acc[1][v], b[v], a[0][1]); }
      if (k < 19) ldb(k + 1, v);
      if (v < 2 && k + DEPTH - 1 < 20) lda(k + DEPTH - 1, v);
      if (v == 0 && k + DEPTH - 1 == 20) st0 = ld16c(rs, t16 * 64 + g * 16, OFF_STAT + mf * 1024);
      if (v == 1 && k + DEPTH - 1 == 20) st1 = ld16c(rs, t16 * 64 + g * 16, OFF_STAT + mf * 1024 + 1024);
      SBAR();
    }
  });
  asm volatile("s_barrier\n\ts_nop 7" ::: "memory");
  SBAR();
  STAMP(8);
  float nm[2], vk[2], c1[2];
#pragma unroll
  for (int u = 0; u < 2; ++u) {
    const f32x4 s = __builtin_bit_cast(f32x4, u ? st1 : st0);
    const float mean = xsum32(xsum16(s[0] + s[2])) * (1.0f / DM);
    nm[u] = -mean; vk[u] = fmaxf(xsum32(xsum16(s[1] + s[3])) * (1.0f / DM) - mean * mean, 0.f) + LN_EPS;
    c1[u] = -LOG2E * __builtin_amdgcn_rsqf(vk[u]);
  }
  const LDS_AS f32x4* cs = (const LDS_AS f32x4*)((lds_cptr)smem + L_CS) + g;
  auto swi = [&](int u, int p) {
    const f32x4 gt = acc[u][2 * p] + cs[8 * p] * nm[u], up = acc[u][2 * p + 1] + cs[8 * p + 4] * nm[u], e = gt * c1[u], n = gt * up;
    f32x4 d;
#pragma unroll
    for (int i = 0; i < 4; ++i) d[i] = __builtin_amdgcn_exp2f(e[i]);
    d = d * vk[u] + vk[u];
#pragma unroll
    for (int i = 0; i < 4; ++i) d[i] = __builtin_amdgcn_rcpf(d[i]);
    d *= n;
    return u32x2{pk2(d[0], d[1]), pk2(d[2], d[3])};
  };
  const unsigned ob = OFF_ACT + mf * 54 * 1024;
#pragma unroll
  for (int i = 0; i < 3; ++i)
#pragma unroll
    for (int u = 0; u < 2; ++u) {
      const u32x2 pa = swi(u, 2 * i), pb = swi(u, 2 * i + 1);
      st16c(rs, fo, ob + (u * 54 + R.UB + i) * 1024, u32x4{pa[0], pa[1], pb[0], pb[1]});
    }
  if ((R.cg & 7) < 6) {
    const u32x2 pa = swi(0, 6), pb = swi(1, 6);
    const auto s0 = __builtin_amdgcn_permlane16_swap(pa[0], pb[0], false, false), s1 = __builtin_amdgcn_permlane16_swap(pa[1], pb[1], false, false);
    st16c(rs, (g & 1) * 54 * 1024 + (g >> 1) * 256 + t16 * 16, ob + R.UL * 1024 + (R.cg & 1) * 512, u32x4{s0[0], s1[0], s0[1], s1[1]});
  }
}

extern "C" __global__ __launch_bounds__(512, 2) void k_main(const char* __restrict__ x, const char* __restrict__ wo, const char* __restrict__ wg,
                                                            const char* __restrict__ wu, const char* __restrict__ wd, char* __restrict__ ws,
                                                            char* __restrict__ out) {
  __shared__ __attribute__((aligned(16))) char smem[163840];
  const int wid = __builtin_amdgcn_readfirstlane(threadIdx.x >> 6);
  PROF_DECL STAMP(0);
  const Roles R(blockIdx.x);
  const rsrc_t rs = __builtin_amdgcn_make_buffer_rsrc(ws, 0, (int)WS_BYTES, 0x00020000);
  if (wid >= 4) {
    helper_waves(smem, wo, wg, wu, wd, ws, rs, R, wid - 4, lane_id() PROF_PASS);
  } else {
    attention(smem, ws, rs, R, wid, lane_id() PROF_PASS);
    STX(1); wait_vm<0>(); STX(2);
    wait_vm_bar<0>();
    STAMP(3);
    proj<0>(smem, x, rs, out, R, wid, lane_id() PROF_PASS);
    STX(3); wait_vm<0>(); STX(4);
    wait_vm_bar<0>();
    STAMP(6);
    gateup(smem, rs, R, wid, lane_id() PROF_PASS);
    STX(5); wait_vm<0>(); STX(6);
    wait_vm_bar<0>();
    STAMP(9);
    proj<1>(smem, x, rs, out, R, wid, lane_id() PROF_PASS);
  }
  PROF_END(1);
}
extern "C" __global__ void k_end() {}

extern "C" void kernel_launch(void* const* d_in, const int* in_sizes, int n_in, void* d_out, int out_size, void* d_ws, size_t ws_size,
                              hipStream_t stream) {
  const char* x = (const char*)d_in[0]; const char* wqkv = (const char*)d_in[1]; const char* wo = (const char*)d_in[2];
  const char* wg = (const char*)d_in[4]; const char* wu = (const char*)d_in[5]; const char* wd = (const char*)d_in[6];
  char* ws = (char*)d_ws;
  k_qkv<<<256, 512, 0, stream>>>(x, wqkv, ws);
  k_main<<<256, 512, 0, stream>>>(x, wo, wg, wu, wd, ws, (char*)d_out);
}
```

```cpp
#define PROF
#define REALT
#define MEAS 0
#define FACT 2
#include <hip/hip_runtime.h>
#include <hip/hip_bf16.h>
#include <stdint.h>

typedef __attribute__((ext_vector_type(8))) short bf16x8;
typedef __attribute__((ext_vector_type(4))) short bf16x4;
typedef __attribute__((ext_vector_type(2))) float f32x2;
typedef __attribute__((ext_vector_type(4))) float f32x4;
typedef __attribute__((ext_vector_type(16))) float f32x16;
typedef __attribute__((ext_vector_type(4))) unsigned u32x4;
typedef __attribute__((ext_vector_type(2))) unsigned u32x2;
typedef __attribute__((ext_vector_type(2))) __bf16 bf2;
typedef __attribute__((address_space(3))) const char* lds_cptr;
typedef __attribute__((address_space(3))) char* lds_ptr;
#define LDS_AS __attribute__((address_space(3)))
#define DI __device__ __forceinline__

constexpr int SEQ = 2048, DM = 640, NH = 32, DH = 20, FF = 1728;
constexpr float LN_EPS = 1e-5f;
constexpr float LOG2E = 1.4426950408889634f;
constexpr float QSCALE = 0.22360679774997896f * LOG2E;

constexpr int Q_TILE = 1280;
constexpr int Q_HEAD = 64 * Q_TILE;
constexpr int KV_TILE = 2688;
constexpr int KV_HEAD = 64 * KV_TILE;
constexpr unsigned OFF_Q    = 0;
constexpr unsigned OFF_KV   = OFF_Q + NH * Q_HEAD;
constexpr unsigned OFF_OF   = OFF_KV + NH * KV_HEAD;
constexpr unsigned OFF_HF   = OFF_OF + SEQ * DM * 2;
constexpr unsigned OFF_ACT  = OFF_HF + SEQ * DM * 2;
constexpr unsigned OFF_WOF  = OFF_ACT + SEQ * FF * 2;
constexpr unsigned OFF_WGUF = OFF_WOF + DM * DM * 2;
constexpr unsigned OFF_WDF  = OFF_WGUF + DM * FF * 4;
constexpr unsigned OFF_STAT = OFF_WDF + FF * DM * 2;
constexpr unsigned OFF_CSUM = OFF_STAT + SEQ * 64;
constexpr unsigned OFF_FLAG = OFF_CSUM + 20 * 216 * 64;
constexpr unsigned FL_O = OFF_FLAG, FL_H = FL_O + 64 * 256, FL_ACT = FL_H + 128 * 256, FL_W = FL_ACT + 128 * 256;
constexpr int N_FLAGS = 384;
constexpr unsigned WS_BYTES = OFF_FLAG + N_FLAGS * 256;

#define PROF_DECL unsigned long long pt_[16] = {}; asm volatile("s_memrealtime %0\n\ts_waitcnt lgkmcnt(0)" : "=s"(pt_[14]) :: "memory");
#define TIMER "s_memrealtime"
#define STAMP(i) do { __builtin_amdgcn_sched_barrier(0); asm volatile(TIMER " %0\n\ts_waitcnt lgkmcnt(0)" : "=s"(pt_[i]) :: "memory"); __builtin_amdgcn_sched_barrier(0); } while (0)
#define PROF_END(k) do { asm volatile("s_waitcnt vmcnt(0)" ::: "memory"); STAMP(13); asm volatile("s_memrealtime %0\n\ts_waitcnt lgkmcnt(0)" : "=s"(pt_[15]) :: "memory"); \
  if (__builtin_amdgcn_mbcnt_hi(~0u, __builtin_amdgcn_mbcnt_lo(~0u, 0u)) == 0) { unsigned long long* pp_ = (unsigned long long*)(ws + (128ull << 20)) + (((k) * 256 + blockIdx.x) * 8 + wid) * 16; for (int i_ = 0; i_ < 16; ++i_) pp_[i_] = pt_[i_]; } } while (0)
#define PROF_ARG , unsigned long long (&pt_)[16]
#define PROF_PASS , pt_
#define STXN 0
#define STX(n) do { if (n == STXN) STAMP(12); } while (0)
template <int N> DI void glds(unsigned voff, const void* sbase, unsigned lds, int imm) {
#define GL(o) "\n\tglobal_load_lds_dwordx4 %0, %1 offset:" #o
  if (N == 1) asm volatile("s_add_u32 m0, %2, %3\n\ts_nop 0" GL(0) :: "v"(voff), "s"(sbase), "s"(lds), "ns"(imm) : "memory", "scc");
  if (N == 2) asm volatile("s_add_u32 m0, %2, %3\n\ts_nop 0" GL(0) GL(1024) :: "v"(voff), "s"(sbase), "s"(lds), "ns"(imm) : "memory", "scc");
  if (N == 4) asm volatile("s_add_u32 m0, %2, %3\n\ts_nop 0" GL(0) GL(1024) GL(2048) GL(3072) :: "v"(voff), "s"(sbase), "s"(lds), "ns"(imm) : "memory", "scc");
#undef GL
}
#define CODE_TOUCH(first, last, part, sink) do { unsigned long long pc_; int a_, b_; \
  asm volatile("s_getpc_b64 %0\n.Lct%=:\n\ts_mov_b32 %1, " #first "-.Lct%=\n\ts_mov_b32 %2, " #last "-.Lct%=" : "=s"(pc_), "=s"(a_), "=s"(b_)); \
  const int o_ = min(max(a_, 0) + lane * 128 + (part) * 8192, max(b_, 0)); \
  asm volatile("global_load_dword %0, %1, %2" : "=v"(sink) : "v"(o_), "s"(pc_) : "memory"); } while (0)
#define CODE_DONE(sink) asm volatile("s_waitcnt vmcnt(0)" :: "v"(sink) : "memory")
typedef __attribute__((address_space(1))) unsigned gu32;
typedef __amdgpu_buffer_rsrc_t rsrc_t;
#define LDAUX 16
#define XK(k) (k)
DI u32x4 ld16c(rsrc_t rs, unsigned vo, unsigned so) { return __builtin_amdgcn_raw_buffer_load_b128(rs, vo, so, LDAUX); }
DI u32x2 ld8c(rsrc_t rs, unsigned vo, unsigned so) { return __builtin_amdgcn_raw_buffer_load_b64(rs, vo, so, 16); }
DI void st16c(rsrc_t rs, unsigned vo, unsigned so, u32x4 v) { __builtin_amdgcn_raw_buffer_store_b128(v, rs, vo + so, 0, 16); }
DI void st8c(rsrc_t rs, unsigned vo, unsigned so, u32x2 v) { __builtin_amdgcn_raw_buffer_store_b64(v, rs, vo, so, 16); }
DI void arrive(char* ws, unsigned off, int lane, int stride) {
  if (lane < 8) __hip_atomic_fetch_add((gu32*)(ws + off + lane * stride), 1u, __ATOMIC_RELAXED, __HIP_MEMORY_SCOPE_AGENT);
}
DI void await8(char* ws, unsigned off, int lane, unsigned want) {
#pragma unroll 1
  for (int n = 0; n < (1 << 16); ++n) {
    const unsigned v = __hip_atomic_load((gu32*)(ws + off + (lane & 7) * 256), __ATOMIC_RELAXED, __HIP_MEMORY_SCOPE_AGENT);
    if (__all(v >= want)) break;
    __builtin_amdgcn_s_sleep(1);
  }
}
DI void await(char* ws, unsigned off, unsigned want) {
#pragma unroll 1
  for (int n = 0; n < (1 << 16); ++n) {
    const unsigned v = __hip_atomic_load((gu32*)(ws + off), __ATOMIC_RELAXED, __HIP_MEMORY_SCOPE_AGENT);
    if ((unsigned)__builtin_amdgcn_readfirstlane(v) >= want) break;
    __builtin_amdgcn_s_sleep(1);
  }
  __builtin_amdgcn_fence(__ATOMIC_ACQUIRE, "wavefront");
}
template <int N> DI void wait_vm_bar() { asm volatile("s_waitcnt vmcnt(%0)\n\ts_barrier" :: "n"(N) : "memory"); }
template <int N> DI void wait_vm() { asm volatile("s_waitcnt vmcnt(%0)" :: "n"(N) : "memory"); }
#define SBAR() __builtin_amdgcn_sched_barrier(0)
#define SGB(mask, n) __builtin_amdgcn_sched_group_barrier(mask, n, 0)
DI void wait_lgkm_bar() { SBAR(); __builtin_amdgcn_s_waitcnt(0xC07F); asm volatile("s_barrier" ::: "memory"); SBAR(); }
#define GEMM_OFF 0
template <int I> struct IC { static constexpr int value = I; };
template <int B, int E, class F> DI void static_for(F&& f) { if constexpr (B < E) { f(IC<B>{}); static_for<B + 1, E>(f); } }
DI bf16x8 lds_b128(lds_cptr p) { return *(const LDS_AS bf16x8*)p; }
DI bf16x4 lds_b64(lds_cptr p) { return *(const LDS_AS bf16x4*)p; }
DI bf16x4 lds_tr(lds_cptr p) { return __builtin_amdgcn_ds_read_tr16_b64_v4i16((LDS_AS bf16x4*)p); }
DI f32x4 mfma16(bf16x8 a, bf16x8 b, f32x4 c) { return __builtin_amdgcn_mfma_f32_16x16x32_bf16(a, b, c, 0, 0, 0); }
DI void mfma16a(f32x4& c, bf16x8 a, u32x4 b) { asm volatile("v_mfma_f32_16x16x32_bf16 %0, %1, %2, %0" : "+v"(c) : "v"(a), "v"(b)); }
DI void mfma16z(f32x4& c, bf16x8 a, u32x4 b) { asm volatile("v_mfma_f32_16x16x32_bf16 %0, %1, %2, 0" : "=&v"(c) : "v"(a), "v"(b)); }
DI f32x16 mfma32(bf16x8 a, bf16x8 b, f32x16 c) { return __builtin_amdgcn_mfma_f32_32x32x16_bf16(a, b, c, 0, 0, 0); }
DI f32x16 mfma32k8(bf16x4 a, bf16x4 b, f32x16 c) { return __builtin_amdgcn_mfma_f32_32x32x8bf16_1k(a, b, c, 0, 0, 0); }
DI unsigned pk2(float lo, float hi) { f32x2 v = {lo, hi}; return __builtin_bit_cast(unsigned, __builtin_convertvector(v, bf2)); }
DI float bflo(unsigned u) { return __uint_as_float(u << 16); }
DI float bfhi(unsigned u) { return __uint_as_float(u & 0xffff0000u); }
DI int div20(int c) { return (c * 3277) >> 16; }
DI float xsum32(float v) { auto r = __builtin_amdgcn_permlane32_swap(__float_as_uint(v), __float_as_uint(v), false, false); return __uint_as_float(r[0]) + __uint_as_float(r[1]); }
DI float xsum16(float v) { auto r = __builtin_amdgcn_permlane16_swap(__float_as_uint(v), __float_as_uint(v), false, false); return __uint_as_float(r[0]) + __uint_as_float(r[1]); }
constexpr bf16x8 ONES8 = {0x3f80, 0x3f80, 0x3f80, 0x3f80, 0x3f80, 0x3f80, 0x3f80, 0x3f80};

template <int MODE>
DI void qkv_compute(lds_cptr L, lds_ptr scr, int wid, int lane, int row0, int col0, char* __restrict__ ws PROF_ARG) {
  const int wr = wid >> 1, wc = wid & 1, r16 = lane & 15, g = lane >> 4, q = r16 >> 2, p = lane & 3;
  const int f = q | ((g & 1) << 2);
  const int rp = MODE == 2 ? 16 * (r16 >> 3) + (r16 & 7) : r16;
  constexpr int RSTEP = MODE == 2 ? 8 : 16;
  int aoff[2][2], boff[4];
#pragma unroll
  for (int e = 0; e < 2; ++e)
#pragma unroll
    for (int ks = 0; ks < 2; ++ks) { const int r = rp + e * RSTEP; aoff[e][ks] = (64 * wr + r) * 128 + (((4 * ks + g) ^ ((r >> 1) & 7) ^ ((r >> 2) & 4)) << 4); }
#pragma unroll
  for (int jj = 0; jj < 4; ++jj) { const int j = (jj + 2 * wr) & 3; boff[jj] = 16384 + (8 * g + q) * 256 + ((((4 * wc + j) ^ f) * 2 + (p >> 1)) << 4) + (p & 1) * 8; }
  const int pr0 = wc * 4096, pr1 = 4096 - pr0;

  f32x4 acc[4][4], rs[2], gr[2], cs[2];
#pragma unroll
  for (int i = 0; i < 4; ++i)
#pragma unroll
    for (int j = 0; j < 4; ++j) acc[i][j] = f32x4{0.f, 0.f, 0.f, 0.f};
#pragma unroll
  for (int i = 0; i < 2; ++i) { rs[i] = f32x4{0.f, 0.f, 0.f, 0.f}; gr[i] = rs[i]; cs[i] = rs[i]; }
  bf16x8 a0[4], b0[4], a1[4], b1[4];

  auto rd = [&](bf16x8 (&a)[4], bf16x8 (&b)[4], int slot, int ks) {
    if (GEMM_OFF && slot + ks > 0) return;
    lds_cptr S = L + slot;
#pragma unroll
    for (int jj = 0; jj < 4; ++jj) {
      bf16x4 lo = lds_tr(S + boff[jj] + ks * 8192), hi = lds_tr(S + boff[jj] + ks * 8192 + 1024);
      b[jj] = __builtin_shufflevector(lo, hi, 0, 1, 2, 3, 4, 5, 6, 7);
      a[jj] = lds_b128(S + aoff[jj & 1][ks] + (jj < 2 ? pr0 : pr1));
    }
  };
  auto mm = [&](bf16x8 (&a)[4], bf16x8 (&b)[4]) {
    if (GEMM_OFF) return;
#pragma unroll
    for (int ii = 0; ii < 4; ++ii)
#pragma unroll
      for (int jj = 0; jj < 4; ++jj)
        acc[ii][jj] = (MODE == 2) ? mfma16(a[ii], b[jj], acc[ii][jj]) : mfma16(b[jj], a[ii], acc[ii][jj]);
#pragma unroll
    for (int u = 0; u < 2; ++u) {
      rs[u] = mfma16(a[u], ONES8, rs[u]);
      gr[u] = mfma16(a[u], a[u], gr[u]);
      cs[u] = mfma16(ONES8, b[u], cs[u]);
    }
  };
  auto ilv = [&] {
#pragma unroll
    for (int i = 0; i < 12; ++i) { SGB(0x8, 1); SGB(0x100, 1); }
    SGB(0x8, 10);
    SBAR();
  };

  wait_lgkm_bar();
  STAMP(1);
  rd(a0, b0, 0, 0);
  SBAR();
#pragma unroll 1
  for (int kt = 0; kt < 9; ++kt) {
    rd(a1, b1, (kt & 3) * 32768, 1); mm(a0, b0); ilv();
    wait_lgkm_bar();
    rd(a0, b0, ((kt + 1) & 3) * 32768, 0); mm(a1, b1); ilv();
  }
  rd(a1, b1, 32768, 1); mm(a0, b0); ilv();
  mm(a1, b1);
  SBAR();
  STAMP(2);
  LDS_AS float* rsum = (LDS_AS float*)scr; LDS_AS float* rsq = rsum + 128; LDS_AS float* csum = rsum + 256;
#pragma unroll
  for (int u = 0; u < 2; ++u) {
    const int rb = 64 * wr + 32 * wc + 16 * u;
    if (r16 == 0) *(LDS_AS f32x4*)(rsum + rb + 4 * g) = rs[u];
    if (q == g) { const float d = p == 0 ? gr[u][0] : p == 1 ? gr[u][1] : p == 2 ? gr[u][2] : gr[u][3]; rsq[rb + r16] = d; }
    if (g == 0) csum[64 * wc + 32 * wr + 16 * u + r16] = cs[u][0];
  }
  constexpr float invD = 1.0f / DM;
  if (MODE != 2) {
    constexpr int TS = MODE == 0 ? Q_TILE : KV_TILE, HS = MODE == 0 ? Q_HEAD : KV_HEAD;
    unsigned off[4][2];
#pragma unroll
    for (int jj = 0; jj < 4; ++jj) {
      const int ck = col0 - MODE * DM + 64 * wc + 16 * ((jj + 2 * wr) & 3) + 4 * g, head = div20(ck), d = ck - head * DH;
      off[jj][0] = head * HS + (d < 16 ? (d >> 3) * 512 + (d & 7) * 2 + r16 * 16 : 1024 + r16 * 8);
      off[jj][1] = off[jj][0] + (d < 16 ? 256 : 128);
    }
    char* base = ws + (MODE == 0 ? OFF_Q : OFF_KV) + ((row0 >> 5) + 2 * wr) * TS;
    wait_lgkm_bar();
    f32x4 c4[4];
#pragma unroll
    for (int jj = 0; jj < 4; ++jj) c4[jj] = *(const LDS_AS f32x4*)(csum + 64 * wc + 16 * ((jj + 2 * wr) & 3) + 4 * g);
#pragma unroll
    for (int ii = 0; ii < 4; ++ii) {
      const int half = ((ii >> 1) + wc) & 1, rl = 64 * wr + 32 * half + 16 * (ii & 1) + r16;
      const float mean = rsum[rl] * invD, var = fmaxf(rsq[rl] * invD - mean * mean, 0.f);
      const float rstd = __builtin_amdgcn_rsqf(var + LN_EPS) * (MODE == 0 ? QSCALE : 1.0f), nb = -mean * rstd;
      char* tb = base + half * TS;
#pragma unroll
      for (int jj = 0; jj < 4; ++jj) {
        const f32x4 v = acc[ii][jj] * rstd + c4[jj] * nb;
        *(u32x2*)(tb + off[jj][ii & 1]) = u32x2{pk2(v[0], v[1]), pk2(v[2], v[3])};
      }
    }
  } else {
    unsigned off[4];
#pragma unroll
    for (int jj = 0; jj < 4; ++jj) {
      const int cv = col0 - 2 * DM + 64 * wc + 16 * ((jj + 2 * wr) & 3) + r16, head = div20(cv), d = cv - head * DH;
      off[jj] = head * KV_HEAD + 1280 + g * 352 + d * 16;
    }
    char* base = ws + OFF_KV + ((row0 >> 5) + 2 * wr) * KV_TILE;
    { const int head = div20(col0 - 2 * DM + 19) + (lane >> 3), t = lane & 7;
      if (head * DH < col0 - 2 * DM + 128) *(u32x4*)(base + head * KV_HEAD + (t >> 2) * KV_TILE + 1280 + (t & 3) * 352 + 320 + wc * 16) = wc ? u32x4{0u, 0u, 0u, 0u} : __builtin_bit_cast(u32x4, ONES8); }
    wait_lgkm_bar();
    float c1[4];
#pragma unroll
    for (int jj = 0; jj < 4; ++jj) c1[jj] = csum[64 * wc + 16 * ((jj + 2 * wr) & 3) + r16];
#pragma unroll
    for (int ip = 0; ip < 2; ++ip) {
      const int half = (ip + wc) & 1;
      f32x4 rstd[2], nb[2];
#pragma unroll
      for (int e = 0; e < 2; ++e) {
        const int rl = 64 * wr + 32 * half + 16 * e + 4 * g;
        const f32x4 mean = *(const LDS_AS f32x4*)(rsum + rl) * invD, q4 = *(const LDS_AS f32x4*)(rsq + rl);
#pragma unroll
        for (int i = 0; i < 4; ++i) rstd[e][i] = __builtin_amdgcn_rsqf(fmaxf(q4[i] * invD - mean[i] * mean[i], 0.f) + LN_EPS);
        nb[e] = -mean * rstd[e];
      }
      char* tb = base + half * KV_TILE;
#pragma unroll
      for (int jj = 0; jj < 4; ++jj) {
        const f32x4 v0 = acc[2 * ip][jj] * rstd[0] + nb[0] * c1[jj], v1 = acc[2 * ip + 1][jj] * rstd[1] + nb[1] * c1[jj];
        *(u32x4*)(tb + off[jj]) = u32x4{pk2(v0[0], v0[1]), pk2(v0[2], v0[3]), pk2(v1[0], v1[1]), pk2(v1[2], v1[3])};
      }
    }
  }
}

extern "C" __global__ __launch_bounds__(512, 2) void k_qkv(const char* __restrict__ x, const char* __restrict__ w, char* __restrict__ ws) {
  __shared__ __attribute__((aligned(16))) char smem[4 * 32768 + 2048];
  const int bid = blockIdx.x, xcd = bid & 7, idx = bid >> 3;
  const int rt = (xcd >> 1) * 4 + (idx & 3), ct = (xcd & 1) * 8 + (idx >> 2);
  if (ct >= 15) {
    if (rt == 0 && threadIdx.x < N_FLAGS) *(unsigned*)(ws + OFF_FLAG + threadIdx.x * 256) = 0u;
    return;
  }
  PROF_DECL STAMP(0);
  const int tid = threadIdx.x, lane = tid & 63, wid = __builtin_amdgcn_readfirstlane(tid >> 6);
  if (wid >= 4) {
    const int lw = wid - 4;
    const unsigned lds0 = (unsigned)(uintptr_t)smem + lw * 1024;
    const int ra = 8 * lw + (lane >> 3), rb = 4 * lw + (lane >> 4), f = (rb & 3) | ((rb >> 1) & 4);
    const unsigned voA = ra * 1280 + (((lane & 7) ^ ((ra >> 1) & 7) ^ ((ra >> 2) & 4)) << 4), voB = rb * 3840 + (((lane & 15) ^ (f << 1)) << 4);
    const char* pa = x + (size_t)rt * 128 * 1280; const char* pb = w + ct * 256;
    auto issue = [&](int st) {
      const unsigned base = lds0 + (st & 3) * 32768;
#pragma unroll
      for (int j = 0; j < 4; ++j) { glds<1>(voA + j * 32 * 1280, pa + st * 128, base, j * 4096); glds<1>(voB + j * 16 * 3840, pb + st * 64 * 3840, base, 16384 + j * 4096); }
    };
    STAMP(1); issue(0); issue(1); STAMP(2);
    wait_vm_bar<8>(); STAMP(3);
    issue(2); issue(3);
#pragma unroll 1
    for (int s = 1; s < 7; ++s) { wait_vm_bar<16>(); issue(s + 3); }
    wait_vm_bar<16>(); wait_vm_bar<8>(); wait_vm_bar<0>();
    PROF_END(0);
    return;
  }
  lds_cptr L = (lds_cptr)smem; lds_ptr scr = (lds_ptr)smem + 4 * 32768;
  unsigned cs0, cs1; CODE_TOUCH(k_qkv, k_end, wid, cs0); CODE_TOUCH(k_qkv, k_end, wid + 4, cs1);
  if (ct < 5) qkv_compute<0>(L, scr, wid, lane, rt * 128, ct * 128, ws PROF_PASS);
  else if (ct < 10) qkv_compute<1>(L, scr, wid, lane, rt * 128, ct * 128, ws PROF_PASS);
  else qkv_compute<2>(L, scr, wid, lane, rt * 128, ct * 128, ws PROF_PASS);
  CODE_DONE(cs0); CODE_DONE(cs1);
  PROF_END(0);
}

constexpr int L_STG = 48 * KV_TILE;
constexpr int L_WO = 4 * KV_TILE;
constexpr int G_STAGE = 28 * 1024;
constexpr int L_RED = 5 * G_STAGE;
constexpr int D_STAGE = 10 * 1024;
constexpr int L_CS = 15 * D_STAGE;
constexpr int L_CNT = 163840 - 16;

DI float rowmax16(const f32x16& s) {
  float a = fmaxf(fmaxf(s[0], s[1]), s[2]), b = fmaxf(fmaxf(s[3], s[4]), s[5]);
  a = fmaxf(fmaxf(a, s[6]), s[7]); b = fmaxf(fmaxf(b, s[8]), s[9]);
  a = fmaxf(fmaxf(a, s[10]), s[11]); b = fmaxf(fmaxf(b, s[12]), s[13]);
  a = fmaxf(fmaxf(a, s[14]), s[15]);
  const float m = fmaxf(a, b);
  auto rr = __builtin_amdgcn_permlane32_swap(__float_as_uint(m), __float_as_uint(m), false, false);
  return fmaxf(__uint_as_float(rr[0]), __uint_as_float(rr[1]));
}
DI unsigned exp_pk(float a, float b) { return pk2(__builtin_amdgcn_exp2f(a), __builtin_amdgcn_exp2f(b)); }
DI int lane_id() { int v; asm volatile("v_mbcnt_lo_u32_b32 %0, -1, 0\n\tv_mbcnt_hi_u32_b32 %0, -1, %0" : "=v"(v)); __builtin_assume(v >= 0 && v < 64); return v; }
DI bf16x8 as_frag(u32x4 v) { return __builtin_bit_cast(bf16x8, v); }

template <int KIND>
DI void retile_issue(const char* __restrict__ w0, const char* __restrict__ w1, int tile, int ks, int lane, unsigned stg) {
  if (KIND == 0) {
    const unsigned vo = (ks * 32 + (lane >> 3)) * (FF * 2) + tile * 128 + (lane & 7) * 16;
#pragma unroll
    for (int j = 0; j < 4; ++j) { glds<1>(vo + j * 8 * (FF * 2), w0, stg, j * 1024); glds<1>(vo + j * 8 * (FF * 2), w1, stg, 4096 + j * 1024); }
  } else {
#pragma unroll
    for (int j = 0; j < 8; ++j) {
      int k = ks * 32 + 4 * j + (lane >> 4);
      if (KIND == 2) { const int pp = div20(k) >> 1, e = k - 40 * pp; k = 40 * pp + (e < 16 ? e : e < 32 ? e + 4 : e < 36 ? e - 16 : e); }
      glds<1>(k * (DM * 2) + tile * 256 + (lane & 15) * 16, w0, stg, j * 1024);
    }
  }
}
template <int KIND>
DI void retile_store(rsrc_t rs, lds_cptr S, int tile, int ks, int lane) {
  const int g = lane >> 4, q = (lane & 15) >> 2, p = lane & 3;
#pragma unroll
  for (int f = 0; f < 8; ++f) {
    lds_cptr a; int hi; unsigned dst;
    if (KIND == 0) {
      const int U = 2 * tile + (f >> 2), j = (f >> 1) & 1;
      a = S + (f & 1) * 4096 + (8 * g + q) * 128 + (32 * (f >> 2) + ((U >= 27 ? U - 27 : U) >= 24 ? 16 * j + 4 * p : 8 * p + 4 * j)) * 2; hi = 512;
      dst = OFF_WGUF + ((8 * tile + f) * 20 + ks) * 1024;
    } else {
      const int U = 4 * tile + (f >> 1), j = f & 1;
      a = S + (8 * g + q) * 256 + (32 * (f >> 1) + (U % 5 == 2 ? 16 * j + 4 * p : 8 * p + 4 * j)) * 2; hi = 1024;
      const int n10 = (2 * U + j) % 10, nf = 2 * U + j + (n10 == 5 ? 4 : n10 > 5 ? -1 : 0);
      dst = (KIND == 1 ? OFF_WDF : OFF_WOF) + (nf * (KIND == 1 ? 54 : 20) + ks) * 1024;
    }
    const bf16x8 fr = __builtin_shufflevector(lds_tr(a), lds_tr(a + hi), 0, 1, 2, 3, 4, 5, 6, 7);
    st16c(rs, lane * 16, dst, __builtin_bit_cast(u32x4, fr));
    if (KIND == 0) {
      const f32x4 c = mfma16(fr, ONES8, f32x4{0.f, 0.f, 0.f, 0.f});
      if ((lane & 15) == 0) st16c(rs, g * 16, OFF_CSUM + (ks * 216 + 8 * tile + f) * 64, __builtin_bit_cast(u32x4, c));
    }
  }
}

struct Roles {
  int head, qt, rt, ct, rg, cg, UB, UL;
  DI Roles(int bid) {
    const int xcd = bid & 7, idx = bid >> 3;
    head = xcd * 4 + (idx >> 3); qt = idx & 7;
    rt = (xcd >> 1) * 8 + (idx & 7); ct = (xcd & 1) * 4 + (idx >> 3);
    rg = (xcd >> 2) * 8 + (idx & 7); cg = (xcd & 3) * 4 + (idx >> 3);
    UB = (cg >> 3) * 27 + 3 * (cg & 7); UL = (cg >> 3) * 27 + 24 + min((cg & 7) >> 1, 2);
  }
  DI int gu_nf(int f) const { return f < 12 ? 4 * UB + f : 4 * UL + 2 * (cg & 1) + f - 12; }
};

DI void helper_waves(char* smem, const char* __restrict__ wo, const char* __restrict__ wg, const char* __restrict__ wu, const char* __restrict__ wd,
                     char* __restrict__ ws, rsrc_t rs, const Roles& R, int hw, int lane PROF_ARG) {
  const int bid = blockIdx.x;
  const unsigned lds0 = (unsigned)(uintptr_t)smem, vo = lane * 16;
  LDS_AS unsigned* cnt = (LDS_AS unsigned*)((lds_ptr)smem + L_CNT);
  const char* kv = ws + OFF_KV + R.head * KV_HEAD;
  const unsigned vk = vo + hw * 4096, lk = lds0 + hw * 4096;
  auto runs = [&](int i0, int i1) {
#pragma unroll
    for (int i = i0; i < i1; ++i) glds<4>(vk, kv + i * 16384, lk, i * 16384);
  };
  runs(0, 3);
  if (hw == 0) { cnt[0] = 0u; cnt[1] = 0u; }
  STAMP(1);
  asm volatile("s_waitcnt vmcnt(8) lgkmcnt(0)\n\ts_barrier" ::: "memory");
  runs(3, 6);
  const int job = bid * 4 + hw;
  const unsigned stg = lds0 + L_STG + hw * 8192;
  int tile = 0, ks = 0;
  if (job < 540)      { ks = job / 27; tile = job - ks * 27; retile_issue<0>(wg, wu, tile, ks, lane, stg); }
  else if (job < 810) { ks = (job - 540) / 5; tile = job - 540 - ks * 5; retile_issue<1>(wd, wd, tile, ks, lane, stg); }
  else                { ks = min(job - 810, 99) / 5; tile = min(job - 810, 99) - ks * 5; retile_issue<2>(wo, wo, tile, ks, lane, stg); }
  wait_vm_bar<20>();
  runs(6, 7);
  if (hw < 3) glds<4>(vk, kv + 7 * 16384, lk, 7 * 16384); else { glds<2>(vk, kv + 7 * 16384, lk, 7 * 16384); glds<2>(vk, kv + 7 * 16384, lk, 7 * 16384); }
  wait_vm_bar<16>();
  if (hw < 3) glds<4>(vk, kv + 60 * KV_TILE, lk, 0);
  if (hw < 3) wait_vm<4>(); else wait_vm<0>();
  STAMP(2);
  lds_cptr S = (lds_cptr)smem + L_STG + hw * 8192;
  if (job < 540) retile_store<0>(rs, S, tile, ks, lane);
  else if (job < 810) retile_store<1>(rs, S, tile, ks, lane);
  else if (job < 910) retile_store<2>(rs, S, tile, ks, lane);
  asm volatile("s_waitcnt lgkmcnt(0)" ::: "memory");
  glds<4>(vo + hw * 8192, kv + L_STG, stg, 0); glds<4>(vo + hw * 8192, kv + L_STG + 4096, stg, 4096);
  wait_vm<0>();
  STAMP(3);
  unsigned old = 0;
  if (lane == 0) old = __hip_atomic_fetch_add(cnt, 1u, __ATOMIC_RELAXED, __HIP_MEMORY_SCOPE_WORKGROUP);
  if (__builtin_amdgcn_readfirstlane(old) == 3) arrive(ws, FL_W + (bid & 7) * 256, lane, 2048);
  asm volatile("s_barrier" ::: "memory");
  if (hw == 0) {
    await8(ws, FL_W + ((bid >> 3) & 7) * 2048, lane, 32);
    __builtin_amdgcn_fence(__ATOMIC_ACQUIRE, "agent");
    wait_vm<0>();
    if (lane == 0) __hip_atomic_fetch_add(cnt + 1, 1u, __ATOMIC_RELAXED, __HIP_MEMORY_SCOPE_WORKGROUP);
  }
  STAMP(4);
  while (__builtin_amdgcn_readfirstlane(__hip_atomic_load(cnt + 1, __ATOMIC_RELAXED, __HIP_MEMORY_SCOPE_WORKGROUP)) < 5) __builtin_amdgcn_s_sleep(4);
  unsigned tw[5];
  CODE_TOUCH(k_main, k_end, hw, tw[2]); CODE_TOUCH(k_main, k_end, hw + 4, tw[3]);
  {
    const char* src = ws + OFF_WOF + R.ct * 102400;
#pragma unroll
    for (int i = 0; i < 6; ++i) glds<4>(vk, src + i * 16384, lk, L_WO + i * 16384);
    glds<1>(vo + hw * 1024, src + 98304, lds0 + hw * 1024, L_WO + 98304);
  }
  if (hw == 0) {
    f32x4 csum;
    {
      const int t = min(lane, 55);
      const f32x4* cp = (const f32x4*)(ws + OFF_CSUM) + R.gu_nf(t >> 2) * 4 + (t & 3);
      f32x4 c[20];
#pragma unroll
      for (int k = 0; k < 20; ++k) c[k] = cp[k * 864];
#pragma unroll
      for (int k = 0; k < 10; ++k) c[k] += c[k + 10];
      csum = ((c[0] + c[1]) + (c[2] + c[3])) + ((c[4] + c[5]) + (c[6] + c[7])) + (c[8] + c[9]);
    }
    wait_vm_bar<0>();
    asm volatile("" :: "v"(tw[2]), "v"(tw[3]));
    STAMP(5);
    arrive(ws, FL_O + R.qt * 2048, lane, 256);
    await(ws, FL_O + (R.rt >> 2) * 2048 + R.ct * 256, 32);
    STAMP(6);
    asm volatile("s_barrier\n\ts_barrier\n\ts_barrier" ::: "memory");
    STAMP(7);
    arrive(ws, FL_H + (R.rt >> 1) * 2048, lane, 256);
    if (lane < 56) *(LDS_AS f32x4*)((lds_ptr)smem + L_CS + lane * 16) = csum;
    await(ws, FL_H + R.rg * 2048 + (R.cg & 7) * 256, 16);
    STAMP(8);
    asm volatile("s_waitcnt lgkmcnt(0)\n\ts_barrier" ::: "memory");
#pragma unroll 1
    for (int s = 0; s < 12; ++s) asm volatile("s_barrier" ::: "memory");
    STAMP(9);
    arrive(ws, FL_ACT + R.rg * 2048, lane, 256);
    await(ws, FL_ACT + (R.rt >> 1) * 2048 + R.ct * 256, 16);
    STAMP(10);
    asm volatile("s_barrier" ::: "memory");
    return;
  }
  const int l = hw - 1;
  unsigned vg[5], vd[5];
#pragma unroll
  for (int i = 0; i < 5; ++i) { vg[i] = vo + R.gu_nf(min(l + 3 * i, 13)) * 20480; vd[i] = vo + i * 54 * 1024; }
  const char* gsrc = ws + OFF_WGUF; const char* dsrc = ws + OFF_WDF + (R.ct * 5 * 54 + 2 * l) * 1024;
  u32x4 wr[4][5][2];
#pragma unroll
  for (int m = 0; m < 4; ++m)
#pragma unroll
    for (int i = 0; i < 5; ++i)
      asm volatile("global_load_dwordx4 %0, %2, %3\n\tglobal_load_dwordx4 %1, %2, %3 offset:1024" : "=&v"(wr[m][i][0]), "=&v"(wr[m][i][1]) : "v"(vd[i]), "s"(dsrc + (m + 5) * 6144) : "memory");
#pragma unroll
  for (int r = 0; r < 2; ++r) {
    const int part = (((bid >> 3) & 7) * 3 + l) * 2 + r, n = min(part * 47 + min(lane, 46), 2239), f = (n * 6554) >> 20;
    const unsigned o0 = OFF_WGUF + R.gu_nf(f) * 20480 + (n - f * 160) * 128;
    asm volatile("global_load_dword %0, %1, %2" : "=&v"(tw[r]) : "v"(o0), "s"(ws) : "memory");
  }
  {
    const int n = (((bid >> 3) & 7) * 3 + l) * 50 + min(lane, 49), f = (n * 4370) >> 20;
    const unsigned o1 = OFF_WDF + (R.ct * 5 + f) * 55296 + (n - f * 240) * 128;
    asm volatile("global_load_dword %0, %1, %2" : "=&v"(tw[4]) : "v"(o1), "s"(ws) : "memory");
  }
  const int g4 = min(l + 12, 13) * 2048 - l * 2048;
  auto gstage = [&](int s) {
    const unsigned base = lds0 + (s >= 6 ? s - 6 : s >= 1 ? s - 1 : 4) * G_STAGE + l * 2048;
#pragma unroll
    for (int i = 0; i < 5; ++i) glds<2>(vg[i], gsrc + s * 2048, base, i < 4 ? i * 6144 : g4);
  };
  auto dstage = [&](int m) {
    const unsigned base = lds0 + ((m ? 3 * m - 3 : 12) + l) * D_STAGE;
#pragma unroll
    for (int i = 0; i < 5; ++i) glds<2>(vd[i], dsrc + m * 6144, base, i * 2048);
  };
  asm volatile("s_barrier" ::: "memory");
  gstage(0);
  wait_vm_bar<53>();
  asm volatile("s_barrier" ::: "memory");
  gstage(1);
  asm volatile("s_barrier" ::: "memory");
  gstage(2); gstage(3); gstage(4);
  wait_vm_bar<40>();
  asm volatile("" :: "v"(tw[0]), "v"(tw[1]), "v"(tw[2]), "v"(tw[3]), "v"(tw[4]));
  wait_vm_bar<30>();
#pragma unroll 1
  for (int s = 2; s < 7; ++s) { wait_vm_bar<20>(); gstage(s + 3); }
  wait_vm_bar<20>(); dstage(0);
  wait_vm_bar<20>();
  wait_vm_bar<10>(); dstage(1);
  asm volatile("s_barrier" ::: "memory"); dstage(2);
  asm volatile("s_barrier" ::: "memory");
  dstage(3); dstage(4);
  asm volatile("s_barrier" ::: "memory");
  wait_vm_bar<0>();
#pragma unroll
  for (int m = 0; m < 4; ++m) {
    asm volatile("s_barrier" ::: "memory");
    lds_ptr d = (lds_ptr)smem + ((m ? 3 * m - 3 : 12) + l) * D_STAGE + vo;
#pragma unroll
    for (int i = 0; i < 5; ++i) {
      asm volatile("" : "+v"(wr[m][i][0]), "+v"(wr[m][i][1]));
      *(LDS_AS u32x4*)(d + i * 2048) = wr[m][i][0]; *(LDS_AS u32x4*)(d + i * 2048 + 1024) = wr[m][i][1];
    }
  }
  asm volatile("s_waitcnt lgkmcnt(0)\n\ts_barrier" ::: "memory");
}

DI void attention(char* smem, char* __restrict__ ws, rsrc_t rs, const Roles& R, int wid, int lane PROF_ARG) {
  lds_cptr L = (lds_cptr)smem;
  const int r = lane & 31, h = lane >> 5;
  const char* qb = ws + OFF_Q + R.head * Q_HEAD + (R.qt * 8 + wid * 2) * Q_TILE;
  const bf16x8 qaA = *(const bf16x8*)(qb + lane * 16), qaB = *(const bf16x8*)(qb + Q_TILE + lane * 16);
  u32x2 qtA = *(const u32x2*)(qb + 1024 + r * 8), qtB = *(const u32x2*)(qb + Q_TILE + 1024 + r * 8);
  if (h) { qtA = u32x2{0u, 0u}; qtB = qtA; }

  const int rm = r <= 20 ? r : (r == 24 ? 20 : 21);
  const int kaO = lane * 16, kbO = h ? 1280 + 320 : 1024 + r * 8, vO = 1280 + h * 352 + rm * 16;
  f32x16 zero, oA, oB, sA, sB;
#pragma unroll
  for (int i = 0; i < 16; ++i) { zero[i] = 0.f; oA[i] = 0.f; oB[i] = 0.f; }
  bf16x8 ka[2], v0[2], v1[2]; bf16x4 kb[2];
  lds_cptr pk = L + kaO, pb = L + kbO, pv = L + vO;
  wait_lgkm_bar();
  STAMP(1);
  kb[0] = lds_b64(pb); ka[0] = lds_b128(pk); v0[0] = lds_b128(pv); v1[0] = lds_b128(pv + 704);
  v1[1] = v1[0];
  {
    sA = mfma32(ka[0], qaA, mfma32k8(kb[0], __builtin_bit_cast(bf16x4, qtA), zero));
    sB = mfma32(ka[0], qaB, mfma32k8(kb[0], __builtin_bit_cast(bf16x4, qtB), zero));
    const unsigned nA = pk2(-rowmax16(sA), 0.f), nB = pk2(-rowmax16(sB), 0.f);
    if (h) { qtA[0] = nA; qtB[0] = nB; }
  }
  const bf16x4 qbA = __builtin_bit_cast(bf16x4, qtA), qbB = __builtin_bit_cast(bf16x4, qtB);
  sA = mfma32(ka[0], qaA, mfma32k8(kb[0], qbA, zero));
  const unsigned pcnt = (unsigned)(uintptr_t)smem + L_CNT + 4, pone = lane == 0;
  u32x4 pl = {0u, 0u, 0u, 0u};
  float e14 = 0.f, e15 = 0.f;

#define EX(i) __builtin_amdgcn_exp2f(sC[i])
  auto phase = [&](const f32x16& sC, f32x16& oC, f32x16& oP, bf16x8 vP, f32x16& sN, bf16x8 qa, bf16x4 qb4, bf16x8 kA, bf16x4 kB, bf16x8 vC, auto&& rd1, auto&& rd2) {
    u32x4 p0; float a, b, c, d;
    sN = mfma32k8(kB, qb4, zero); SBAR(); rd1(); SBAR();
    a = EX(0); SBAR(); pl[3] = pk2(e14, e15); SBAR(); b = EX(1); SBAR(); c = EX(2); SBAR(); p0[0] = pk2(a, b); SBAR(); d = EX(3); SBAR();
    oP = mfma32(vP, as_frag(pl), oP); SBAR(); rd2(); SBAR();
    a = EX(4); SBAR(); p0[1] = pk2(c, d); SBAR(); b = EX(5); SBAR(); c = EX(6); SBAR(); p0[2] = pk2(a, b); SBAR(); d = EX(7); SBAR();
    sN = mfma32(kA, qa, sN); SBAR();
    a = EX(8); SBAR(); p0[3] = pk2(c, d); SBAR(); b = EX(9); SBAR(); c = EX(10); SBAR(); pl[0] = pk2(a, b); SBAR(); d = EX(11); SBAR();
    oC = mfma32(vC, as_frag(p0), oC); SBAR(); __builtin_amdgcn_s_waitcnt(0xC07F); SBAR();
    a = EX(12); SBAR(); pl[1] = pk2(c, d); SBAR(); b = EX(13); SBAR(); e14 = EX(14); SBAR(); pl[2] = pk2(a, b); SBAR(); e15 = EX(15); SBAR();
  };
#undef EX
#pragma unroll 1
  for (int it = 0; it < 16; ++it) {
    const int dn = it == 14 ? -56 * KV_TILE : 4 * KV_TILE;
#pragma unroll
    for (int t = 0; t < 4; ++t) {
      const int c = t & 1, n = c ^ 1, no = t < 3 ? (t + 1) * KV_TILE : 0;
      phase(sA, oA, oB, v1[n], sB, qaB, qbB, ka[c], kb[c], v0[c],
            [&] {
              if (t == 0) asm volatile("s_bitcmp1_b32 %0, %1\n\ts_cbranch_scc0 .Lsk%=\n\ts_cmp_eq_u32 %1, 11\n\ts_cbranch_scc1 .Lsp%=\n\ts_barrier\n\ts_branch .Lsk%=\n"
                                       ".Lsp%=:\n\tds_add_u32 %2, %3\n.Lsk%=:" :: "s"(0x90A), "s"(it), "v"(pcnt), "v"(pone) : "scc", "memory");
              if (t == 3) { pk += dn; pb += dn; pv += dn; }
              kb[n] = lds_b64(pb + no); },
            [&] { ka[n] = lds_b128(pk + no); });
      phase(sB, oB, oA, v1[c], sA, qaA, qbA, ka[n], kb[n], v0[c], [&] { v0[n] = lds_b128(pv + no); }, [&] { v1[n] = lds_b128(pv + no + 704); });
    }
  }
  pl[3] = pk2(e14, e15);
  oB = mfma32(v1[1], as_frag(pl), oB);
  STAMP(2);

  float lA = h ? oA[8] : oA[12], lB = h ? oB[8] : oB[12];
  {
    f32x2 z0 = {0.f, 0.f}, z1 = z0;
#pragma unroll
    for (int i = 0; i < 7; ++i) { z0 = __builtin_elementwise_fma(f32x2{oA[2 * i], oA[2 * i + 1]}, f32x2{0.f, 0.f}, z0); z1 = __builtin_elementwise_fma(f32x2{oB[2 * i], oB[2 * i + 1]}, f32x2{0.f, 0.f}, z1); }
    z0 += z1;
    const bool ok = fmaxf(lA, lB) + (z0[0] + z0[1]) < 1.0e18f && fminf(lA, lB) > 1.0e-18f;
    if (__builtin_expect(!__all(ok), 0)) {
      const char* kvb = ws + OFF_KV + R.head * KV_HEAD;
      const bf16x4 z4 = {0, 0, 0, 0}, tA = h ? z4 : qbA, tB = h ? z4 : qbB;
      float mA = -INFINITY, mB = -INFINITY; oA = zero; oB = zero;
      auto upd = [&](f32x16 s, float& m, f32x16& o, bf16x8 w0, bf16x8 w1) {
        const float mn = fmaxf(m, rowmax16(s)), alpha = __builtin_amdgcn_exp2f(m - mn);
        m = mn; o *= alpha;
        u32x4 a, b;
#pragma unroll
        for (int i = 0; i < 4; ++i) { a[i] = exp_pk(s[2 * i] - mn, s[2 * i + 1] - mn); b[i] = exp_pk(s[8 + 2 * i] - mn, s[9 + 2 * i] - mn); }
        o = mfma32(w1, as_frag(b), mfma32(w0, as_frag(a), o));
      };
#pragma unroll 1
      for (int t = 0; t < 64; ++t) {
        const char* tb = kvb + t * KV_TILE;
        const bf16x8 k8 = *(const bf16x8*)(tb + kaO), w0 = *(const bf16x8*)(tb + vO), w1 = *(const bf16x8*)(tb + vO + 704);
        const bf16x4 k4 = *(const bf16x4*)(tb + kbO);
        upd(mfma32(k8, qaA, mfma32k8(k4, tA, zero)), mA, oA, w0, w1);
        upd(mfma32(k8, qaB, mfma32k8(k4, tB, zero)), mB, oB, w0, w1);
      }
      lA = h ? oA[8] : oA[12]; lB = h ? oB[8] : oB[12];
    }
  }
  const unsigned ob = OFF_OF + (R.qt * 16 + wid * 4) * 20480 + (R.head >> 1) * 1280;
  const unsigned lo = (r >> 4) * 20480 + (r & 15) * 16;
  auto fin = [&](const f32x16& o, float l, int blk) {
    const float inv = __builtin_amdgcn_rcpf(l);
    unsigned w[6];
#pragma unroll
    for (int u = 0; u < 6; ++u) w[u] = pk2(o[2 * u] * inv, o[2 * u + 1] * inv);
    const auto s0 = __builtin_amdgcn_permlane32_swap(w[0], w[2], false, false), s1 = __builtin_amdgcn_permlane32_swap(w[1], w[3], false, false);
    st16c(rs, lo + h * 256, ob + blk * 40960 + (R.head & 1) * 512, u32x4{s0[0], s1[0], s0[1], s1[1]});
    if (!h) st8c(rs, lo, ob + blk * 40960 + 1024 + (R.head & 1) * 8, u32x2{w[4], w[5]});
  };
  fin(oA, lA, 0); fin(oB, lB, 1);
}

#define P_DEPTH 14
#define P_PRO 4
#define P_RAMP 3
constexpr int ld_pos(int s, int pro, int ramp, int depth, int n) {
  int j = 2 * pro;
  for (int i = 0; i < s; ++i) { j += ramp; if (j > 2 * (i + 1 + depth)) j = 2 * (i + 1 + depth); if (j > 2 * n) j = 2 * n; }
  return j;
}
template <int MODE>
DI void proj(char* smem, const char* __restrict__ x, rsrc_t rs, char* __restrict__ out, const Roles& R, int wid, int lane PROF_ARG) {
  constexpr int NST = MODE ? 27 : 10, NKS = 2 * NST, DEPTH = MODE ? P_DEPTH : 10, PRO = P_PRO, RAMP = P_RAMP;
  lds_cptr L = (lds_cptr)smem;
  const int rh = wid >> 1, kp = wid & 1, t16 = lane & 15, g = lane >> 4;
  const int mf = R.rt * 4 + 2 * rh + kp, tok = mf * 16 + t16;
  const int ODD = R.ct & 1, U0 = (R.ct >> 1) * 5 + 3 * ODD, UH = (R.ct >> 1) * 5 + 2;
  const unsigned hp = OFF_HF + (mf * 20 + U0) * 1024, hh = OFF_HF + (mf * 20 + UH) * 1024 + ODD * 512;
  const unsigned fo = lane * 16, fh = (g >> 1) * 256 + t16 * 16 + (g & 1) * 8;
  const unsigned ro = tok * 1280 + U0 * 64 + g * 16, rhh = tok * 1280 + UH * 64 + ODD * 32 + g * 8;
  const unsigned ab = (MODE ? OFF_ACT : OFF_OF) + (mf * NKS + kp) * 1024, ab1 = ab + (1 - 2 * kp) * NKS * 1024;
  lds_cptr B = L + (MODE ? 0 : L_WO) + kp * 1024 + lane * 16, B1 = B + 6 * D_STAGE, B2 = B + 12 * D_STAGE;
  asm volatile("" : "+v"(B1), "+v"(B2));
  u32x4 res0, res1; u32x2 resh;
  if (MODE == 0) { res0 = *(const u32x4*)(x + ro); res1 = *(const u32x4*)(x + ro + 64); resh = *(const u32x2*)(x + rhh); }
  else { res0 = ld16c(rs, fo, hp); res1 = ld16c(rs, fo, hp + 1024); resh = ld8c(rs, fh, hh); }
  f32x4 acc[2][5];
  acc[0][0] = f32x4{bflo(res0[0]), bfhi(res0[0]), bflo(res0[1]), bfhi(res0[1])}; acc[0][1] = f32x4{bflo(res0[2]), bfhi(res0[2]), bflo(res0[3]), bfhi(res0[3])};
  acc[0][2] = f32x4{bflo(res1[0]), bfhi(res1[0]), bflo(res1[1]), bfhi(res1[1])}; acc[0][3] = f32x4{bflo(res1[2]), bfhi(res1[2]), bflo(res1[3]), bfhi(res1[3])};
  acc[0][4] = f32x4{bflo(resh[0]), bfhi(resh[0]), bflo(resh[1]), bfhi(resh[1])};
  asm volatile("" : "+v"(acc[0][0]), "+v"(acc[0][1]), "+v"(acc[0][2]), "+v"(acc[0][3]), "+v"(acc[0][4]));
  SBAR(); asm volatile("s_barrier" ::: "memory"); SBAR();
  STAMP(MODE ? 10 : 4);
  u32x4 a[DEPTH + 1][2];
  bf16x8 b[2][5];
  auto lda = [&](int j) { a[(j >> 1) % (DEPTH + 1)][j & 1] = ld16c(rs, fo, (j & 1 ? ab1 : ab) + (j >> 1) * 2048); };
  auto ldb = [&](int s, int v) { constexpr int W = 6 * D_STAGE; const int o = MODE ? ((s + 12) % 15) * D_STAGE + v * 2048 : s * 2048 + v * 20480; b[s & 1][v] = lds_b128((o < W ? B : o < 2 * W ? B1 : B2) + o % W); };
#pragma unroll
  for (int j = 0; j < 2 * PRO; ++j) lda(j);
#pragma unroll
  for (int v = 0; v < 5; ++v) ldb(0, v);
  SBAR();
  static_for<0, NST>([&](auto S) {
    constexpr int s = S.value, c = s % (DEPTH + 1);
    constexpr int j0 = ld_pos(s, PRO, RAMP, DEPTH, NST), j1 = ld_pos(s + 1, PRO, RAMP, DEPTH, NST);
    if (MODE && (s + 1) % 3 == 0 && s + 1 < 16) wait_lgkm_bar();
#pragma unroll
    for (int u = 0; u < 2; ++u)
#pragma unroll
      for (int v = 0; v < 5; ++v) {
        if (s || !u) mfma16a(acc[u][v], b[s & 1][v], a[c][u]); else mfma16z(acc[u][v], b[0][v], a[0][u]);
        if (u == 0 && s + 1 < NST) ldb(s + 1, v);
        if (u == 1 && j0 + v < j1) lda(j0 + v);
        SBAR();
      }
  });
  asm volatile("s_nop 7" ::: "memory");
  SBAR();
  STAMP(MODE ? 11 : 5);
  lds_ptr scr = (lds_ptr)smem + (MODE ? 9 * D_STAGE : L_RED) + lane * 16;
#pragma unroll
  for (int v = 0; v < 5; ++v) *(LDS_AS f32x4*)(scr + wid * 5120 + v * 1024) = acc[1][v];
  wait_lgkm_bar();
  f32x4 y[5];
#pragma unroll
  for (int v = 0; v < 5; ++v) y[v] = *(const LDS_AS f32x4*)(scr + (wid ^ 1) * 5120 + v * 1024);
  SBAR();
  unsigned o[10];
#pragma unroll
  for (int v = 0; v < 5; ++v) { y[v] += acc[0][v]; o[2 * v] = pk2(y[v][0], y[v][1]); o[2 * v + 1] = pk2(y[v][2], y[v][3]); }
  if (MODE == 0) {
    st16c(rs, fo, hp, u32x4{o[0], o[1], o[2], o[3]}); st16c(rs, fo, hp + 1024, u32x4{o[4], o[5], o[6], o[7]}); st8c(rs, fh, hh, u32x2{o[8], o[9]});
    float s0 = 0.f, s1 = 0.f, q0 = 0.f, q1 = 0.f;
    const bf2 one2 = __builtin_bit_cast(bf2, 0x3f803f80u);
#pragma unroll
    for (int i = 0; i < 5; ++i) {
      const bf2 e0 = __builtin_bit_cast(bf2, o[2 * i]), e1 = __builtin_bit_cast(bf2, o[2 * i + 1]);
      s0 = __builtin_amdgcn_fdot2_f32_bf16(e0, one2, s0, false); s1 = __builtin_amdgcn_fdot2_f32_bf16(e1, one2, s1, false);
      q0 = __builtin_amdgcn_fdot2_f32_bf16(e0, e0, q0, false); q1 = __builtin_amdgcn_fdot2_f32_bf16(e1, e1, q1, false);
    }
    const float ssum = xsum32(xsum16(s0 + s1)), ssq = xsum32(xsum16(q0 + q1));
    if (g == 0) st8c(rs, tok * 64, OFF_STAT + R.ct * 8, u32x2{__float_as_uint(ssum), __float_as_uint(ssq)});
  } else {
    *(u32x4*)(out + ro) = u32x4{o[0], o[1], o[2], o[3]}; *(u32x4*)(out + ro + 64) = u32x4{o[4], o[5], o[6], o[7]}; *(u32x2*)(out + rhh) = u32x2{o[8], o[9]};
  }
}

DI void gateup(char* smem, rsrc_t rs, const Roles& R, int wid, int lane PROF_ARG) {
  constexpr int DEPTH = 6;
  const int t16 = lane & 15, g = lane >> 4, mf = R.rg * 8 + 2 * wid;
  lds_cptr B0 = (lds_cptr)smem + lane * 16, B1 = B0 + 2 * G_STAGE, B2 = B0 + 4 * G_STAGE;
  asm volatile("" : "+v"(B1), "+v"(B2));
  const unsigned fo = lane * 16, ab = OFF_HF + mf * 20480;
  SBAR(); asm volatile("s_barrier" ::: "memory"); SBAR();
  STAMP(7);
  u32x4 a[DEPTH][2];
  bf16x8 b[14];
  f32x4 acc[2][14];
  auto lda = [&](int k, int u) { a[k % DEPTH][u] = ld16c(rs, fo, ab + XK(k) * 1024 + u * 20480); };
  auto ldb = [&](int k, int v) {
    const int slot = ((k >> 1) + 4) % 5;
    b[v] = lds_b128((slot < 2 ? B0 : slot < 4 ? B1 : B2) + (slot & 1 & (slot < 4)) * G_STAGE + (k & 1) * 1024 + v * 2048);
  };
#pragma unroll
  for (int k = 0; k < DEPTH - 1; ++k) { lda(k, 0); lda(k, 1); }
  u32x4 st0, st1;
#pragma unroll
  for (int v = 0; v < 14; ++v) ldb(0, v);
  SBAR();
  static_for<0, 20>([&](auto S) {
    constexpr int k = S.value;
    if (k & 1) { asm volatile("s_barrier" ::: "memory"); SBAR(); }
#pragma unroll
    for (int v = 0; v < 14; ++v) {
      if (k) { mfma16a(acc[0][v], b[v], a[k % DEPTH][0]); mfma16a(acc[1][v], b[v], a[k % DEPTH][1]); }
      else   { mfma16z(acc[0][v], b[v], a[0][0]); mfma16z(acc[1][v], b[v], a[0][1]); }
      if (k < 19) ldb(k + 1, v);
      if (v < 2 && k + DEPTH - 1 < 20) lda(k + DEPTH - 1, v);
      if (v == 0 && k + DEPTH - 1 == 20) st0 = ld16c(rs, t16 * 64 + g * 16, OFF_STAT + mf * 1024);
      if (v == 1 && k + DEPTH - 1 == 20) st1 = ld16c(rs, t16 * 64 + g * 16, OFF_STAT + mf * 1024 + 1024);
      SBAR();
    }
  });
  asm volatile("s_barrier\n\ts_nop 7" ::: "memory");
  SBAR();
  STAMP(8);
  float nm[2], vk[2], c1[2];
#pragma unroll
  for (int u = 0; u < 2; ++u) {
    const f32x4 s = __builtin_bit_cast(f32x4, u ? st1 : st0);
    const float mean = xsum32(xsum16(s[0] + s[2])) * (1.0f / DM);
    nm[u] = -mean; vk[u] = fmaxf(xsum32(xsum16(s[1] + s[3])) * (1.0f / DM) - mean * mean, 0.f) + LN_EPS;
    c1[u] = -LOG2E * __builtin_amdgcn_rsqf(vk[u]);
  }
  const LDS_AS f32x4* cs = (const LDS_AS f32x4*)((lds_cptr)smem + L_CS) + g;
  auto swi = [&](int u, int p) {
    const f32x4 gt = acc[u][2 * p] + cs[8 * p] * nm[u], up = acc[u][2 * p + 1] + cs[8 * p + 4] * nm[u], e = gt * c1[u], n = gt * up;
    f32x4 d;
#pragma unroll
    for (int i = 0; i < 4; ++i) d[i] = __builtin_amdgcn_exp2f(e[i]);
    d = d * vk[u] + vk[u];
#pragma unroll
    for (int i = 0; i < 4; ++i) d[i] = __builtin_amdgcn_rcpf(d[i]);
    d *= n;
    return u32x2{pk2(d[0], d[1]), pk2(d[2], d[3])};
  };
  const unsigned ob = OFF_ACT + mf * 54 * 1024;
#pragma unroll
  for (int i = 0; i < 3; ++i)
#pragma unroll
    for (int u = 0; u < 2; ++u) {
      const u32x2 pa = swi(u, 2 * i), pb = swi(u, 2 * i + 1);
      st16c(rs, fo, ob + (u * 54 + R.UB + i) * 1024, u32x4{pa[0], pa[1], pb[0], pb[1]});
    }
  if ((R.cg & 7) < 6) {
    const u32x2 pa = swi(0, 6), pb = swi(1, 6);
    const auto s0 = __builtin_amdgcn_permlane16_swap(pa[0], pb[0], false, false), s1 = __builtin_amdgcn_permlane16_swap(pa[1], pb[1], false, false);
    st16c(rs, (g & 1) * 54 * 1024 + (g >> 1) * 256 + t16 * 16, ob + R.UL * 1024 + (R.cg & 1) * 512, u32x4{s0[0], s1[0], s0[1], s1[1]});
  }
}

extern "C" __global__ __launch_bounds__(512, 2) void k_main(const char* __restrict__ x, const char* __restrict__ wo, const char* __restrict__ wg,
                                                            const char* __restrict__ wu, const char* __restrict__ wd, char* __restrict__ ws,
                                                            char* __restrict__ out) {
  __shared__ __attribute__((aligned(16))) char smem[163840];
  const int wid = __builtin_amdgcn_readfirstlane(threadIdx.x >> 6);
  PROF_DECL STAMP(0);
  const Roles R(blockIdx.x);
  const rsrc_t rs = __builtin_amdgcn_make_buffer_rsrc(ws, 0, (int)WS_BYTES, 0x00020000);
  if (wid >= 4) {
    helper_waves(smem, wo, wg, wu, wd, ws, rs, R, wid - 4, lane_id() PROF_PASS);
  } else {
    attention(smem, ws, rs, R, wid, lane_id() PROF_PASS);
    STX(1); wait_vm<0>(); STX(2);
    wait_vm_bar<0>();
    STAMP(3);
    proj<0>(smem, x, rs, out, R, wid, lane_id() PROF_PASS);
    STX(3); wait_vm<0>(); STX(4);
    wait_vm_bar<0>();
    STAMP(6);
    gateup(smem, rs, R, wid, lane_id() PROF_PASS);
    STX(5); wait_vm<0>(); STX(6);
    wait_vm_bar<0>();
    STAMP(9);
    proj<1>(smem, x, rs, out, R, wid, lane_id() PROF_PASS);
  }
  PROF_END(1);
}
extern "C" __global__ void k_end() {}

__global__ void k_report(char* ws) {
  __shared__ unsigned long long smin[2], smax[2], ssum, scnt;
  const int b = threadIdx.x;
  if (b < 2) { smin[b] = ~0ull; smax[b] = 0; ssum = 0; scnt = 0; }
  __syncthreads();
  const unsigned long long* pr = (const unsigned long long*)(ws + (128ull << 20));
  const bool idle = ((b & 1) * 8 + (b >> 5)) >= 15;
  for (int k = 0; k < 2; ++k) {
    if (k == 0 && idle) continue;
    const unsigned long long* t = pr + ((k * 256 + b) * 8) * 16;
    atomicMin(&smin[k], t[14]); atomicMax(&smax[k], t[15]);
  }
  __syncthreads();
  if (MEAS >= 10) {
    const int k = MEAS >= 30 && MEAS < 50 ? 0 : 1, w = MEAS >= 50 ? 4 : 0, i = (MEAS - 10) % 20;
    if (!(k == 0 && idle)) { atomicAdd(&ssum, pr[((k * 256 + b) * 8 + w) * 16 + i] - smin[0]); atomicAdd(&scnt, 1ull); }
  }
  __syncthreads();
  long long x = 0;
  if (MEAS == 1) x = smax[0] - smin[0];
  if (MEAS == 2) x = smin[1] - smin[0];
  if (MEAS == 3) x = smax[1] - smin[0];
  if (MEAS >= 10) x = ssum / scnt;
  if (x < 0) x = 0; if (x > 10000) x = 10000;
  unsigned long long t0, t1;
  asm volatile("s_memrealtime %0\n\ts_waitcnt lgkmcnt(0)" : "=s"(t0) :: "memory");
  do { asm volatile("s_memrealtime %0\n\ts_waitcnt lgkmcnt(0)" : "=s"(t1) :: "memory"); } while ((long long)(t1 - t0) < FACT * x);
}
extern "C" void kernel_launch(void* const* d_in, const int* in_sizes, int n_in, void* d_out, int out_size, void* d_ws, size_t ws_size,
                              hipStream_t stream) {
  const char* x = (const char*)d_in[0]; const char* wqkv = (const char*)d_in[1]; const char* wo = (const char*)d_in[2];
  const char* wg = (const char*)d_in[4]; const char* wu = (const char*)d_in[5]; const char* wd = (const char*)d_in[6];
  char* ws = (char*)d_ws;
  k_qkv<<<256, 512, 0, stream>>>(x, wqkv, ws);
  k_main<<<256, 512, 0, stream>>>(x, wo, wg, wu, wd, ws, (char*)d_out);
  k_report<<<1, 256, 0, stream>>>(ws);
}
```

```cpp
#include <hip/hip_runtime.h>
#include <hip/hip_bf16.h>
#include <stdint.h>

typedef __attribute__((ext_vector_type(8))) short bf16x8;
typedef __attribute__((ext_vector_type(4))) short bf16x4;
typedef __attribute__((ext_vector_type(2))) float f32x2;
typedef __attribute__((ext_vector_type(4))) float f32x4;
typedef __attribute__((ext_vector_type(16))) float f32x16;
typedef __attribute__((ext_vector_type(4))) unsigned u32x4;
typedef __attribute__((ext_vector_type(2))) unsigned u32x2;
typedef __attribute__((ext_vector_type(2))) __bf16 bf2;
typedef __attribute__((address_space(3))) const char* lds_cptr;
typedef __attribute__((address_space(3))) char* lds_ptr;
#define LDS_AS __attribute__((address_space(3)))
#define DI __device__ __forceinline__

constexpr int SEQ = 2048, DM = 640, NH = 32, DH = 20, FF = 1728;
constexpr float LN_EPS = 1e-5f;
constexpr float LOG2E = 1.4426950408889634f;
constexpr float QSCALE = 0.22360679774997896f * LOG2E;

constexpr int Q_TILE = 1280;
constexpr int Q_HEAD = 64 * Q_TILE;
constexpr int KV_TILE = 2688;
constexpr int KV_HEAD = 64 * KV_TILE;
constexpr unsigned OFF_Q    = 0;
constexpr unsigned OFF_KV   = OFF_Q + NH * Q_HEAD;
constexpr unsigned OFF_OF   = OFF_KV + NH * KV_HEAD;
constexpr unsigned OFF_HF   = OFF_OF + SEQ * DM * 2;
constexpr unsigned OFF_ACT  = OFF_HF + SEQ * DM * 2;
constexpr unsigned OFF_WOF  = OFF_ACT + SEQ * FF * 2;
constexpr unsigned OFF_WGUF = OFF_WOF + DM * DM * 2;
constexpr unsigned OFF_WDF  = OFF_WGUF + DM * FF * 4;
constexpr unsigned OFF_STAT = OFF_WDF + FF * DM * 2;
constexpr unsigned OFF_CSUM = OFF_STAT + SEQ * 64;
constexpr unsigned OFF_FLAG = OFF_CSUM + 20 * 216 * 64;
constexpr unsigned FL_O = OFF_FLAG, FL_H = FL_O + 64 * 256, FL_ACT = FL_H + 128 * 256, FL_W = FL_ACT + 128 * 256;
constexpr int N_FLAGS = 384;
constexpr unsigned WS_BYTES = OFF_FLAG + N_FLAGS * 256;

#define PROF_DECL
#define STAMP(i)
#define PROF_END(k)
#define PROF_ARG
#define PROF_PASS
#define STXN 0
#define STX(n) do { if (n == STXN) STAMP(12); } while (0)
template <int N> DI void glds(unsigned voff, const void* sbase, unsigned lds, int imm) {
#define GL(o) "\n\tglobal_load_lds_dwordx4 %0, %1 offset:" #o
  if (N == 1) asm volatile("s_add_u32 m0, %2, %3\n\ts_nop 0" GL(0) :: "v"(voff), "s"(sbase), "s"(lds), "ns"(imm) : "memory", "scc");
  if (N == 2) asm volatile("s_add_u32 m0, %2, %3\n\ts_nop 0" GL(0) GL(1024) :: "v"(voff), "s"(sbase), "s"(lds), "ns"(imm) : "memory", "scc");
  if (N == 4) asm volatile("s_add_u32 m0, %2, %3\n\ts_nop 0" GL(0) GL(1024) GL(2048) GL(3072) :: "v"(voff), "s"(sbase), "s"(lds), "ns"(imm) : "memory", "scc");
#undef GL
}
#define CODE_TOUCH(first, last, part, sink) do { unsigned long long pc_; int a_, b_; \
  asm volatile("s_getpc_b64 %0\n.Lct%=:\n\ts_mov_b32 %1, " #first "-.Lct%=\n\ts_mov_b32 %2, " #last "-.Lct%=" : "=s"(pc_), "=s"(a_), "=s"(b_)); \
  const int o_ = min(max(a_, 0) + lane * 128 + (part) * 8192, max(b_, 0)); \
  asm volatile("global_load_dword %0, %1, %2" : "=v"(sink) : "v"(o_), "s"(pc_) : "memory"); } while (0)
#define CODE_DONE(sink) asm volatile("s_waitcnt vmcnt(0)" :: "v"(sink) : "memory")
typedef __attribute__((address_space(1))) unsigned gu32;
typedef __amdgpu_buffer_rsrc_t rsrc_t;
#define LDAUX 16
#define XK(k) (k)
DI u32x4 ld16c(rsrc_t rs, unsigned vo, unsigned so) { return __builtin_amdgcn_raw_buffer_load_b128(rs, vo, so, LDAUX); }
DI u32x2 ld8c(rsrc_t rs, unsigned vo, unsigned so) { return __builtin_amdgcn_raw_buffer_load_b64(rs, vo, so, 16); }
DI void st16c(rsrc_t rs, unsigned vo, unsigned so, u32x4 v) { __builtin_amdgcn_raw_buffer_store_b128(v, rs, vo + so, 0, 16); }
DI void st8c(rsrc_t rs, unsigned vo, unsigned so, u32x2 v) { __builtin_amdgcn_raw_buffer_store_b64(v, rs, vo, so, 16); }
DI void arrive(char* ws, unsigned off, int lane, int stride) {
  if (lane < 8) __hip_atomic_fetch_add((gu32*)(ws + off + lane * stride), 1u, __ATOMIC_RELAXED, __HIP_MEMORY_SCOPE_AGENT);
}
DI void await8(char* ws, unsigned off, int lane, unsigned want) {
#pragma unroll 1
  for (int n = 0; n < (1 << 16); ++n) {
    const unsigned v = __hip_atomic_load((gu32*)(ws + off + (lane & 7) * 256), __ATOMIC_RELAXED, __HIP_MEMORY_SCOPE_AGENT);
    if (__all(v >= want)) break;
    __builtin_amdgcn_s_sleep(1);
  }
}
DI void await(char* ws, unsigned off, unsigned want) {
#pragma unroll 1
  for (int n = 0; n < (1 << 16); ++n) {
    const unsigned v = __hip_atomic_load((gu32*)(ws + off), __ATOMIC_RELAXED, __HIP_MEMORY_SCOPE_AGENT);
    if ((unsigned)__builtin_amdgcn_readfirstlane(v) >= want) break;
    __builtin_amdgcn_s_sleep(1);
  }
  __builtin_amdgcn_fence(__ATOMIC_ACQUIRE, "wavefront");
}
template <int N> DI void wait_vm_bar() { asm volatile("s_waitcnt vmcnt(%0)\n\ts_barrier" :: "n"(N) : "memory"); }
template <int N> DI void wait_vm() { asm volatile("s_waitcnt vmcnt(%0)" :: "n"(N) : "memory"); }
#define SBAR() __builtin_amdgcn_sched_barrier(0)
#define SGB(mask, n) __builtin_amdgcn_sched_group_barrier(mask, n, 0)
DI void wait_lgkm_bar() { SBAR(); __builtin_amdgcn_s_waitcnt(0xC07F); asm volatile("s_barrier" ::: "memory"); SBAR(); }
#define GEMM_OFF 0
template <int I> struct IC { static constexpr int value = I; };
template <int B, int E, class F> DI void static_for(F&& f) { if constexpr (B < E) { f(IC<B>{}); static_for<B + 1, E>(f); } }
DI bf16x8 lds_b128(lds_cptr p) { return *(const LDS_AS bf16x8*)p; }
DI bf16x4 lds_b64(lds_cptr p) { return *(const LDS_AS bf16x4*)p; }
DI bf16x4 lds_tr(lds_cptr p) { return __builtin_amdgcn_ds_read_tr16_b64_v4i16((LDS_AS bf16x4*)p); }
DI f32x4 mfma16(bf16x8 a, bf16x8 b, f32x4 c) { return __builtin_amdgcn_mfma_f32_16x16x32_bf16(a, b, c, 0, 0, 0); }
DI void mfma16a(f32x4& c, bf16x8 a, u32x4 b) { asm volatile("v_mfma_f32_16x16x32_bf16 %0, %1, %2, %0" : "+v"(c) : "v"(a), "v"(b)); }
DI void mfma16z(f32x4& c, bf16x8 a, u32x4 b) { asm volatile("v_mfma_f32_16x16x32_bf16 %0, %1, %2, 0" : "=&v"(c) : "v"(a), "v"(b)); }
DI f32x16 mfma32(bf16x8 a, bf16x8 b, f32x16 c) { return __builtin_amdgcn_mfma_f32_32x32x16_bf16(a, b, c, 0, 0, 0); }
DI f32x16 mfma32k8(bf16x4 a, bf16x4 b, f32x16 c) { return __builtin_amdgcn_mfma_f32_32x32x8bf16_1k(a, b, c, 0, 0, 0); }
DI unsigned pk2(float lo, float hi) { f32x2 v = {lo, hi}; return __builtin_bit_cast(unsigned, __builtin_convertvector(v, bf2)); }
DI float bflo(unsigned u) { return __uint_as_float(u << 16); }
DI float bfhi(unsigned u) { return __uint_as_float(u & 0xffff0000u); }
DI int div20(int c) { return (c * 3277) >> 16; }
DI float xsum32(float v) { auto r = __builtin_amdgcn_permlane32_swap(__float_as_uint(v), __float_as_uint(v), false, false); return __uint_as_float(r[0]) + __uint_as_float(r[1]); }
DI float xsum16(float v) { auto r = __builtin_amdgcn_permlane16_swap(__float_as_uint(v), __float_as_uint(v), false, false); return __uint_as_float(r[0]) + __uint_as_float(r[1]); }
constexpr bf16x8 ONES8 = {0x3f80, 0x3f80, 0x3f80, 0x3f80, 0x3f80, 0x3f80, 0x3f80, 0x3f80};

template <int MODE>
DI void qkv_compute(lds_cptr L, lds_ptr scr, int wid, int lane, int row0, int col0, char* __restrict__ ws PROF_ARG) {
  const int wr = wid >> 1, wc = wid & 1, r16 = lane & 15, g = lane >> 4, q = r16 >> 2, p = lane & 3;
  const int f = q | ((g & 1) << 2);
  const int rp = MODE == 2 ? 16 * (r16 >> 3) + (r16 & 7) : r16;
  constexpr int RSTEP = MODE == 2 ? 8 : 16;
  int aoff[2][2], boff[4];
#pragma unroll
  for (int e = 0; e < 2; ++e)
#pragma unroll
    for (int ks = 0; ks < 2; ++ks) { const int r = rp + e * RSTEP; aoff[e][ks] = (64 * wr + r) * 128 + (((4 * ks + g) ^ ((r >> 1) & 7) ^ ((r >> 2) & 4)) << 4); }
#pragma unroll
  for (int jj = 0; jj < 4; ++jj) { const int j = (jj + 2 * wr) & 3; boff[jj] = 16384 + (8 * g + q) * 256 + ((((4 * wc + j) ^ f) * 2 + (p >> 1)) << 4) + (p & 1) * 8; }
  const int pr0 = wc * 4096, pr1 = 4096 - pr0;

  f32x4 acc[4][4], rs[2], gr[2], cs[2];
#pragma unroll
  for (int i = 0; i < 4; ++i)
#pragma unroll
    for (int j = 0; j < 4; ++j) acc[i][j] = f32x4{0.f, 0.f, 0.f, 0.f};
#pragma unroll
  for (int i = 0; i < 2; ++i) { rs[i] = f32x4{0.f, 0.f, 0.f, 0.f}; gr[i] = rs[i]; cs[i] = rs[i]; }
  bf16x8 a0[4], b0[4], a1[4], b1[4];

  auto rd = [&](bf16x8 (&a)[4], bf16x8 (&b)[4], int slot, int ks) {
    if (GEMM_OFF && slot + ks > 0) return;
    lds_cptr S = L + slot;
#pragma unroll
    for (int jj = 0; jj < 4; ++jj) {
      bf16x4 lo = lds_tr(S + boff[jj] + ks * 8192), hi = lds_tr(S + boff[jj] + ks * 8192 + 1024);
      b[jj] = __builtin_shufflevector(lo, hi, 0, 1, 2, 3, 4, 5, 6, 7);
      a[jj] = lds_b128(S + aoff[jj & 1][ks] + (jj < 2 ? pr0 : pr1));
    }
  };
  auto mm = [&](bf16x8 (&a)[4], bf16x8 (&b)[4]) {
    if (GEMM_OFF) return;
#pragma unroll
    for (int ii = 0; ii < 4; ++ii)
#pragma unroll
      for (int jj = 0; jj < 4; ++jj)
        acc[ii][jj] = (MODE == 2) ? mfma16(a[ii], b[jj], acc[ii][jj]) : mfma16(b[jj], a[ii], acc[ii][jj]);
#pragma unroll
    for (int u = 0; u < 2; ++u) {
      rs[u] = mfma16(a[u], ONES8, rs[u]);
      gr[u] = mfma16(a[u], a[u], gr[u]);
      cs[u] = mfma16(ONES8, b[u], cs[u]);
    }
  };
  auto ilv = [&] {
#pragma unroll
    for (int i = 0; i < 12; ++i) { SGB(0x8, 1); SGB(0x100, 1); }
    SGB(0x8, 10);
    SBAR();
  };

  wait_lgkm_bar();
  STAMP(1);
  rd(a0, b0, 0, 0);
  SBAR();
#pragma unroll 1
  for (int kt = 0; kt < 9; ++kt) {
    rd(a1, b1, (kt & 3) * 32768, 1); mm(a0, b0); ilv();
    wait_lgkm_bar();
    rd(a0, b0, ((kt + 1) & 3) * 32768, 0); mm(a1, b1); ilv();
  }
  rd(a1, b1, 32768, 1); mm(a0, b0); ilv();
  mm(a1, b1);
  SBAR();
  STAMP(2);
  LDS_AS float* rsum = (LDS_AS float*)scr; LDS_AS float* rsq = rsum + 128; LDS_AS float* csum = rsum + 256;
#pragma unroll
  for (int u = 0; u < 2; ++u) {
    const int rb = 64 * wr + 32 * wc + 16 * u;
    if (r16 == 0) *(LDS_AS f32x4*)(rsum + rb + 4 * g) = rs[u];
    if (q == g) { const float d = p == 0 ? gr[u][0] : p == 1 ? gr[u][1] : p == 2 ? gr[u][2] : gr[u][3]; rsq[rb + r16] = d; }
    if (g == 0) csum[64 * wc + 32 * wr + 16 * u + r16] = cs[u][0];
  }
  constexpr float invD = 1.0f / DM;
  if (MODE != 2) {
    constexpr int TS = MODE == 0 ? Q_TILE : KV_TILE, HS = MODE == 0 ? Q_HEAD : KV_HEAD;
    unsigned off[4][2];
#pragma unroll
    for (int jj = 0; jj < 4; ++jj) {
      const int ck = col0 - MODE * DM + 64 * wc + 16 * ((jj + 2 * wr) & 3) + 4 * g, head = div20(ck), d = ck - head * DH;
      off[jj][0] = head * HS + (d < 16 ? (d >> 3) * 512 + (d & 7) * 2 + r16 * 16 : 1024 + r16 * 8);
      off[jj][1] = off[jj][0] + (d < 16 ? 256 : 128);
    }
    char* base = ws + (MODE == 0 ? OFF_Q : OFF_KV) + ((row0 >> 5) + 2 * wr) * TS;
    wait_lgkm_bar();
    f32x4 c4[4];
#pragma unroll
    for (int jj = 0; jj < 4; ++jj) c4[jj] = *(const LDS_AS f32x4*)(csum + 64 * wc + 16 * ((jj + 2 * wr) & 3) + 4 * g);
#pragma unroll
    for (int ii = 0; ii < 4; ++ii) {
      const int half = ((ii >> 1) + wc) & 1, rl = 64 * wr + 32 * half + 16 * (ii & 1) + r16;
      const float mean = rsum[rl] * invD, var = fmaxf(rsq[rl] * invD - mean * mean, 0.f);
      const float rstd = __builtin_amdgcn_rsqf(var + LN_EPS) * (MODE == 0 ? QSCALE : 1.0f), nb = -mean * rstd;
      char* tb = base + half * TS;
#pragma unroll
      for (int jj = 0; jj < 4; ++jj) {
        const f32x4 v = acc[ii][jj] * rstd + c4[jj] * nb;
        *(u32x2*)(tb + off[jj][ii & 1]) = u32x2{pk2(v[0], v[1]), pk2(v[2], v[3])};
      }
    }
  } else {
    unsigned off[4];
#pragma unroll
    for (int jj = 0; jj < 4; ++jj) {
      const int cv = col0 - 2 * DM + 64 * wc + 16 * ((jj + 2 * wr) & 3) + r16, head = div20(cv), d = cv - head * DH;
      off[jj] = head * KV_HEAD + 1280 + g * 352 + d * 16;
    }
    char* base = ws + OFF_KV + ((row0 >> 5) + 2 * wr) * KV_TILE;
    { const int head = div20(col0 - 2 * DM + 19) + (lane >> 3), t = lane & 7;
      if (head * DH < col0 - 2 * DM + 128) *(u32x4*)(base + head * KV_HEAD + (t >> 2) * KV_TILE + 1280 + (t & 3) * 352 + 320 + wc * 16) = wc ? u32x4{0u, 0u, 0u, 0u} : __builtin_bit_cast(u32x4, ONES8); }
    wait_lgkm_bar();
    float c1[4];
#pragma unroll
    for (int jj = 0; jj < 4; ++jj) c1[jj] = csum[64 * wc + 16 * ((jj + 2 * wr) & 3) + r16];
#pragma unroll
    for (int ip = 0; ip < 2; ++ip) {
      const int half = (ip + wc) & 1;
      f32x4 rstd[2], nb[2];
#pragma unroll
      for (int e = 0; e < 2; ++e) {
        const int rl = 64 * wr + 32 * half + 16 * e + 4 * g;
        const f32x4 mean = *(const LDS_AS f32x4*)(rsum + rl) * invD, q4 = *(const LDS_AS f32x4*)(rsq + rl);
#pragma unroll
        for (int i = 0; i < 4; ++i) rstd[e][i] = __builtin_amdgcn_rsqf(fmaxf(q4[i] * invD - mean[i] * mean[i], 0.f) + LN_EPS);
        nb[e] = -mean * rstd[e];
      }
      char* tb = base + half * KV_TILE;
#pragma unroll
      for (int jj = 0; jj < 4; ++jj) {
        const f32x4 v0 = acc[2 * ip][jj] * rstd[0] + nb[0] * c1[jj], v1 = acc[2 * ip + 1][jj] * rstd[1] + nb[1] * c1[jj];
        *(u32x4*)(tb + off[jj]) = u32x4{pk2(v0[0], v0[1]), pk2(v0[2], v0[3]), pk2(v1[0], v1[1]), pk2(v1[2], v1[3])};
      }
    }
  }
}

extern "C" __global__ __launch_bounds__(512, 2) void k_qkv(const char* __restrict__ x, const char* __restrict__ w, char* __restrict__ ws) {
  __shared__ __attribute__((aligned(16))) char smem[4 * 32768 + 2048];
  const int bid = blockIdx.x, xcd = bid & 7, idx = bid >> 3;
  const int rt = (xcd >> 1) * 4 + (idx & 3), ct = (xcd & 1) * 8 + (idx >> 2);
  if (ct >= 15) {
    if (rt == 0 && threadIdx.x < N_FLAGS) *(unsigned*)(ws + OFF_FLAG + threadIdx.x * 256) = 0u;
    return;
  }
  PROF_DECL STAMP(0);
  const int tid = threadIdx.x, lane = tid & 63, wid = __builtin_amdgcn_readfirstlane(tid >> 6);
  if (wid >= 4) {
    const int lw = wid - 4;
    const unsigned lds0 = (unsigned)(uintptr_t)smem + lw * 1024;
    const int ra = 8 * lw + (lane >> 3), rb = 4 * lw + (lane >> 4), f = (rb & 3) | ((rb >> 1) & 4);
    const unsigned voA = ra * 1280 + (((lane & 7) ^ ((ra >> 1) & 7) ^ ((ra >> 2) & 4)) << 4), voB = rb * 3840 + (((lane & 15) ^ (f << 1)) << 4);
    const char* pa = x + (size_t)rt * 128 * 1280; const char* pb = w + ct * 256;
    auto issue = [&](int st) {
      const unsigned base = lds0 + (st & 3) * 32768;
#pragma unroll
      for (int j = 0; j < 4; ++j) { glds<1>(voA + j * 32 * 1280, pa + st * 128, base, j * 4096); glds<1>(voB + j * 16 * 3840, pb + st * 64 * 3840, base, 16384 + j * 4096); }
    };
    STAMP(1); issue(0); issue(1); STAMP(2);
    wait_vm_bar<8>(); STAMP(3);
    issue(2); issue(3);
#pragma unroll 1
    for (int s = 1; s < 7; ++s) { wait_vm_bar<16>(); issue(s + 3); }
    wait_vm_bar<16>(); wait_vm_bar<8>(); wait_vm_bar<0>();
    PROF_END(0);
    return;
  }
  lds_cptr L = (lds_cptr)smem; lds_ptr scr = (lds_ptr)smem + 4 * 32768;
  unsigned cs0, cs1; CODE_TOUCH(k_qkv, k_end, wid, cs0); CODE_TOUCH(k_qkv, k_end, wid + 4, cs1);
  if (ct < 5) qkv_compute<0>(L, scr, wid, lane, rt * 128, ct * 128, ws PROF_PASS);
  else if (ct < 10) qkv_compute<1>(L, scr, wid, lane, rt * 128, ct * 128, ws PROF_PASS);
  else qkv_compute<2>(L, scr, wid, lane, rt * 128, ct * 128, ws PROF_PASS);
  CODE_DONE(cs0); CODE_DONE(cs1);
  PROF_END(0);
}

constexpr int L_STG = 48 * KV_TILE;
constexpr int L_WO = 4 * KV_TILE;
constexpr int G_STAGE = 28 * 1024;
constexpr int L_RED = 5 * G_STAGE;
constexpr int D_STAGE = 10 * 1024;
constexpr int L_CS = 15 * D_STAGE;
constexpr int L_CNT = 163840 - 16;

DI float rowmax16(const f32x16& s) {
  float a = fmaxf(fmaxf(s[0], s[1]), s[2]), b = fmaxf(fmaxf(s[3], s[4]), s[5]);
  a = fmaxf(fmaxf(a, s[6]), s[7]); b = fmaxf(fmaxf(b, s[8]), s[9]);
  a = fmaxf(fmaxf(a, s[10]), s[11]); b = fmaxf(fmaxf(b, s[12]), s[13]);
  a = fmaxf(fmaxf(a, s[14]), s[15]);
  const float m = fmaxf(a, b);
  auto rr = __builtin_amdgcn_permlane32_swap(__float_as_uint(m), __float_as_uint(m), false, false);
  return fmaxf(__uint_as_float(rr[0]), __uint_as_float(rr[1]));
}
DI unsigned exp_pk(float a, float b) { return pk2(__builtin_amdgcn_exp2f(a), __builtin_amdgcn_exp2f(b)); }
DI int lane_id() { int v; asm volatile("v_mbcnt_lo_u32_b32 %0, -1, 0\n\tv_mbcnt_hi_u32_b32 %0, -1, %0" : "=v"(v)); __builtin_assume(v >= 0 && v < 64); return v; }
DI bf16x8 as_frag(u32x4 v) { return __builtin_bit_cast(bf16x8, v); }

template <int KIND>
DI void retile_issue(const char* __restrict__ w0, const char* __restrict__ w1, int tile, int ks, int lane, unsigned stg) {
  if (KIND == 0) {
    const unsigned vo = (ks * 32 + (lane >> 3)) * (FF * 2) + tile * 128 + (lane & 7) * 16;
#pragma unroll
    for (int j = 0; j < 4; ++j) { glds<1>(vo + j * 8 * (FF * 2), w0, stg, j * 1024); glds<1>(vo + j * 8 * (FF * 2), w1, stg, 4096 + j * 1024); }
  } else {
#pragma unroll
    for (int j = 0; j < 8; ++j) {
      int k = ks * 32 + 4 * j + (lane >> 4);
      if (KIND == 2) { const int pp = div20(k) >> 1, e = k - 40 * pp; k = 40 * pp + (e < 16 ? e : e < 32 ? e + 4 : e < 36 ? e - 16 : e); }
      glds<1>(k * (DM * 2) + tile * 256 + (lane & 15) * 16, w0, stg, j * 1024);
    }
  }
}
template <int KIND>
DI void retile_store(rsrc_t rs, lds_cptr S, int tile, int ks, int lane) {
  const int g = lane >> 4, q = (lane & 15) >> 2, p = lane & 3;
#pragma unroll
  for (int f = 0; f < 8; ++f) {
    lds_cptr a; int hi; unsigned dst;
    if (KIND == 0) {
      const int U = 2 * tile + (f >> 2), j = (f >> 1) & 1;
      a = S + (f & 1) * 4096 + (8 * g + q) * 128 + (32 * (f >> 2) + ((U >= 27 ? U - 27 : U) >= 24 ? 16 * j + 4 * p : 8 * p + 4 * j)) * 2; hi = 512;
      dst = OFF_WGUF + ((8 * tile + f) * 20 + ks) * 1024;
    } else {
      const int U = 4 * tile + (f >> 1), j = f & 1;
      a = S + (8 * g + q) * 256 + (32 * (f >> 1) + (U % 5 == 2 ? 16 * j + 4 * p : 8 * p + 4 * j)) * 2; hi = 1024;
      const int n10 = (2 * U + j) % 10, nf = 2 * U + j + (n10 == 5 ? 4 : n10 > 5 ? -1 : 0);
      dst = (KIND == 1 ? OFF_WDF : OFF_WOF) + (nf * (KIND == 1 ? 54 : 20) + ks) * 1024;
    }
    const bf16x8 fr = __builtin_shufflevector(lds_tr(a), lds_tr(a + hi), 0, 1, 2, 3, 4, 5, 6, 7);
    st16c(rs, lane * 16, dst, __builtin_bit_cast(u32x4, fr));
    if (KIND == 0) {
      const f32x4 c = mfma16(fr, ONES8, f32x4{0.f, 0.f, 0.f, 0.f});
      if ((lane & 15) == 0) st16c(rs, g * 16, OFF_CSUM + (ks * 216 + 8 * tile + f) * 64, __builtin_bit_cast(u32x4, c));
    }
  }
}

struct Roles {
  int head, qt, rt, ct, rg, cg, UB, UL;
  DI Roles(int bid) {
    const int xcd = bid & 7, idx = bid >> 3;
    head = xcd * 4 + (idx >> 3); qt = idx & 7;
    rt = (xcd >> 1) * 8 + (idx & 7); ct = (xcd & 1) * 4 + (idx >> 3);
    rg = (xcd >> 2) * 8 + (idx & 7); cg = (xcd & 3) * 4 + (idx >> 3);
    UB = (cg >> 3) * 27 + 3 * (cg & 7); UL = (cg >> 3) * 27 + 24 + min((cg & 7) >> 1, 2);
  }
  DI int gu_nf(int f) const { return f < 12 ? 4 * UB + f : 4 * UL + 2 * (cg & 1) + f - 12; }
};

DI void helper_waves(char* smem, const char* __restrict__ wo, const char* __restrict__ wg, const char* __restrict__ wu, const char* __restrict__ wd,
                     char* __restrict__ ws, rsrc_t rs, const Roles& R, int hw, int lane PROF_ARG) {
  const int bid = blockIdx.x;
  const unsigned lds0 = (unsigned)(uintptr_t)smem, vo = lane * 16;
  LDS_AS unsigned* cnt = (LDS_AS unsigned*)((lds_ptr)smem + L_CNT);
  const char* kv = ws + OFF_KV + R.head * KV_HEAD;
  const unsigned vk = vo + hw * 4096, lk = lds0 + hw * 4096;
  auto runs = [&](int i0, int i1) {
#pragma unroll
    for (int i = i0; i < i1; ++i) glds<4>(vk, kv + i * 16384, lk, i * 16384);
  };
  runs(0, 3);
  if (hw == 0) { cnt[0] = 0u; cnt[1] = 0u; }
  STAMP(1);
  asm volatile("s_waitcnt vmcnt(8) lgkmcnt(0)\n\ts_barrier" ::: "memory");
  runs(3, 6);
  const int job = bid * 4 + hw;
  const unsigned stg = lds0 + L_STG + hw * 8192;
  int tile = 0, ks = 0;
  if (job < 540)      { ks = job / 27; tile = job - ks * 27; retile_issue<0>(wg, wu, tile, ks, lane, stg); }
  else if (job < 810) { ks = (job - 540) / 5; tile = job - 540 - ks * 5; retile_issue<1>(wd, wd, tile, ks, lane, stg); }
  else                { ks = min(job - 810, 99) / 5; tile = min(job - 810, 99) - ks * 5; retile_issue<2>(wo, wo, tile, ks, lane, stg); }
  wait_vm_bar<20>();
  runs(6, 7);
  if (hw < 3) glds<4>(vk, kv + 7 * 16384, lk, 7 * 16384); else { glds<2>(vk, kv + 7 * 16384, lk, 7 * 16384); glds<2>(vk, kv + 7 * 16384, lk, 7 * 16384); }
  wait_vm_bar<16>();
  if (hw < 3) glds<4>(vk, kv + 60 * KV_TILE, lk, 0);
  if (hw < 3) wait_vm<4>(); else wait_vm<0>();
  STAMP(2);
  lds_cptr S = (lds_cptr)smem + L_STG + hw * 8192;
  if (job < 540) retile_store<0>(rs, S, tile, ks, lane);
  else if (job < 810) retile_store<1>(rs, S, tile, ks, lane);
  else if (job < 910) retile_store<2>(rs, S, tile, ks, lane);
  asm volatile("s_waitcnt lgkmcnt(0)" ::: "memory");
  glds<4>(vo + hw * 8192, kv + L_STG, stg, 0); glds<4>(vo + hw * 8192, kv + L_STG + 4096, stg, 4096);
  wait_vm<0>();
  STAMP(3);
  unsigned old = 0;
  if (lane == 0) old = __hip_atomic_fetch_add(cnt, 1u, __ATOMIC_RELAXED, __HIP_MEMORY_SCOPE_WORKGROUP);
  if (__builtin_amdgcn_readfirstlane(old) == 3) arrive(ws, FL_W + (bid & 7) * 256, lane, 2048);
  asm volatile("s_barrier" ::: "memory");
  if (hw == 0) {
    await8(ws, FL_W + ((bid >> 3) & 7) * 2048, lane, 32);
    __builtin_amdgcn_fence(__ATOMIC_ACQUIRE, "agent");
    wait_vm<0>();
    if (lane == 0) __hip_atomic_fetch_add(cnt + 1, 1u, __ATOMIC_RELAXED, __HIP_MEMORY_SCOPE_WORKGROUP);
  }
  STAMP(4);
  while (__builtin_amdgcn_readfirstlane(__hip_atomic_load(cnt + 1, __ATOMIC_RELAXED, __HIP_MEMORY_SCOPE_WORKGROUP)) < 5) __builtin_amdgcn_s_sleep(4);
  unsigned tw[5];
  CODE_TOUCH(k_main, k_end, hw, tw[2]); CODE_TOUCH(k_main, k_end, hw + 4, tw[3]);
  {
    const char* src = ws + OFF_WOF + R.ct * 102400;
#pragma unroll
    for (int i = 0; i < 6; ++i) glds<4>(vk, src + i * 16384, lk, L_WO + i * 16384);
    glds<1>(vo + hw * 1024, src + 98304, lds0 + hw * 1024, L_WO + 98304);
  }
  if (hw == 0) {
    f32x4 csum;
    {
      const int t = min(lane, 55);
      const f32x4* cp = (const f32x4*)(ws + OFF_CSUM) + R.gu_nf(t >> 2) * 4 + (t & 3);
      f32x4 c[20];
#pragma unroll
      for (int k = 0; k < 20; ++k) c[k] = cp[k * 864];
#pragma unroll
      for (int k = 0; k < 10; ++k) c[k] += c[k + 10];
      csum = ((c[0] + c[1]) + (c[2] + c[3])) + ((c[4] + c[5]) + (c[6] + c[7])) + (c[8] + c[9]);
    }
    wait_vm_bar<0>();
    asm volatile("" :: "v"(tw[2]), "v"(tw[3]));
    STAMP(5);
    arrive(ws, FL_O + R.qt * 2048, lane, 256);
    await(ws, FL_O + (R.rt >> 2) * 2048 + R.ct * 256, 32);
    STAMP(6);
    asm volatile("s_barrier\n\ts_barrier\n\ts_barrier" ::: "memory");
    STAMP(7);
    arrive(ws, FL_H + (R.rt >> 1) * 2048, lane, 256);
    if (lane < 56) *(LDS_AS f32x4*)((lds_ptr)smem + L_CS + lane * 16) = csum;
    await(ws, FL_H + R.rg * 2048 + (R.cg & 7) * 256, 16);
    STAMP(8);
    asm volatile("s_waitcnt lgkmcnt(0)\n\ts_barrier" ::: "memory");
#pragma unroll 1
    for (int s = 0; s < 12; ++s) asm volatile("s_barrier" ::: "memory");
    STAMP(9);
    arrive(ws, FL_ACT + R.rg * 2048, lane, 256);
    await(ws, FL_ACT + (R.rt >> 1) * 2048 + R.ct * 256, 16);
    STAMP(10);
    asm volatile("s_barrier" ::: "memory");
    return;
  }
  const int l = hw - 1;
  unsigned vg[5], vd[5];
#pragma unroll
  for (int i = 0; i < 5; ++i) { vg[i] = vo + R.gu_nf(min(l + 3 * i, 13)) * 20480; vd[i] = vo + i * 54 * 1024; }
  const char* gsrc = ws + OFF_WGUF; const char* dsrc = ws + OFF_WDF + (R.ct * 5 * 54 + 2 * l) * 1024;
  u32x4 wr[4][5][2];
#pragma unroll
  for (int m = 0; m < 4; ++m)
#pragma unroll
    for (int i = 0; i < 5; ++i)
      asm volatile("global_load_dwordx4 %0, %2, %3\n\tglobal_load_dwordx4 %1, %2, %3 offset:1024" : "=&v"(wr[m][i][0]), "=&v"(wr[m][i][1]) : "v"(vd[i]), "s"(dsrc + (m + 5) * 6144) : "memory");
#pragma unroll
  for (int r = 0; r < 2; ++r) {
    const int part = (((bid >> 3) & 7) * 3 + l) * 2 + r, n = min(part * 47 + min(lane, 46), 2239), f = (n * 6554) >> 20;
    const unsigned o0 = OFF_WGUF + R.gu_nf(f) * 20480 + (n - f * 160) * 128;
    asm volatile("global_load_dword %0, %1, %2" : "=&v"(tw[r]) : "v"(o0), "s"(ws) : "memory");
  }
  {
    const int n = (((bid >> 3) & 7) * 3 + l) * 50 + min(lane, 49), f = (n * 4370) >> 20;
    const unsigned o1 = OFF_WDF + (R.ct * 5 + f) * 55296 + (n - f * 240) * 128;
    asm volatile("global_load_dword %0, %1, %2" : "=&v"(tw[4]) : "v"(o1), "s"(ws) : "memory");
  }
  const int g4 = min(l + 12, 13) * 2048 - l * 2048;
  auto gstage = [&](int s) {
    const unsigned base = lds0 + (s >= 6 ? s - 6 : s >= 1 ? s - 1 : 4) * G_STAGE + l * 2048;
#pragma unroll
    for (int i = 0; i < 5; ++i) glds<2>(vg[i], gsrc + s * 2048, base, i < 4 ? i * 6144 : g4);
  };
  auto dstage = [&](int m) {
    const unsigned base = lds0 + ((m ? 3 * m - 3 : 12) + l) * D_STAGE;
#pragma unroll
    for (int i = 0; i < 5; ++i) glds<2>(vd[i], dsrc + m * 6144, base, i * 2048);
  };
  asm volatile("s_barrier" ::: "memory");
  gstage(0);
  wait_vm_bar<53>();
  asm volatile("s_barrier" ::: "memory");
  gstage(1);
  asm volatile("s_barrier" ::: "memory");
  gstage(2); gstage(3); gstage(4);
  wait_vm_bar<40>();
  asm volatile("" :: "v"(tw[0]), "v"(tw[1]), "v"(tw[2]), "v"(tw[3]), "v"(tw[4]));
  wait_vm_bar<30>();
#pragma unroll 1
  for (int s = 2; s < 7; ++s) { wait_vm_bar<20>(); gstage(s + 3); }
  wait_vm_bar<20>(); dstage(0);
  wait_vm_bar<20>();
  wait_vm_bar<10>(); dstage(1);
  asm volatile("s_barrier" ::: "memory"); dstage(2);
  asm volatile("s_barrier" ::: "memory");
  dstage(3); dstage(4);
  asm volatile("s_barrier" ::: "memory");
  wait_vm_bar<0>();
#pragma unroll
  for (int m = 0; m < 4; ++m) {
    asm volatile("s_barrier" ::: "memory");
    lds_ptr d = (lds_ptr)smem + ((m ? 3 * m - 3 : 12) + l) * D_STAGE + vo;
#pragma unroll
    for (int i = 0; i < 5; ++i) {
      asm volatile("" : "+v"(wr[m][i][0]), "+v"(wr[m][i][1]));
      *(LDS_AS u32x4*)(d + i * 2048) = wr[m][i][0]; *(LDS_AS u32x4*)(d + i * 2048 + 1024) = wr[m][i][1];
    }
  }
  asm volatile("s_waitcnt lgkmcnt(0)\n\ts_barrier" ::: "memory");
}

DI void attention(char* smem, char* __restrict__ ws, rsrc_t rs, const Roles& R, int wid, int lane PROF_ARG) {
  lds_cptr L = (lds_cptr)smem;
  const int r = lane & 31, h = lane >> 5;
  const char* qb = ws + OFF_Q + R.head * Q_HEAD + (R.qt * 8 + wid * 2) * Q_TILE;
  const bf16x8 qaA = *(const bf16x8*)(qb + lane * 16), qaB = *(const bf16x8*)(qb + Q_TILE + lane * 16);
  u32x2 qtA = *(const u32x2*)(qb + 1024 + r * 8), qtB = *(const u32x2*)(qb + Q_TILE + 1024 + r * 8);
  if (h) { qtA = u32x2{0u, 0u}; qtB = qtA; }

  const int rm = r <= 20 ? r : (r == 24 ? 20 : 21);
  const int kaO = lane * 16, kbO = h ? 1280 + 320 : 1024 + r * 8, vO = 1280 + h * 352 + rm * 16;
  f32x16 zero, oA, oB, sA, sB;
#pragma unroll
  for (int i = 0; i < 16; ++i) { zero[i] = 0.f; oA[i] = 0.f; oB[i] = 0.f; }
  bf16x8 ka[2], v0[2], v1[2]; bf16x4 kb[2];
  lds_cptr pk = L + kaO, pb = L + kbO, pv = L + vO;
  wait_lgkm_bar();
  STAMP(1);
  kb[0] = lds_b64(pb); ka[0] = lds_b128(pk); v0[0] = lds_b128(pv); v1[0] = lds_b128(pv + 704);
  v1[1] = v1[0];
  {
    sA = mfma32(ka[0], qaA, mfma32k8(kb[0], __builtin_bit_cast(bf16x4, qtA), zero));
    sB = mfma32(ka[0], qaB, mfma32k8(kb[0], __builtin_bit_cast(bf16x4, qtB), zero));
    const unsigned nA = pk2(-rowmax16(sA), 0.f), nB = pk2(-rowmax16(sB), 0.f);
    if (h) { qtA[0] = nA; qtB[0] = nB; }
  }
  const bf16x4 qbA = __builtin_bit_cast(bf16x4, qtA), qbB = __builtin_bit_cast(bf16x4, qtB);
  sA = mfma32(ka[0], qaA, mfma32k8(kb[0], qbA, zero));
  const unsigned pcnt = (unsigned)(uintptr_t)smem + L_CNT + 4, pone = lane == 0;
  u32x4 pl = {0u, 0u, 0u, 0u};
  float e14 = 0.f, e15 = 0.f;

#define EX(i) __builtin_amdgcn_exp2f(sC[i])
  auto phase = [&](const f32x16& sC, f32x16& oC, f32x16& oP, bf16x8 vP, f32x16& sN, bf16x8 qa, bf16x4 qb4, bf16x8 kA, bf16x4 kB, bf16x8 vC, auto&& rd1, auto&& rd2) {
    u32x4 p0; float a, b, c, d;
    sN = mfma32k8(kB, qb4, zero); SBAR(); rd1(); SBAR();
    a = EX(0); SBAR(); pl[3] = pk2(e14, e15); SBAR(); b = EX(1); SBAR(); c = EX(2); SBAR(); p0[0] = pk2(a, b); SBAR(); d = EX(3); SBAR();
    oP = mfma32(vP, as_frag(pl), oP); SBAR(); rd2(); SBAR();
    a = EX(4); SBAR(); p0[1] = pk2(c, d); SBAR(); b = EX(5); SBAR(); c = EX(6); SBAR(); p0[2] = pk2(a, b); SBAR(); d = EX(7); SBAR();
    sN = mfma32(kA, qa, sN); SBAR();
    a = EX(8); SBAR(); p0[3] = pk2(c, d); SBAR(); b = EX(9); SBAR(); c = EX(10); SBAR(); pl[0] = pk2(a, b); SBAR(); d = EX(11); SBAR();
    oC = mfma32(vC, as_frag(p0), oC); SBAR(); __builtin_amdgcn_s_waitcnt(0xC07F); SBAR();
    a = EX(12); SBAR(); pl[1] = pk2(c, d); SBAR(); b = EX(13); SBAR(); e14 = EX(14); SBAR(); pl[2] = pk2(a, b); SBAR(); e15 = EX(15); SBAR();
  };
#undef EX
#pragma unroll 1
  for (int it = 0; it < 16; ++it) {
    const int dn = it == 14 ? -56 * KV_TILE : 4 * KV_TILE;
#pragma unroll
    for (int t = 0; t < 4; ++t) {
      const int c = t & 1, n = c ^ 1, no = t < 3 ? (t + 1) * KV_TILE : 0;
      phase(sA, oA, oB, v1[n], sB, qaB, qbB, ka[c], kb[c], v0[c],
            [&] {
              if (t == 0) asm volatile("s_bitcmp1_b32 %0, %1\n\ts_cbranch_scc0 .Lsk%=\n\ts_cmp_eq_u32 %1, 11\n\ts_cbranch_scc1 .Lsp%=\n\ts_barrier\n\ts_branch .Lsk%=\n"
                                       ".Lsp%=:\n\tds_add_u32 %2, %3\n.Lsk%=:" :: "s"(0x90A), "s"(it), "v"(pcnt), "v"(pone) : "scc", "memory");
              if (t == 3) { pk += dn; pb += dn; pv += dn; }
              kb[n] = lds_b64(pb + no); },
            [&] { ka[n] = lds_b128(pk + no); });
      phase(sB, oB, oA, v1[c], sA, qaA, qbA, ka[n], kb[n], v0[c], [&] { v0[n] = lds_b128(pv + no); }, [&] { v1[n] = lds_b128(pv + no + 704); });
    }
  }
  pl[3] = pk2(e14, e15);
  oB = mfma32(v1[1], as_frag(pl), oB);
  STAMP(2);

  float lA = h ? oA[8] : oA[12], lB = h ? oB[8] : oB[12];
  {
    f32x2 z0 = {0.f, 0.f}, z1 = z0;
#pragma unroll
    for (int i = 0; i < 7; ++i) { z0 = __builtin_elementwise_fma(f32x2{oA[2 * i], oA[2 * i + 1]}, f32x2{0.f, 0.f}, z0); z1 = __builtin_elementwise_fma(f32x2{oB[2 * i], oB[2 * i + 1]}, f32x2{0.f, 0.f}, z1); }
    z0 += z1;
    const bool ok = fmaxf(lA, lB) + (z0[0] + z0[1]) < 1.0e18f && fminf(lA, lB) > 1.0e-18f;
    if (__builtin_expect(!__all(ok), 0)) {
      const char* kvb = ws + OFF_KV + R.head * KV_HEAD;
      const bf16x4 z4 = {0, 0, 0, 0}, tA = h ? z4 : qbA, tB = h ? z4 : qbB;
      float mA = -INFINITY, mB = -INFINITY; oA = zero; oB = zero;
      auto upd = [&](f32x16 s, float& m, f32x16& o, bf16x8 w0, bf16x8 w1) {
        const float mn = fmaxf(m, rowmax16(s)), alpha = __builtin_amdgcn_exp2f(m - mn);
        m = mn; o *= alpha;
        u32x4 a, b;
#pragma unroll
        for (int i = 0; i < 4; ++i) { a[i] = exp_pk(s[2 * i] - mn, s[2 * i + 1] - mn); b[i] = exp_pk(s[8 + 2 * i] - mn, s[9 + 2 * i] - mn); }
        o = mfma32(w1, as_frag(b), mfma32(w0, as_frag(a), o));
      };
#pragma unroll 1
      for (int t = 0; t < 64; ++t) {
        const char* tb = kvb + t * KV_TILE;
        const bf16x8 k8 = *(const bf16x8*)(tb + kaO), w0 = *(const bf16x8*)(tb + vO), w1 = *(const bf16x8*)(tb + vO + 704);
        const bf16x4 k4 = *(const bf16x4*)(tb + kbO);
        upd(mfma32(k8, qaA, mfma32k8(k4, tA, zero)), mA, oA, w0, w1);
        upd(mfma32(k8, qaB, mfma32k8(k4, tB, zero)), mB, oB, w0, w1);
      }
      lA = h ? oA[8] : oA[12]; lB = h ? oB[8] : oB[12];
    }
  }
  const unsigned ob = OFF_OF + (R.qt * 16 + wid * 4) * 20480 + (R.head >> 1) * 1280;
  const unsigned lo = (r >> 4) * 20480 + (r & 15) * 16;
  auto fin = [&](const f32x16& o, float l, int blk) {
    const float inv = __builtin_amdgcn_rcpf(l);
    unsigned w[6];
#pragma unroll
    for (int u = 0; u < 6; ++u) w[u] = pk2(o[2 * u] * inv, o[2 * u + 1] * inv);
    const auto s0 = __builtin_amdgcn_permlane32_swap(w[0], w[2], false, false), s1 = __builtin_amdgcn_permlane32_swap(w[1], w[3], false, false);
    st16c(rs, lo + h * 256, ob + blk * 40960 + (R.head & 1) * 512, u32x4{s0[0], s1[0], s0[1], s1[1]});
    if (!h) st8c(rs, lo, ob + blk * 40960 + 1024 + (R.head & 1) * 8, u32x2{w[4], w[5]});
  };
  fin(oA, lA, 0); fin(oB, lB, 1);
}

#define P_DEPTH 14
#define P_PRO 4
#define P_RAMP 3
constexpr int ld_pos(int s, int pro, int ramp, int depth, int n) {
  int j = 2 * pro;
  for (int i = 0; i < s; ++i) { j += ramp; if (j > 2 * (i + 1 + depth)) j = 2 * (i + 1 + depth); if (j > 2 * n) j = 2 * n; }
  return j;
}
template <int MODE>
DI void proj(char* smem, const char* __restrict__ x, rsrc_t rs, char* __restrict__ out, const Roles& R, int wid, int lane PROF_ARG) {
  constexpr int NST = MODE ? 27 : 10, NKS = 2 * NST, DEPTH = MODE ? P_DEPTH : 10, PRO = P_PRO, RAMP = P_RAMP;
  lds_cptr L = (lds_cptr)smem;
  const int rh = wid >> 1, kp = wid & 1, t16 = lane & 15, g = lane >> 4;
  const int mf = R.rt * 4 + 2 * rh + kp, tok = mf * 16 + t16;
  const int ODD = R.ct & 1, U0 = (R.ct >> 1) * 5 + 3 * ODD, UH = (R.ct >> 1) * 5 + 2;
  const unsigned hp = OFF_HF + (mf * 20 + U0) * 1024, hh = OFF_HF + (mf * 20 + UH) * 1024 + ODD * 512;
  const unsigned fo = lane * 16, fh = (g >> 1) * 256 + t16 * 16 + (g & 1) * 8;
  const unsigned ro = tok * 1280 + U0 * 64 + g * 16, rhh = tok * 1280 + UH * 64 + ODD * 32 + g * 8;
  const unsigned ab = (MODE ? OFF_ACT : OFF_OF) + (mf * NKS + kp) * 1024, ab1 = ab + (1 - 2 * kp) * NKS * 1024;
  lds_cptr B = L + (MODE ? 0 : L_WO) + kp * 1024 + lane * 16, B1 = B + 6 * D_STAGE, B2 = B + 12 * D_STAGE;
  asm volatile("" : "+v"(B1), "+v"(B2));
  u32x4 res0, res1; u32x2 resh;
  if (MODE == 0) { res0 = *(const u32x4*)(x + ro); res1 = *(const u32x4*)(x + ro + 64); resh = *(const u32x2*)(x + rhh); }
  else { res0 = ld16c(rs, fo, hp); res1 = ld16c(rs, fo, hp + 1024); resh = ld8c(rs, fh, hh); }
  f32x4 acc[2][5];
  acc[0][0] = f32x4{bflo(res0[0]), bfhi(res0[0]), bflo(res0[1]), bfhi(res0[1])}; acc[0][1] = f32x4{bflo(res0[2]), bfhi(res0[2]), bflo(res0[3]), bfhi(res0[3])};
  acc[0][2] = f32x4{bflo(res1[0]), bfhi(res1[0]), bflo(res1[1]), bfhi(res1[1])}; acc[0][3] = f32x4{bflo(res1[2]), bfhi(res1[2]), bflo(res1[3]), bfhi(res1[3])};
  acc[0][4] = f32x4{bflo(resh[0]), bfhi(resh[0]), bflo(resh[1]), bfhi(resh[1])};
  asm volatile("" : "+v"(acc[0][0]), "+v"(acc[0][1]), "+v"(acc[0][2]), "+v"(acc[0][3]), "+v"(acc[0][4]));
  SBAR(); asm volatile("s_barrier" ::: "memory"); SBAR();
  STAMP(MODE ? 10 : 4);
  u32x4 a[DEPTH + 1][2];
  bf16x8 b[2][5];
  auto lda = [&](int j) { a[(j >> 1) % (DEPTH + 1)][j & 1] = ld16c(rs, fo, (j & 1 ? ab1 : ab) + (j >> 1) * 2048); };
  auto ldb = [&](int s, int v) { constexpr int W = 6 * D_STAGE; const int o = MODE ? ((s + 12) % 15) * D_STAGE + v * 2048 : s * 2048 + v * 20480; b[s & 1][v] = lds_b128((o < W ? B : o < 2 * W ? B1 : B2) + o % W); };
#pragma unroll
  for (int j = 0; j < 2 * PRO; ++j) lda(j);
#pragma unroll
  for (int v = 0; v < 5; ++v) ldb(0, v);
  SBAR();
  static_for<0, NST>([&](auto S) {
    constexpr int s = S.value, c = s % (DEPTH + 1);
    constexpr int j0 = ld_pos(s, PRO, RAMP, DEPTH, NST), j1 = ld_pos(s + 1, PRO, RAMP, DEPTH, NST);
    if (MODE && (s + 1) % 3 == 0 && s + 1 < 16) wait_lgkm_bar();
#pragma unroll
    for (int u = 0; u < 2; ++u)
#pragma unroll
      for (int v = 0; v < 5; ++v) {
        if (s || !u) mfma16a(acc[u][v], b[s & 1][v], a[c][u]); else mfma16z(acc[u][v], b[0][v], a[0][u]);
        if (u == 0 && s + 1 < NST) ldb(s + 1, v);
        if (u == 1 && j0 + v < j1) lda(j0 + v);
        SBAR();
      }
  });
  asm volatile("s_nop 7" ::: "memory");
  SBAR();
  STAMP(MODE ? 11 : 5);
  lds_ptr scr = (lds_ptr)smem + (MODE ? 9 * D_STAGE : L_RED) + lane * 16;
#pragma unroll
  for (int v = 0; v < 5; ++v) *(LDS_AS f32x4*)(scr + wid * 5120 + v * 1024) = acc[1][v];
  wait_lgkm_bar();
  f32x4 y[5];
#pragma unroll
  for (int v = 0; v < 5; ++v) y[v] = *(const LDS_AS f32x4*)(scr + (wid ^ 1) * 5120 + v * 1024);
  SBAR();
  unsigned o[10];
#pragma unroll
  for (int v = 0; v < 5; ++v) { y[v] += acc[0][v]; o[2 * v] = pk2(y[v][0], y[v][1]); o[2 * v + 1] = pk2(y[v][2], y[v][3]); }
  if (MODE == 0) {
    st16c(rs, fo, hp, u32x4{o[0], o[1], o[2], o[3]}); st16c(rs, fo, hp + 1024, u32x4{o[4], o[5], o[6], o[7]}); st8c(rs, fh, hh, u32x2{o[8], o[9]});
    float s0 = 0.f, s1 = 0.f, q0 = 0.f, q1 = 0.f;
    const bf2 one2 = __builtin_bit_cast(bf2, 0x3f803f80u);
#pragma unroll
    for (int i = 0; i < 5; ++i) {
      const bf2 e0 = __builtin_bit_cast(bf2, o[2 * i]), e1 = __builtin_bit_cast(bf2, o[2 * i + 1]);
      s0 = __builtin_amdgcn_fdot2_f32_bf16(e0, one2, s0, false); s1 = __builtin_amdgcn_fdot2_f32_bf16(e1, one2, s1, false);
      q0 = __builtin_amdgcn_fdot2_f32_bf16(e0, e0, q0, false); q1 = __builtin_amdgcn_fdot2_f32_bf16(e1, e1, q1, false);
    }
    const float ssum = xsum32(xsum16(s0 + s1)), ssq = xsum32(xsum16(q0 + q1));
    if (g == 0) st8c(rs, tok * 64, OFF_STAT + R.ct * 8, u32x2{__float_as_uint(ssum), __float_as_uint(ssq)});
  } else {
    const rsrc_t ro_ = __builtin_amdgcn_make_buffer_rsrc(out, 0, SEQ * DM * 2, 0x00020000);
    st16c(ro_, ro, 0, u32x4{o[0], o[1], o[2], o[3]}); st16c(ro_, ro + 64, 0, u32x4{o[4], o[5], o[6], o[7]}); st8c(ro_, rhh, 0, u32x2{o[8], o[9]});
  }
}

DI void gateup(char* smem, rsrc_t rs, const Roles& R, int wid, int lane PROF_ARG) {
  constexpr int DEPTH = 6;
  const int t16 = lane & 15, g = lane >> 4, mf = R.rg * 8 + 2 * wid;
  lds_cptr B0 = (lds_cptr)smem + lane * 16, B1 = B0 + 2 * G_STAGE, B2 = B0 + 4 * G_STAGE;
  asm volatile("" : "+v"(B1), "+v"(B2));
  const unsigned fo = lane * 16, ab = OFF_HF + mf * 20480;
  SBAR(); asm volatile("s_barrier" ::: "memory"); SBAR();
  STAMP(7);
  u32x4 a[DEPTH][2];
  bf16x8 b[14];
  f32x4 acc[2][14];
  auto lda = [&](int k, int u) { a[k % DEPTH][u] = ld16c(rs, fo, ab + XK(k) * 1024 + u * 20480); };
  auto ldb = [&](int k, int v) {
    const int slot = ((k >> 1) + 4) % 5;
    b[v] = lds_b128((slot < 2 ? B0 : slot < 4 ? B1 : B2) + (slot & 1 & (slot < 4)) * G_STAGE + (k & 1) * 1024 + v * 2048);
  };
#pragma unroll
  for (int k = 0; k < DEPTH - 1; ++k) { lda(k, 0); lda(k, 1); }
  u32x4 st0, st1;
#pragma unroll
  for (int v = 0; v < 14; ++v) ldb(0, v);
  SBAR();
  static_for<0, 20>([&](auto S) {
    constexpr int k = S.value;
    if (k & 1) { asm volatile("s_barrier" ::: "memory"); SBAR(); }
#pragma unroll
    for (int v = 0; v < 14; ++v) {
      if (k) { mfma16a(acc[0][v], b[v], a[k % DEPTH][0]); mfma16a(acc[1][v], b[v], a[k % DEPTH][1]); }
      else   { mfma16z(acc[0][v], b[v], a[0][0]); mfma16z(acc[1][v], b[v], a[0][1]); }
      if (k < 19) ldb(k + 1, v);
      if (v < 2 && k + DEPTH - 1 < 20) lda(k + DEPTH - 1, v);
      if (v == 0 && k + DEPTH - 1 == 20) st0 = ld16c(rs, t16 * 64 + g * 16, OFF_STAT + mf * 1024);
      if (v == 1 && k + DEPTH - 1 == 20) st1 = ld16c(rs, t16 * 64 + g * 16, OFF_STAT + mf * 1024 + 1024);
      SBAR();
    }
  });
  asm volatile("s_barrier\n\ts_nop 7" ::: "memory");
  SBAR();
  STAMP(8);
  float nm[2], vk[2], c1[2];
#pragma unroll
  for (int u = 0; u < 2; ++u) {
    const f32x4 s = __builtin_bit_cast(f32x4, u ? st1 : st0);
    const float mean = xsum32(xsum16(s[0] + s[2])) * (1.0f / DM);
    nm[u] = -mean; vk[u] = fmaxf(xsum32(xsum16(s[1] + s[3])) * (1.0f / DM) - mean * mean, 0.f) + LN_EPS;
    c1[u] = -LOG2E * __builtin_amdgcn_rsqf(vk[u]);
  }
  const LDS_AS f32x4* cs = (const LDS_AS f32x4*)((lds_cptr)smem + L_CS) + g;
  auto swi = [&](int u, int p) {
    const f32x4 gt = acc[u][2 * p] + cs[8 * p] * nm[u], up = acc[u][2 * p + 1] + cs[8 * p + 4] * nm[u], e = gt * c1[u], n = gt * up;
    f32x4 d;
#pragma unroll
    for (int i = 0; i < 4; ++i) d[i] = __builtin_amdgcn_exp2f(e[i]);
    d = d * vk[u] + vk[u];
#pragma unroll
    for (int i = 0; i < 4; ++i) d[i] = __builtin_amdgcn_rcpf(d[i]);
    d *= n;
    return u32x2{pk2(d[0], d[1]), pk2(d[2], d[3])};
  };
  const unsigned ob = OFF_ACT + mf * 54 * 1024;
#pragma unroll
  for (int i = 0; i < 3; ++i)
#pragma unroll
    for (int u = 0; u < 2; ++u) {
      const u32x2 pa = swi(u, 2 * i), pb = swi(u, 2 * i + 1);
      st16c(rs, fo, ob + (u * 54 + R.UB + i) * 1024, u32x4{pa[0], pa[1], pb[0], pb[1]});
    }
  if ((R.cg & 7) < 6) {
    const u32x2 pa = swi(0, 6), pb = swi(1, 6);
    const auto s0 = __builtin_amdgcn_permlane16_swap(pa[0], pb[0], false, false), s1 = __builtin_amdgcn_permlane16_swap(pa[1], pb[1], false, false);
    st16c(rs, (g & 1) * 54 * 1024 + (g >> 1) * 256 + t16 * 16, ob + R.UL * 1024 + (R.cg & 1) * 512, u32x4{s0[0], s1[0], s0[1], s1[1]});
  }
}

extern "C" __global__ __launch_bounds__(512, 2) void k_main(const char* __restrict__ x, const char* __restrict__ wo, const char* __restrict__ wg,
                                                            const char* __restrict__ wu, const char* __restrict__ wd, char* __restrict__ ws,
                                                            char* __restrict__ out) {
  __shared__ __attribute__((aligned(16))) char smem[163840];
  const int wid = __builtin_amdgcn_readfirstlane(threadIdx.x >> 6);
  PROF_DECL STAMP(0);
  const Roles R(blockIdx.x);
  const rsrc_t rs = __builtin_amdgcn_make_buffer_rsrc(ws, 0, (int)WS_BYTES, 0x00020000);
  if (wid >= 4) {
    helper_waves(smem, wo, wg, wu, wd, ws, rs, R, wid - 4, lane_id() PROF_PASS);
  } else {
    attention(smem, ws, rs, R, wid, lane_id() PROF_PASS);
    STX(1); wait_vm<0>(); STX(2);
    wait_vm_bar<0>();
    STAMP(3);
    proj<0>(smem, x, rs, out, R, wid, lane_id() PROF_PASS);
    STX(3); wait_vm<0>(); STX(4);
    wait_vm_bar<0>();
    STAMP(6);
    gateup(smem, rs, R, wid, lane_id() PROF_PASS);
    STX(5); wait_vm<0>(); STX(6);
    wait_vm_bar<0>();
    STAMP(9);
    proj<1>(smem, x, rs, out, R, wid, lane_id() PROF_PASS);
  }
  PROF_END(1);
}
extern "C" __global__ void k_end() {}

extern "C" void kernel_launch(void* const* d_in, const int* in_sizes, int n_in, void* d_out, int out_size, void* d_ws, size_t ws_size,
                              hipStream_t stream) {
  const char* x = (const char*)d_in[0]; const char* wqkv = (const char*)d_in[1]; const char* wo = (const char*)d_in[2];
  const char* wg = (const char*)d_in[4]; const char* wu = (const char*)d_in[5]; const char* wd = (const char*)d_in[6];
  char* ws = (char*)d_ws;
  k_qkv<<<256, 512, 0, stream>>>(x, wqkv, ws);
  k_main<<<256, 512, 0, stream>>>(x, wo, wg, wu, wd, ws, (char*)d_out);
}
```

```cpp
#define PROF
#define REALT
#define MEAS 0
#define FACT 2
#include <hip/hip_runtime.h>
#include <hip/hip_bf16.h>
#include <stdint.h>

typedef __attribute__((ext_vector_type(8))) short bf16x8;
typedef __attribute__((ext_vector_type(4))) short bf16x4;
typedef __attribute__((ext_vector_type(2))) float f32x2;
typedef __attribute__((ext_vector_type(4))) float f32x4;
typedef __attribute__((ext_vector_type(16))) float f32x16;
typedef __attribute__((ext_vector_type(4))) unsigned u32x4;
typedef __attribute__((ext_vector_type(2))) unsigned u32x2;
typedef __attribute__((ext_vector_type(2))) __bf16 bf2;
typedef __attribute__((address_space(3))) const char* lds_cptr;
typedef __attribute__((address_space(3))) char* lds_ptr;
#define LDS_AS __attribute__((address_space(3)))
#define DI __device__ __forceinline__

constexpr int SEQ = 2048, DM = 640, NH = 32, DH = 20, FF = 1728;
constexpr float LN_EPS = 1e-5f;
constexpr float LOG2E = 1.4426950408889634f;
constexpr float QSCALE = 0.22360679774997896f * LOG2E;

constexpr int Q_TILE = 1280;
constexpr int Q_HEAD = 64 * Q_TILE;
constexpr int KV_TILE = 2688;
constexpr int KV_HEAD = 64 * KV_TILE;
constexpr unsigned OFF_Q    = 0;
constexpr unsigned OFF_KV   = OFF_Q + NH * Q_HEAD;
constexpr unsigned OFF_OF   = OFF_KV + NH * KV_HEAD;
constexpr unsigned OFF_HF   = OFF_OF + SEQ * DM * 2;
constexpr unsigned OFF_ACT  = OFF_HF + SEQ * DM * 2;
constexpr unsigned OFF_WOF  = OFF_ACT + SEQ * FF * 2;
constexpr unsigned OFF_WGUF = OFF_WOF + DM * DM * 2;
constexpr unsigned OFF_WDF  = OFF_WGUF + DM * FF * 4;
constexpr unsigned OFF_STAT = OFF_WDF + FF * DM * 2;
constexpr unsigned OFF_CSUM = OFF_STAT + SEQ * 64;
constexpr unsigned OFF_FLAG = OFF_CSUM + 20 * 216 * 64;
constexpr unsigned FL_O = OFF_FLAG, FL_H = FL_O + 64 * 256, FL_ACT = FL_H + 128 * 256, FL_W = FL_ACT + 128 * 256;
constexpr int N_FLAGS = 384;
constexpr unsigned WS_BYTES = OFF_FLAG + N_FLAGS * 256;

#define PROF_DECL unsigned long long pt_[16] = {}; asm volatile("s_memrealtime %0\n\ts_waitcnt lgkmcnt(0)" : "=s"(pt_[14]) :: "memory");
#define TIMER "s_memrealtime"
#define STAMP(i) do { __builtin_amdgcn_sched_barrier(0); asm volatile(TIMER " %0\n\ts_waitcnt lgkmcnt(0)" : "=s"(pt_[i]) :: "memory"); __builtin_amdgcn_sched_barrier(0); } while (0)
#define PROF_END(k) do { asm volatile("s_waitcnt vmcnt(0)" ::: "memory"); STAMP(13); asm volatile("s_memrealtime %0\n\ts_waitcnt lgkmcnt(0)" : "=s"(pt_[15]) :: "memory"); \
  if (__builtin_amdgcn_mbcnt_hi(~0u, __builtin_amdgcn_mbcnt_lo(~0u, 0u)) == 0) { unsigned long long* pp_ = (unsigned long long*)(ws + (128ull << 20)) + (((k) * 256 + blockIdx.x) * 8 + wid) * 16; for (int i_ = 0; i_ < 16; ++i_) pp_[i_] = pt_[i_]; } } while (0)
#define PROF_ARG , unsigned long long (&pt_)[16]
#define PROF_PASS , pt_
#define STXN 0
#define STX(n) do { if (n == STXN) STAMP(12); } while (0)
template <int N> DI void glds(unsigned voff, const void* sbase, unsigned lds, int imm) {
#define GL(o) "\n\tglobal_load_lds_dwordx4 %0, %1 offset:" #o
  if (N == 1) asm volatile("s_add_u32 m0, %2, %3\n\ts_nop 0" GL(0) :: "v"(voff), "s"(sbase), "s"(lds), "ns"(imm) : "memory", "scc");
  if (N == 2) asm volatile("s_add_u32 m0, %2, %3\n\ts_nop 0" GL(0) GL(1024) :: "v"(voff), "s"(sbase), "s"(lds), "ns"(imm) : "memory", "scc");
  if (N == 4) asm volatile("s_add_u32 m0, %2, %3\n\ts_nop 0" GL(0) GL(1024) GL(2048) GL(3072) :: "v"(voff), "s"(sbase), "s"(lds), "ns"(imm) : "memory", "scc");
#undef GL
}
#define CODE_TOUCH(first, last, part, sink) do { unsigned long long pc_; int a_, b_; \
  asm volatile("s_getpc_b64 %0\n.Lct%=:\n\ts_mov_b32 %1, " #first "-.Lct%=\n\ts_mov_b32 %2, " #last "-.Lct%=" : "=s"(pc_), "=s"(a_), "=s"(b_)); \
  const int o_ = min(max(a_, 0) + lane * 128 + (part) * 8192, max(b_, 0)); \
  asm volatile("global_load_dword %0, %1, %2" : "=v"(sink) : "v"(o_), "s"(pc_) : "memory"); } while (0)
#define CODE_DONE(sink) asm volatile("s_waitcnt vmcnt(0)" :: "v"(sink) : "memory")
typedef __attribute__((address_space(1))) unsigned gu32;
typedef __amdgpu_buffer_rsrc_t rsrc_t;
#define LDAUX 16
#define XK(k) (k)
DI u32x4 ld16c(rsrc_t rs, unsigned vo, unsigned so) { return __builtin_amdgcn_raw_buffer_load_b128(rs, vo, so, LDAUX); }
DI u32x2 ld8c(rsrc_t rs, unsigned vo, unsigned so) { return __builtin_amdgcn_raw_buffer_load_b64(rs, vo, so, 16); }
DI void st16c(rsrc_t rs, unsigned vo, unsigned so, u32x4 v) { __builtin_amdgcn_raw_buffer_store_b128(v, rs, vo + so, 0, 16); }
DI void st8c(rsrc_t rs, unsigned vo, unsigned so, u32x2 v) { __builtin_amdgcn_raw_buffer_store_b64(v, rs, vo, so, 16); }
DI void arrive(char* ws, unsigned off, int lane, int stride) {
  if (lane < 8) __hip_atomic_fetch_add((gu32*)(ws + off + lane * stride), 1u, __ATOMIC_RELAXED, __HIP_MEMORY_SCOPE_AGENT);
}
DI void await8(char* ws, unsigned off, int lane, unsigned want) {
#pragma unroll 1
  for (int n = 0; n < (1 << 16); ++n) {
    const unsigned v = __hip_atomic_load((gu32*)(ws + off + (lane & 7) * 256), __ATOMIC_RELAXED, __HIP_MEMORY_SCOPE_AGENT);
    if (__all(v >= want)) break;
    __builtin_amdgcn_s_sleep(1);
  }
}
DI void await(char* ws, unsigned off, unsigned want) {
#pragma unroll 1
  for (int n = 0; n < (1 << 16); ++n) {
    const unsigned v = __hip_atomic_load((gu32*)(ws + off), __ATOMIC_RELAXED, __HIP_MEMORY_SCOPE_AGENT);
    if ((unsigned)__builtin_amdgcn_readfirstlane(v) >= want) break;
    __builtin_amdgcn_s_sleep(1);
  }
  __builtin_amdgcn_fence(__ATOMIC_ACQUIRE, "wavefront");
}
template <int N> DI void wait_vm_bar() { asm volatile("s_waitcnt vmcnt(%0)\n\ts_barrier" :: "n"(N) : "memory"); }
template <int N> DI void wait_vm() { asm volatile("s_waitcnt vmcnt(%0)" :: "n"(N) : "memory"); }
#define SBAR() __builtin_amdgcn_sched_barrier(0)
#define SGB(mask, n) __builtin_amdgcn_sched_group_barrier(mask, n, 0)
DI void wait_lgkm_bar() { SBAR(); __builtin_amdgcn_s_waitcnt(0xC07F); asm volatile("s_barrier" ::: "memory"); SBAR(); }
#define GEMM_OFF 0
template <int I> struct IC { static constexpr int value = I; };
template <int B, int E, class F> DI void static_for(F&& f) { if constexpr (B < E) { f(IC<B>{}); static_for<B + 1, E>(f); } }
DI bf16x8 lds_b128(lds_cptr p) { return *(const LDS_AS bf16x8*)p; }
DI bf16x4 lds_b64(lds_cptr p) { return *(const LDS_AS bf16x4*)p; }
DI bf16x4 lds_tr(lds_cptr p) { return __builtin_amdgcn_ds_read_tr16_b64_v4i16((LDS_AS bf16x4*)p); }
DI f32x4 mfma16(bf16x8 a, bf16x8 b, f32x4 c) { return __builtin_amdgcn_mfma_f32_16x16x32_bf16(a, b, c, 0, 0, 0); }
DI void mfma16a(f32x4& c, bf16x8 a, u32x4 b) { asm volatile("v_mfma_f32_16x16x32_bf16 %0, %1, %2, %0" : "+v"(c) : "v"(a), "v"(b)); }
DI void mfma16z(f32x4& c, bf16x8 a, u32x4 b) { asm volatile("v_mfma_f32_16x16x32_bf16 %0, %1, %2, 0" : "=&v"(c) : "v"(a), "v"(b)); }
DI f32x16 mfma32(bf16x8 a, bf16x8 b, f32x16 c) { return __builtin_amdgcn_mfma_f32_32x32x16_bf16(a, b, c, 0, 0, 0); }
DI f32x16 mfma32k8(bf16x4 a, bf16x4 b, f32x16 c) { return __builtin_amdgcn_mfma_f32_32x32x8bf16_1k(a, b, c, 0, 0, 0); }
DI unsigned pk2(float lo, float hi) { f32x2 v = {lo, hi}; return __builtin_bit_cast(unsigned, __builtin_convertvector(v, bf2)); }
DI float bflo(unsigned u) { return __uint_as_float(u << 16); }
DI float bfhi(unsigned u) { return __uint_as_float(u & 0xffff0000u); }
DI int div20(int c) { return (c * 3277) >> 16; }
DI float xsum32(float v) { auto r = __builtin_amdgcn_permlane32_swap(__float_as_uint(v), __float_as_uint(v), false, false); return __uint_as_float(r[0]) + __uint_as_float(r[1]); }
DI float xsum16(float v) { auto r = __builtin_amdgcn_permlane16_swap(__float_as_uint(v), __float_as_uint(v), false, false); return __uint_as_float(r[0]) + __uint_as_float(r[1]); }
constexpr bf16x8 ONES8 = {0x3f80, 0x3f80, 0x3f80, 0x3f80, 0x3f80, 0x3f80, 0x3f80, 0x3f80};

template <int MODE>
DI void qkv_compute(lds_cptr L, lds_ptr scr, int wid, int lane, int row0, int col0, char* __restrict__ ws PROF_ARG) {
  const int wr = wid >> 1, wc = wid & 1, r16 = lane & 15, g = lane >> 4, q = r16 >> 2, p = lane & 3;
  const int f = q | ((g & 1) << 2);
  const int rp = MODE == 2 ? 16 * (r16 >> 3) + (r16 & 7) : r16;
  constexpr int RSTEP = MODE == 2 ? 8 : 16;
  int aoff[2][2], boff[4];
#pragma unroll
  for (int e = 0; e < 2; ++e)
#pragma unroll
    for (int ks = 0; ks < 2; ++ks) { const int r = rp + e * RSTEP; aoff[e][ks] = (64 * wr + r) * 128 + (((4 * ks + g) ^ ((r >> 1) & 7) ^ ((r >> 2) & 4)) << 4); }
#pragma unroll
  for (int jj = 0; jj < 4; ++jj) { const int j = (jj + 2 * wr) & 3; boff[jj] = 16384 + (8 * g + q) * 256 + ((((4 * wc + j) ^ f) * 2 + (p >> 1)) << 4) + (p & 1) * 8; }
  const int pr0 = wc * 4096, pr1 = 4096 - pr0;

  f32x4 acc[4][4], rs[2], gr[2], cs[2];
#pragma unroll
  for (int i = 0; i < 4; ++i)
#pragma unroll
    for (int j = 0; j < 4; ++j) acc[i][j] = f32x4{0.f, 0.f, 0.f, 0.f};
#pragma unroll
  for (int i = 0; i < 2; ++i) { rs[i] = f32x4{0.f, 0.f, 0.f, 0.f}; gr[i] = rs[i]; cs[i] = rs[i]; }
  bf16x8 a0[4], b0[4], a1[4], b1[4];

  auto rd = [&](bf16x8 (&a)[4], bf16x8 (&b)[4], int slot, int ks) {
    if (GEMM_OFF && slot + ks > 0) return;
    lds_cptr S = L + slot;
#pragma unroll
    for (int jj = 0; jj < 4; ++jj) {
      bf16x4 lo = lds_tr(S + boff[jj] + ks * 8192), hi = lds_tr(S + boff[jj] + ks * 8192 + 1024);
      b[jj] = __builtin_shufflevector(lo, hi, 0, 1, 2, 3, 4, 5, 6, 7);
      a[jj] = lds_b128(S + aoff[jj & 1][ks] + (jj < 2 ? pr0 : pr1));
    }
  };
  auto mm = [&](bf16x8 (&a)[4], bf16x8 (&b)[4]) {
    if (GEMM_OFF) return;
#pragma unroll
    for (int ii = 0; ii < 4; ++ii)
#pragma unroll
      for (int jj = 0; jj < 4; ++jj)
        acc[ii][jj] = (MODE == 2) ? mfma16(a[ii], b[jj], acc[ii][jj]) : mfma16(b[jj], a[ii], acc[ii][jj]);
#pragma unroll
    for (int u = 0; u < 2; ++u) {
      rs[u] = mfma16(a[u], ONES8, rs[u]);
      gr[u] = mfma16(a[u], a[u], gr[u]);
      cs[u] = mfma16(ONES8, b[u], cs[u]);
    }
  };
  auto ilv = [&] {
#pragma unroll
    for (int i = 0; i < 12; ++i) { SGB(0x8, 1); SGB(0x100, 1); }
    SGB(0x8, 10);
    SBAR();
  };

  wait_lgkm_bar();
  STAMP(1);
  rd(a0, b0, 0, 0);
  SBAR();
#pragma unroll 1
  for (int kt = 0; kt < 9; ++kt) {
    rd(a1, b1, (kt & 3) * 32768, 1); mm(a0, b0); ilv();
    wait_lgkm_bar();
    rd(a0, b0, ((kt + 1) & 3) * 32768, 0); mm(a1, b1); ilv();
  }
  rd(a1, b1, 32768, 1); mm(a0, b0); ilv();
  mm(a1, b1);
  SBAR();
  STAMP(2);
  LDS_AS float* rsum = (LDS_AS float*)scr; LDS_AS float* rsq = rsum + 128; LDS_AS float* csum = rsum + 256;
#pragma unroll
  for (int u = 0; u < 2; ++u) {
    const int rb = 64 * wr + 32 * wc + 16 * u;
    if (r16 == 0) *(LDS_AS f32x4*)(rsum + rb + 4 * g) = rs[u];
    if (q == g) { const float d = p == 0 ? gr[u][0] : p == 1 ? gr[u][1] : p == 2 ? gr[u][2] : gr[u][3]; rsq[rb + r16] = d; }
    if (g == 0) csum[64 * wc + 32 * wr + 16 * u + r16] = cs[u][0];
  }
  constexpr float invD = 1.0f / DM;
  if (MODE != 2) {
    constexpr int TS = MODE == 0 ? Q_TILE : KV_TILE, HS = MODE == 0 ? Q_HEAD : KV_HEAD;
    unsigned off[4][2];
#pragma unroll
    for (int jj = 0; jj < 4; ++jj) {
      const int ck = col0 - MODE * DM + 64 * wc + 16 * ((jj + 2 * wr) & 3) + 4 * g, head = div20(ck), d = ck - head * DH;
      off[jj][0] = head * HS + (d < 16 ? (d >> 3) * 512 + (d & 7) * 2 + r16 * 16 : 1024 + r16 * 8);
      off[jj][1] = off[jj][0] + (d < 16 ? 256 : 128);
    }
    char* base = ws + (MODE == 0 ? OFF_Q : OFF_KV) + ((row0 >> 5) + 2 * wr) * TS;
    wait_lgkm_bar();
    f32x4 c4[4];
#pragma unroll
    for (int jj = 0; jj < 4; ++jj) c4[jj] = *(const LDS_AS f32x4*)(csum + 64 * wc + 16 * ((jj + 2 * wr) & 3) + 4 * g);
#pragma unroll
    for (int ii = 0; ii < 4; ++ii) {
      const int half = ((ii >> 1) + wc) & 1, rl = 64 * wr + 32 * half + 16 * (ii & 1) + r16;
      const float mean = rsum[rl] * invD, var = fmaxf(rsq[rl] * invD - mean * mean, 0.f);
      const float rstd = __builtin_amdgcn_rsqf(var + LN_EPS) * (MODE == 0 ? QSCALE : 1.0f), nb = -mean * rstd;
      char* tb = base + half * TS;
#pragma unroll
      for (int jj = 0; jj < 4; ++jj) {
        const f32x4 v = acc[ii][jj] * rstd + c4[jj] * nb;
        *(u32x2*)(tb + off[jj][ii & 1]) = u32x2{pk2(v[0], v[1]), pk2(v[2], v[3])};
      }
    }
  } else {
    unsigned off[4];
#pragma unroll
    for (int jj = 0; jj < 4; ++jj) {
      const int cv = col0 - 2 * DM + 64 * wc + 16 * ((jj + 2 * wr) & 3) + r16, head = div20(cv), d = cv - head * DH;
      off[jj] = head * KV_HEAD + 1280 + g * 352 + d * 16;
    }
    char* base = ws + OFF_KV + ((row0 >> 5) + 2 * wr) * KV_TILE;
    { const int head = div20(col0 - 2 * DM + 19) + (lane >> 3), t = lane & 7;
      if (head * DH < col0 - 2 * DM + 128) *(u32x4*)(base + head * KV_HEAD + (t >> 2) * KV_TILE + 1280 + (t & 3) * 352 + 320 + wc * 16) = wc ? u32x4{0u, 0u, 0u, 0u} : __builtin_bit_cast(u32x4, ONES8); }
    wait_lgkm_bar();
    float c1[4];
#pragma unroll
    for (int jj = 0; jj < 4; ++jj) c1[jj] = csum[64 * wc + 16 * ((jj + 2 * wr) & 3) + r16];
#pragma unroll
    for (int ip = 0; ip < 2; ++ip) {
      const int half = (ip + wc) & 1;
      f32x4 rstd[2], nb[2];
#pragma unroll
      for (int e = 0; e < 2; ++e) {
        const int rl = 64 * wr + 32 * half + 16 * e + 4 * g;
        const f32x4 mean = *(const LDS_AS f32x4*)(rsum + rl) * invD, q4 = *(const LDS_AS f32x4*)(rsq + rl);
#pragma unroll
        for (int i = 0; i < 4; ++i) rstd[e][i] = __builtin_amdgcn_rsqf(fmaxf(q4[i] * invD - mean[i] * mean[i], 0.f) + LN_EPS);
        nb[e] = -mean * rstd[e];
      }
      char* tb = base + half * KV_TILE;
#pragma unroll
      for (int jj = 0; jj < 4; ++jj) {
        const f32x4 v0 = acc[2 * ip][jj] * rstd[0] + nb[0] * c1[jj], v1 = acc[2 * ip + 1][jj] * rstd[1] + nb[1] * c1[jj];
        *(u32x4*)(tb + off[jj]) = u32x4{pk2(v0[0], v0[1]), pk2(v0[2], v0[3]), pk2(v1[0], v1[1]), pk2(v1[2], v1[3])};
      }
    }
  }
}

extern "C" __global__ __launch_bounds__(512, 2) void k_qkv(const char* __restrict__ x, const char* __restrict__ w, char* __restrict__ ws) {
  __shared__ __attribute__((aligned(16))) char smem[4 * 32768 + 2048];
  const int bid = blockIdx.x, xcd = bid & 7, idx = bid >> 3;
  const int rt = (xcd >> 1) * 4 + (idx & 3), ct = (xcd & 1) * 8 + (idx >> 2);
  if (ct >= 15) {
    if (rt == 0 && threadIdx.x < N_FLAGS) *(unsigned*)(ws + OFF_FLAG + threadIdx.x * 256) = 0u;
    return;
  }
  PROF_DECL STAMP(0);
  const int tid = threadIdx.x, lane = tid & 63, wid = __builtin_amdgcn_readfirstlane(tid >> 6);
  if (wid >= 4) {
    const int lw = wid - 4;
    const unsigned lds0 = (unsigned)(uintptr_t)smem + lw * 1024;
    const int ra = 8 * lw + (lane >> 3), rb = 4 * lw + (lane >> 4), f = (rb & 3) | ((rb >> 1) & 4);
    const unsigned voA = ra * 1280 + (((lane & 7) ^ ((ra >> 1) & 7) ^ ((ra >> 2) & 4)) << 4), voB = rb * 3840 + (((lane & 15) ^ (f << 1)) << 4);
    const char* pa = x + (size_t)rt * 128 * 1280; const char* pb = w + ct * 256;
    auto issue = [&](int st) {
      const unsigned base = lds0 + (st & 3) * 32768;
#pragma unroll
      for (int j = 0; j < 4; ++j) { glds<1>(voA + j * 32 * 1280, pa + st * 128, base, j * 4096); glds<1>(voB + j * 16 * 3840, pb + st * 64 * 3840, base, 16384 + j * 4096); }
    };
    STAMP(1); issue(0); issue(1); STAMP(2);
    wait_vm_bar<8>(); STAMP(3);
    issue(2); issue(3);
#pragma unroll 1
    for (int s = 1; s < 7; ++s) { wait_vm_bar<16>(); issue(s + 3); }
    wait_vm_bar<16>(); wait_vm_bar<8>(); wait_vm_bar<0>();
    PROF_END(0);
    return;
  }
  lds_cptr L = (lds_cptr)smem; lds_ptr scr = (lds_ptr)smem + 4 * 32768;
  unsigned cs0, cs1; CODE_TOUCH(k_qkv, k_end, wid, cs0); CODE_TOUCH(k_qkv, k_end, wid + 4, cs1);
  if (ct < 5) qkv_compute<0>(L, scr, wid, lane, rt * 128, ct * 128, ws PROF_PASS);
  else if (ct < 10) qkv_compute<1>(L, scr, wid, lane, rt * 128, ct * 128, ws PROF_PASS);
  else qkv_compute<2>(L, scr, wid, lane, rt * 128, ct * 128, ws PROF_PASS);
  CODE_DONE(cs0); CODE_DONE(cs1);
  PROF_END(0);
}

constexpr int L_STG = 48 * KV_TILE;
constexpr int L_WO = 4 * KV_TILE;
constexpr int G_STAGE = 28 * 1024;
constexpr int L_RED = 5 * G_STAGE;
constexpr int D_STAGE = 10 * 1024;
constexpr int L_CS = 15 * D_STAGE;
constexpr int L_CNT = 163840 - 16;

DI float rowmax16(const f32x16& s) {
  float a = fmaxf(fmaxf(s[0], s[1]), s[2]), b = fmaxf(fmaxf(s[3], s[4]), s[5]);
  a = fmaxf(fmaxf(a, s[6]), s[7]); b = fmaxf(fmaxf(b, s[8]), s[9]);
  a = fmaxf(fmaxf(a, s[10]), s[11]); b = fmaxf(fmaxf(b, s[12]), s[13]);
  a = fmaxf(fmaxf(a, s[14]), s[15]);
  const float m = fmaxf(a, b);
  auto rr = __builtin_amdgcn_permlane32_swap(__float_as_uint(m), __float_as_uint(m), false, false);
  return fmaxf(__uint_as_float(rr[0]), __uint_as_float(rr[1]));
}
DI unsigned exp_pk(float a, float b) { return pk2(__builtin_amdgcn_exp2f(a), __builtin_amdgcn_exp2f(b)); }
DI int lane_id() { int v; asm volatile("v_mbcnt_lo_u32_b32 %0, -1, 0\n\tv_mbcnt_hi_u32_b32 %0, -1, %0" : "=v"(v)); __builtin_assume(v >= 0 && v < 64); return v; }
DI bf16x8 as_frag(u32x4 v) { return __builtin_bit_cast(bf16x8, v); }

template <int KIND>
DI void retile_issue(const char* __restrict__ w0, const char* __restrict__ w1, int tile, int ks, int lane, unsigned stg) {
  if (KIND == 0) {
    const unsigned vo = (ks * 32 + (lane >> 3)) * (FF * 2) + tile * 128 + (lane & 7) * 16;
#pragma unroll
    for (int j = 0; j < 4; ++j) { glds<1>(vo + j * 8 * (FF * 2), w0, stg, j * 1024); glds<1>(vo + j * 8 * (FF * 2), w1, stg, 4096 + j * 1024); }
  } else {
#pragma unroll
    for (int j = 0; j < 8; ++j) {
      int k = ks * 32 + 4 * j + (lane >> 4);
      if (KIND == 2) { const int pp = div20(k) >> 1, e = k - 40 * pp; k = 40 * pp + (e < 16 ? e : e < 32 ? e + 4 : e < 36 ? e - 16 : e); }
      glds<1>(k * (DM * 2) + tile * 256 + (lane & 15) * 16, w0, stg, j * 1024);
    }
  }
}
template <int KIND>
DI void retile_store(rsrc_t rs, lds_cptr S, int tile, int ks, int lane) {
  const int g = lane >> 4, q = (lane & 15) >> 2, p = lane & 3;
#pragma unroll
  for (int f = 0; f < 8; ++f) {
    lds_cptr a; int hi; unsigned dst;
    if (KIND == 0) {
      const int U = 2 * tile + (f >> 2), j = (f >> 1) & 1;
      a = S + (f & 1) * 4096 + (8 * g + q) * 128 + (32 * (f >> 2) + ((U >= 27 ? U - 27 : U) >= 24 ? 16 * j + 4 * p : 8 * p + 4 * j)) * 2; hi = 512;
      dst = OFF_WGUF + ((8 * tile + f) * 20 + ks) * 1024;
    } else {
      const int U = 4 * tile + (f >> 1), j = f & 1;
      a = S + (8 * g + q) * 256 + (32 * (f >> 1) + (U % 5 == 2 ? 16 * j + 4 * p : 8 * p + 4 * j)) * 2; hi = 1024;
      const int n10 = (2 * U + j) % 10, nf = 2 * U + j + (n10 == 5 ? 4 : n10 > 5 ? -1 : 0);
      dst = (KIND == 1 ? OFF_WDF : OFF_WOF) + (nf * (KIND == 1 ? 54 : 20) + ks) * 1024;
    }
    const bf16x8 fr = __builtin_shufflevector(lds_tr(a), lds_tr(a + hi), 0, 1, 2, 3, 4, 5, 6, 7);
    st16c(rs, lane * 16, dst, __builtin_bit_cast(u32x4, fr));
    if (KIND == 0) {
      const f32x4 c = mfma16(fr, ONES8, f32x4{0.f, 0.f, 0.f, 0.f});
      if ((lane & 15) == 0) st16c(rs, g * 16, OFF_CSUM + (ks * 216 + 8 * tile + f) * 64, __builtin_bit_cast(u32x4, c));
    }
  }
}

struct Roles {
  int head, qt, rt, ct, rg, cg, UB, UL;
  DI Roles(int bid) {
    const int xcd = bid & 7, idx = bid >> 3;
    head = xcd * 4 + (idx >> 3); qt = idx & 7;
    rt = (xcd >> 1) * 8 + (idx & 7); ct = (xcd & 1) * 4 + (idx >> 3);
    rg = (xcd >> 2) * 8 + (idx & 7); cg = (xcd & 3) * 4 + (idx >> 3);
    UB = (cg >> 3) * 27 + 3 * (cg & 7); UL = (cg >> 3) * 27 + 24 + min((cg & 7) >> 1, 2);
  }
  DI int gu_nf(int f) const { return f < 12 ? 4 * UB + f : 4 * UL + 2 * (cg & 1) + f - 12; }
};

DI void helper_waves(char* smem, const char* __restrict__ wo, const char* __restrict__ wg, const char* __restrict__ wu, const char* __restrict__ wd,
                     char* __restrict__ ws, rsrc_t rs, const Roles& R, int hw, int lane PROF_ARG) {
  const int bid = blockIdx.x;
  const unsigned lds0 = (unsigned)(uintptr_t)smem, vo = lane * 16;
  LDS_AS unsigned* cnt = (LDS_AS unsigned*)((lds_ptr)smem + L_CNT);
  const char* kv = ws + OFF_KV + R.head * KV_HEAD;
  const unsigned vk = vo + hw * 4096, lk = lds0 + hw * 4096;
  auto runs = [&](int i0, int i1) {
#pragma unroll
    for (int i = i0; i < i1; ++i) glds<4>(vk, kv + i * 16384, lk, i * 16384);
  };
  runs(0, 3);
  if (hw == 0) { cnt[0] = 0u; cnt[1] = 0u; }
  STAMP(1);
  asm volatile("s_waitcnt vmcnt(8) lgkmcnt(0)\n\ts_barrier" ::: "memory");
  runs(3, 6);
  const int job = bid * 4 + hw;
  const unsigned stg = lds0 + L_STG + hw * 8192;
  int tile = 0, ks = 0;
  if (job < 540)      { ks = job / 27; tile = job - ks * 27; retile_issue<0>(wg, wu, tile, ks, lane, stg); }
  else if (job < 810) { ks = (job - 540) / 5; tile = job - 540 - ks * 5; retile_issue<1>(wd, wd, tile, ks, lane, stg); }
  else                { ks = min(job - 810, 99) / 5; tile = min(job - 810, 99) - ks * 5; retile_issue<2>(wo, wo, tile, ks, lane, stg); }
  wait_vm_bar<20>();
  runs(6, 7);
  if (hw < 3) glds<4>(vk, kv + 7 * 16384, lk, 7 * 16384); else { glds<2>(vk, kv + 7 * 16384, lk, 7 * 16384); glds<2>(vk, kv + 7 * 16384, lk, 7 * 16384); }
  wait_vm_bar<16>();
  if (hw < 3) glds<4>(vk, kv + 60 * KV_TILE, lk, 0);
  if (hw < 3) wait_vm<4>(); else wait_vm<0>();
  STAMP(2);
  lds_cptr S = (lds_cptr)smem + L_STG + hw * 8192;
  if (job < 540) retile_store<0>(rs, S, tile, ks, lane);
  else if (job < 810) retile_store<1>(rs, S, tile, ks, lane);
  else if (job < 910) retile_store<2>(rs, S, tile, ks, lane);
  asm volatile("s_waitcnt lgkmcnt(0)" ::: "memory");
  glds<4>(vo + hw * 8192, kv + L_STG, stg, 0); glds<4>(vo + hw * 8192, kv + L_STG + 4096, stg, 4096);
  wait_vm<0>();
  STAMP(3);
  unsigned old = 0;
  if (lane == 0) old = __hip_atomic_fetch_add(cnt, 1u, __ATOMIC_RELAXED, __HIP_MEMORY_SCOPE_WORKGROUP);
  if (__builtin_amdgcn_readfirstlane(old) == 3) arrive(ws, FL_W + (bid & 7) * 256, lane, 2048);
  asm volatile("s_barrier" ::: "memory");
  if (hw == 0) {
    await8(ws, FL_W + ((bid >> 3) & 7) * 2048, lane, 32);
    __builtin_amdgcn_fence(__ATOMIC_ACQUIRE, "agent");
    wait_vm<0>();
    if (lane == 0) __hip_atomic_fetch_add(cnt + 1, 1u, __ATOMIC_RELAXED, __HIP_MEMORY_SCOPE_WORKGROUP);
  }
  STAMP(4);
  while (__builtin_amdgcn_readfirstlane(__hip_atomic_load(cnt + 1, __ATOMIC_RELAXED, __HIP_MEMORY_SCOPE_WORKGROUP)) < 5) __builtin_amdgcn_s_sleep(4);
  unsigned tw[5];
  CODE_TOUCH(k_main, k_end, hw, tw[2]); CODE_TOUCH(k_main, k_end, hw + 4, tw[3]);
  {
    const char* src = ws + OFF_WOF + R.ct * 102400;
#pragma unroll
    for (int i = 0; i < 6; ++i) glds<4>(vk, src + i * 16384, lk, L_WO + i * 16384);
    glds<1>(vo + hw * 1024, src + 98304, lds0 + hw * 1024, L_WO + 98304);
  }
  if (hw == 0) {
    f32x4 csum;
    {
      const int t = min(lane, 55);
      const f32x4* cp = (const f32x4*)(ws + OFF_CSUM) + R.gu_nf(t >> 2) * 4 + (t & 3);
      f32x4 c[20];
#pragma unroll
      for (int k = 0; k < 20; ++k) c[k] = cp[k * 864];
#pragma unroll
      for (int k = 0; k < 10; ++k) c[k] += c[k + 10];
      csum = ((c[0] + c[1]) + (c[2] + c[3])) + ((c[4] + c[5]) + (c[6] + c[7])) + (c[8] + c[9]);
    }
    wait_vm_bar<0>();
    asm volatile("" :: "v"(tw[2]), "v"(tw[3]));
    STAMP(5);
    arrive(ws, FL_O + R.qt * 2048, lane, 256);
    await(ws, FL_O + (R.rt >> 2) * 2048 + R.ct * 256, 32);
    STAMP(6);
    asm volatile("s_barrier\n\ts_barrier\n\ts_barrier" ::: "memory");
    STAMP(7);
    arrive(ws, FL_H + (R.rt >> 1) * 2048, lane, 256);
    if (lane < 56) *(LDS_AS f32x4*)((lds_ptr)smem + L_CS + lane * 16) = csum;
    await(ws, FL_H + R.rg * 2048 + (R.cg & 7) * 256, 16);
    STAMP(8);
    asm volatile("s_waitcnt lgkmcnt(0)\n\ts_barrier" ::: "memory");
#pragma unroll 1
    for (int s = 0; s < 12; ++s) asm volatile("s_barrier" ::: "memory");
    STAMP(9);
    arrive(ws, FL_ACT + R.rg * 2048, lane, 256);
    await(ws, FL_ACT + (R.rt >> 1) * 2048 + R.ct * 256, 16);
    STAMP(10);
    asm volatile("s_barrier" ::: "memory");
    return;
  }
  const int l = hw - 1;
  unsigned vg[5], vd[5];
#pragma unroll
  for (int i = 0; i < 5; ++i) { vg[i] = vo + R.gu_nf(min(l + 3 * i, 13)) * 20480; vd[i] = vo + i * 54 * 1024; }
  const char* gsrc = ws + OFF_WGUF; const char* dsrc = ws + OFF_WDF + (R.ct * 5 * 54 + 2 * l) * 1024;
  u32x4 wr[4][5][2];
#pragma unroll
  for (int m = 0; m < 4; ++m)
#pragma unroll
    for (int i = 0; i < 5; ++i)
      asm volatile("global_load_dwordx4 %0, %2, %3\n\tglobal_load_dwordx4 %1, %2, %3 offset:1024" : "=&v"(wr[m][i][0]), "=&v"(wr[m][i][1]) : "v"(vd[i]), "s"(dsrc + (m + 5) * 6144) : "memory");
#pragma unroll
  for (int r = 0; r < 2; ++r) {
    const int part = (((bid >> 3) & 7) * 3 + l) * 2 + r, n = min(part * 47 + min(lane, 46), 2239), f = (n * 6554) >> 20;
    const unsigned o0 = OFF_WGUF + R.gu_nf(f) * 20480 + (n - f * 160) * 128;
    asm volatile("global_load_dword %0, %1, %2" : "=&v"(tw[r]) : "v"(o0), "s"(ws) : "memory");
  }
  {
    const int n = (((bid >> 3) & 7) * 3 + l) * 50 + min(lane, 49), f = (n * 4370) >> 20;
    const unsigned o1 = OFF_WDF + (R.ct * 5 + f) * 55296 + (n - f * 240) * 128;
    asm volatile("global_load_dword %0, %1, %2" : "=&v"(tw[4]) : "v"(o1), "s"(ws) : "memory");
  }
  const int g4 = min(l + 12, 13) * 2048 - l * 2048;
  auto gstage = [&](int s) {
    const unsigned base = lds0 + (s >= 6 ? s - 6 : s >= 1 ? s - 1 : 4) * G_STAGE + l * 2048;
#pragma unroll
    for (int i = 0; i < 5; ++i) glds<2>(vg[i], gsrc + s * 2048, base, i < 4 ? i * 6144 : g4);
  };
  auto dstage = [&](int m) {
    const unsigned base = lds0 + ((m ? 3 * m - 3 : 12) + l) * D_STAGE;
#pragma unroll
    for (int i = 0; i < 5; ++i) glds<2>(vd[i], dsrc + m * 6144, base, i * 2048);
  };
  asm volatile("s_barrier" ::: "memory");
  gstage(0);
  wait_vm_bar<53>();
  asm volatile("s_barrier" ::: "memory");
  gstage(1);
  asm volatile("s_barrier" ::: "memory");
  gstage(2); gstage(3); gstage(4);
  wait_vm_bar<40>();
  asm volatile("" :: "v"(tw[0]), "v"(tw[1]), "v"(tw[2]), "v"(tw[3]), "v"(tw[4]));
  wait_vm_bar<30>();
#pragma unroll 1
  for (int s = 2; s < 7; ++s) { wait_vm_bar<20>(); gstage(s + 3); }
  wait_vm_bar<20>(); dstage(0);
  wait_vm_bar<20>();
  wait_vm_bar<10>(); dstage(1);
  asm volatile("s_barrier" ::: "memory"); dstage(2);
  asm volatile("s_barrier" ::: "memory");
  dstage(3); dstage(4);
  asm volatile("s_barrier" ::: "memory");
  wait_vm_bar<0>();
#pragma unroll
  for (int m = 0; m < 4; ++m) {
    asm volatile("s_barrier" ::: "memory");
    lds_ptr d = (lds_ptr)smem + ((m ? 3 * m - 3 : 12) + l) * D_STAGE + vo;
#pragma unroll
    for (int i = 0; i < 5; ++i) {
      asm volatile("" : "+v"(wr[m][i][0]), "+v"(wr[m][i][1]));
      *(LDS_AS u32x4*)(d + i * 2048) = wr[m][i][0]; *(LDS_AS u32x4*)(d + i * 2048 + 1024) = wr[m][i][1];
    }
  }
  asm volatile("s_waitcnt lgkmcnt(0)\n\ts_barrier" ::: "memory");
}

DI void attention(char* smem, char* __restrict__ ws, rsrc_t rs, const Roles& R, int wid, int lane PROF_ARG) {
  lds_cptr L = (lds_cptr)smem;
  const int r = lane & 31, h = lane >> 5;
  const char* qb = ws + OFF_Q + R.head * Q_HEAD + (R.qt * 8 + wid * 2) * Q_TILE;
  const bf16x8 qaA = *(const bf16x8*)(qb + lane * 16), qaB = *(const bf16x8*)(qb + Q_TILE + lane * 16);
  u32x2 qtA = *(const u32x2*)(qb + 1024 + r * 8), qtB = *(const u32x2*)(qb + Q_TILE + 1024 + r * 8);
  if (h) { qtA = u32x2{0u, 0u}; qtB = qtA; }

  const int rm = r <= 20 ? r : (r == 24 ? 20 : 21);
  const int kaO = lane * 16, kbO = h ? 1280 + 320 : 1024 + r * 8, vO = 1280 + h * 352 + rm * 16;
  f32x16 zero, oA, oB, sA, sB;
#pragma unroll
  for (int i = 0; i < 16; ++i) { zero[i] = 0.f; oA[i] = 0.f; oB[i] = 0.f; }
  bf16x8 ka[2], v0[2], v1[2]; bf16x4 kb[2];
  lds_cptr pk = L + kaO, pb = L + kbO, pv = L + vO;
  wait_lgkm_bar();
  STAMP(1);
  kb[0] = lds_b64(pb); ka[0] = lds_b128(pk); v0[0] = lds_b128(pv); v1[0] = lds_b128(pv + 704);
  v1[1] = v1[0];
  {
    sA = mfma32(ka[0], qaA, mfma32k8(kb[0], __builtin_bit_cast(bf16x4, qtA), zero));
    sB = mfma32(ka[0], qaB, mfma32k8(kb[0], __builtin_bit_cast(bf16x4, qtB), zero));
    const unsigned nA = pk2(-rowmax16(sA), 0.f), nB = pk2(-rowmax16(sB), 0.f);
    if (h) { qtA[0] = nA; qtB[0] = nB; }
  }
  const bf16x4 qbA = __builtin_bit_cast(bf16x4, qtA), qbB = __builtin_bit_cast(bf16x4, qtB);
  sA = mfma32(ka[0], qaA, mfma32k8(kb[0], qbA, zero));
  const unsigned pcnt = (unsigned)(uintptr_t)smem + L_CNT + 4, pone = lane == 0;
  u32x4 pl = {0u, 0u, 0u, 0u};
  float e14 = 0.f, e15 = 0.f;

#define EX(i) __builtin_amdgcn_exp2f(sC[i])
  auto phase = [&](const f32x16& sC, f32x16& oC, f32x16& oP, bf16x8 vP, f32x16& sN, bf16x8 qa, bf16x4 qb4, bf16x8 kA, bf16x4 kB, bf16x8 vC, auto&& rd1, auto&& rd2) {
    u32x4 p0; float a, b, c, d;
    sN = mfma32k8(kB, qb4, zero); SBAR(); rd1(); SBAR();
    a = EX(0); SBAR(); pl[3] = pk2(e14, e15); SBAR(); b = EX(1); SBAR(); c = EX(2); SBAR(); p0[0] = pk2(a, b); SBAR(); d = EX(3); SBAR();
    oP = mfma32(vP, as_frag(pl), oP); SBAR(); rd2(); SBAR();
    a = EX(4); SBAR(); p0[1] = pk2(c, d); SBAR(); b = EX(5); SBAR(); c = EX(6); SBAR(); p0[2] = pk2(a, b); SBAR(); d = EX(7); SBAR();
    sN = mfma32(kA, qa, sN); SBAR();
    a = EX(8); SBAR(); p0[3] = pk2(c, d); SBAR(); b = EX(9); SBAR(); c = EX(10); SBAR(); pl[0] = pk2(a, b); SBAR(); d = EX(11); SBAR();
    oC = mfma32(vC, as_frag(p0), oC); SBAR(); __builtin_amdgcn_s_waitcnt(0xC07F); SBAR();
    a = EX(12); SBAR(); pl[1] = pk2(c, d); SBAR(); b = EX(13); SBAR(); e14 = EX(14); SBAR(); pl[2] = pk2(a, b); SBAR(); e15 = EX(15); SBAR();
  };
#undef EX
#pragma unroll 1
  for (int it = 0; it < 16; ++it) {
    const int dn = it == 14 ? -56 * KV_TILE : 4 * KV_TILE;
#pragma unroll
    for (int t = 0; t < 4; ++t) {
      const int c = t & 1, n = c ^ 1, no = t < 3 ? (t + 1) * KV_TILE : 0;
      phase(sA, oA, oB, v1[n], sB, qaB, qbB, ka[c], kb[c], v0[c],
            [&] {
              if (t == 0) asm volatile("s_bitcmp1_b32 %0, %1\n\ts_cbranch_scc0 .Lsk%=\n\ts_cmp_eq_u32 %1, 11\n\ts_cbranch_scc1 .Lsp%=\n\ts_barrier\n\ts_branch .Lsk%=\n"
                                       ".Lsp%=:\n\tds_add_u32 %2, %3\n.Lsk%=:" :: "s"(0x90A), "s"(it), "v"(pcnt), "v"(pone) : "scc", "memory");
              if (t == 3) { pk += dn; pb += dn; pv += dn; }
              kb[n] = lds_b64(pb + no); },
            [&] { ka[n] = lds_b128(pk + no); });
      phase(sB, oB, oA, v1[c], sA, qaA, qbA, ka[n], kb[n], v0[c], [&] { v0[n] = lds_b128(pv + no); }, [&] { v1[n] = lds_b128(pv + no + 704); });
    }
  }
  pl[3] = pk2(e14, e15);
  oB = mfma32(v1[1], as_frag(pl), oB);
  STAMP(2);

  float lA = h ? oA[8] : oA[12], lB = h ? oB[8] : oB[12];
  {
    f32x2 z0 = {0.f, 0.f}, z1 = z0;
#pragma unroll
    for (int i = 0; i < 7; ++i) { z0 = __builtin_elementwise_fma(f32x2{oA[2 * i], oA[2 * i + 1]}, f32x2{0.f, 0.f}, z0); z1 = __builtin_elementwise_fma(f32x2{oB[2 * i], oB[2 * i + 1]}, f32x2{0.f, 0.f}, z1); }
    z0 += z1;
    const bool ok = fmaxf(lA, lB) + (z0[0] + z0[1]) < 1.0e18f && fminf(lA, lB) > 1.0e-18f;
    if (__builtin_expect(!__all(ok), 0)) {
      const char* kvb = ws + OFF_KV + R.head * KV_HEAD;
      const bf16x4 z4 = {0, 0, 0, 0}, tA = h ? z4 : qbA, tB = h ? z4 : qbB;
      float mA = -INFINITY, mB = -INFINITY; oA = zero; oB = zero;
      auto upd = [&](f32x16 s, float& m, f32x16& o, bf16x8 w0, bf16x8 w1) {
        const float mn = fmaxf(m, rowmax16(s)), alpha = __builtin_amdgcn_exp2f(m - mn);
        m = mn; o *= alpha;
        u32x4 a, b;
#pragma unroll
        for (int i = 0; i < 4; ++i) { a[i] = exp_pk(s[2 * i] - mn, s[2 * i + 1] - mn); b[i] = exp_pk(s[8 + 2 * i] - mn, s[9 + 2 * i] - mn); }
        o = mfma32(w1, as_frag(b), mfma32(w0, as_frag(a), o));
      };
#pragma unroll 1
      for (int t = 0; t < 64; ++t) {
        const char* tb = kvb + t * KV_TILE;
        const bf16x8 k8 = *(const bf16x8*)(tb + kaO), w0 = *(const bf16x8*)(tb + vO), w1 = *(const bf16x8*)(tb + vO + 704);
        const bf16x4 k4 = *(const bf16x4*)(tb + kbO);
        upd(mfma32(k8, qaA, mfma32k8(k4, tA, zero)), mA, oA, w0, w1);
        upd(mfma32(k8, qaB, mfma32k8(k4, tB, zero)), mB, oB, w0, w1);
      }
      lA = h ? oA[8] : oA[12]; lB = h ? oB[8] : oB[12];
    }
  }
  const unsigned ob = OFF_OF + (R.qt * 16 + wid * 4) * 20480 + (R.head >> 1) * 1280;
  const unsigned lo = (r >> 4) * 20480 + (r & 15) * 16;
  auto fin = [&](const f32x16& o, float l, int blk) {
    const float inv = __builtin_amdgcn_rcpf(l);
    unsigned w[6];
#pragma unroll
    for (int u = 0; u < 6; ++u) w[u] = pk2(o[2 * u] * inv, o[2 * u + 1] * inv);
    const auto s0 = __builtin_amdgcn_permlane32_swap(w[0], w[2], false, false), s1 = __builtin_amdgcn_permlane32_swap(w[1], w[3], false, false);
    st16c(rs, lo + h * 256, ob + blk * 40960 + (R.head & 1) * 512, u32x4{s0[0], s1[0], s0[1], s1[1]});
    if (!h) st8c(rs, lo, ob + blk * 40960 + 1024 + (R.head & 1) * 8, u32x2{w[4], w[5]});
  };
  fin(oA, lA, 0); fin(oB, lB, 1);
}

#define P_DEPTH 14
#define P_PRO 4
#define P_RAMP 3
constexpr int ld_pos(int s, int pro, int ramp, int depth, int n) {
  int j = 2 * pro;
  for (int i = 0; i < s; ++i) { j += ramp; if (j > 2 * (i + 1 + depth)) j = 2 * (i + 1 + depth); if (j > 2 * n) j = 2 * n; }
  return j;
}
template <int MODE>
DI void proj(char* smem, const char* __restrict__ x, rsrc_t rs, char* __restrict__ out, const Roles& R, int wid, int lane PROF_ARG) {
  constexpr int NST = MODE ? 27 : 10, NKS = 2 * NST, DEPTH = MODE ? P_DEPTH : 10, PRO = P_PRO, RAMP = P_RAMP;
  lds_cptr L = (lds_cptr)smem;
  const int rh = wid >> 1, kp = wid & 1, t16 = lane & 15, g = lane >> 4;
  const int mf = R.rt * 4 + 2 * rh + kp, tok = mf * 16 + t16;
  const int ODD = R.ct & 1, U0 = (R.ct >> 1) * 5 + 3 * ODD, UH = (R.ct >> 1) * 5 + 2;
  const unsigned hp = OFF_HF + (mf * 20 + U0) * 1024, hh = OFF_HF + (mf * 20 + UH) * 1024 + ODD * 512;
  const unsigned fo = lane * 16, fh = (g >> 1) * 256 + t16 * 16 + (g & 1) * 8;
  const unsigned ro = tok * 1280 + U0 * 64 + g * 16, rhh = tok * 1280 + UH * 64 + ODD * 32 + g * 8;
  const unsigned ab = (MODE ? OFF_ACT : OFF_OF) + (mf * NKS + kp) * 1024, ab1 = ab + (1 - 2 * kp) * NKS * 1024;
  lds_cptr B = L + (MODE ? 0 : L_WO) + kp * 1024 + lane * 16, B1 = B + 6 * D_STAGE, B2 = B + 12 * D_STAGE;
  asm volatile("" : "+v"(B1), "+v"(B2));
  u32x4 res0, res1; u32x2 resh;
  if (MODE == 0) { res0 = *(const u32x4*)(x + ro); res1 = *(const u32x4*)(x + ro + 64); resh = *(const u32x2*)(x + rhh); }
  else { res0 = ld16c(rs, fo, hp); res1 = ld16c(rs, fo, hp + 1024); resh = ld8c(rs, fh, hh); }
  f32x4 acc[2][5];
  acc[0][0] = f32x4{bflo(res0[0]), bfhi(res0[0]), bflo(res0[1]), bfhi(res0[1])}; acc[0][1] = f32x4{bflo(res0[2]), bfhi(res0[2]), bflo(res0[3]), bfhi(res0[3])};
  acc[0][2] = f32x4{bflo(res1[0]), bfhi(res1[0]), bflo(res1[1]), bfhi(res1[1])}; acc[0][3] = f32x4{bflo(res1[2]), bfhi(res1[2]), bflo(res1[3]), bfhi(res1[3])};
  acc[0][4] = f32x4{bflo(resh[0]), bfhi(resh[0]), bflo(resh[1]), bfhi(resh[1])};
  asm volatile("" : "+v"(acc[0][0]), "+v"(acc[0][1]), "+v"(acc[0][2]), "+v"(acc[0][3]), "+v"(acc[0][4]));
  SBAR(); asm volatile("s_barrier" ::: "memory"); SBAR();
  STAMP(MODE ? 10 : 4);
  u32x4 a[DEPTH + 1][2];
  bf16x8 b[2][5];
  auto lda = [&](int j) { a[(j >> 1) % (DEPTH + 1)][j & 1] = ld16c(rs, fo, (j & 1 ? ab1 : ab) + (j >> 1) * 2048); };
  auto ldb = [&](int s, int v) { constexpr int W = 6 * D_STAGE; const int o = MODE ? ((s + 12) % 15) * D_STAGE + v * 2048 : s * 2048 + v * 20480; b[s & 1][v] = lds_b128((o < W ? B : o < 2 * W ? B1 : B2) + o % W); };
#pragma unroll
  for (int j = 0; j < 2 * PRO; ++j) lda(j);
#pragma unroll
  for (int v = 0; v < 5; ++v) ldb(0, v);
  SBAR();
  static_for<0, NST>([&](auto S) {
    constexpr int s = S.value, c = s % (DEPTH + 1);
    constexpr int j0 = ld_pos(s, PRO, RAMP, DEPTH, NST), j1 = ld_pos(s + 1, PRO, RAMP, DEPTH, NST);
    if (MODE && (s + 1) % 3 == 0 && s + 1 < 16) wait_lgkm_bar();
#pragma unroll
    for (int u = 0; u < 2; ++u)
#pragma unroll
      for (int v = 0; v < 5; ++v) {
        if (s || !u) mfma16a(acc[u][v], b[s & 1][v], a[c][u]); else mfma16z(acc[u][v], b[0][v], a[0][u]);
        if (u == 0 && s + 1 < NST) ldb(s + 1, v);
        if (u == 1 && j0 + v < j1) lda(j0 + v);
        SBAR();
      }
  });
  asm volatile("s_nop 7" ::: "memory");
  SBAR();
  STAMP(MODE ? 11 : 5);
  lds_ptr scr = (lds_ptr)smem + (MODE ? 9 * D_STAGE : L_RED) + lane * 16;
#pragma unroll
  for (int v = 0; v < 5; ++v) *(LDS_AS f32x4*)(scr + wid * 5120 + v * 1024) = acc[1][v];
  wait_lgkm_bar();
  f32x4 y[5];
#pragma unroll
  for (int v = 0; v < 5; ++v) y[v] = *(const LDS_AS f32x4*)(scr + (wid ^ 1) * 5120 + v * 1024);
  SBAR();
  unsigned o[10];
#pragma unroll
  for (int v = 0; v < 5; ++v) { y[v] += acc[0][v]; o[2 * v] = pk2(y[v][0], y[v][1]); o[2 * v + 1] = pk2(y[v][2], y[v][3]); }
  if (MODE == 0) {
    st16c(rs, fo, hp, u32x4{o[0], o[1], o[2], o[3]}); st16c(rs, fo, hp + 1024, u32x4{o[4], o[5], o[6], o[7]}); st8c(rs, fh, hh, u32x2{o[8], o[9]});
    float s0 = 0.f, s1 = 0.f, q0 = 0.f, q1 = 0.f;
    const bf2 one2 = __builtin_bit_cast(bf2, 0x3f803f80u);
#pragma unroll
    for (int i = 0; i < 5; ++i) {
      const bf2 e0 = __builtin_bit_cast(bf2, o[2 * i]), e1 = __builtin_bit_cast(bf2, o[2 * i + 1]);
      s0 = __builtin_amdgcn_fdot2_f32_bf16(e0, one2, s0, false); s1 = __builtin_amdgcn_fdot2_f32_bf16(e1, one2, s1, false);
      q0 = __builtin_amdgcn_fdot2_f32_bf16(e0, e0, q0, false); q1 = __builtin_amdgcn_fdot2_f32_bf16(e1, e1, q1, false);
    }
    const float ssum = xsum32(xsum16(s0 + s1)), ssq = xsum32(xsum16(q0 + q1));
    if (g == 0) st8c(rs, tok * 64, OFF_STAT + R.ct * 8, u32x2{__float_as_uint(ssum), __float_as_uint(ssq)});
  } else {
    *(u32x4*)(out + ro) = u32x4{o[0], o[1], o[2], o[3]}; *(u32x4*)(out + ro + 64) = u32x4{o[4], o[5], o[6], o[7]}; *(u32x2*)(out + rhh) = u32x2{o[8], o[9]};
  }
}

DI void gateup(char* smem, rsrc_t rs, const Roles& R, int wid, int lane PROF_ARG) {
  constexpr int DEPTH = 6;
  const int t16 = lane & 15, g = lane >> 4, mf = R.rg * 8 + 2 * wid;
  lds_cptr B0 = (lds_cptr)smem + lane * 16, B1 = B0 + 2 * G_STAGE, B2 = B0 + 4 * G_STAGE;
  asm volatile("" : "+v"(B1), "+v"(B2));
  const unsigned fo = lane * 16, ab = OFF_HF + mf * 20480;
  SBAR(); asm volatile("s_barrier" ::: "memory"); SBAR();
  STAMP(7);
  u32x4 a[DEPTH][2];
  bf16x8 b[14];
  f32x4 acc[2][14];
  auto lda = [&](int k, int u) { a[k % DEPTH][u] = ld16c(rs, fo, ab + XK(k) * 1024 + u * 20480); };
  auto ldb = [&](int k, int v) {
    const int slot = ((k >> 1) + 4) % 5;
    b[v] = lds_b128((slot < 2 ? B0 : slot < 4 ? B1 : B2) + (slot & 1 & (slot < 4)) * G_STAGE + (k & 1) * 1024 + v * 2048);
  };
#pragma unroll
  for (int k = 0; k < DEPTH - 1; ++k) { lda(k, 0); lda(k, 1); }
  u32x4 st0, st1;
#pragma unroll
  for (int v = 0; v < 14; ++v) ldb(0, v);
  SBAR();
  static_for<0, 20>([&](auto S) {
    constexpr int k = S.value;
    if (k & 1) { asm volatile("s_barrier" ::: "memory"); SBAR(); }
#pragma unroll
    for (int v = 0; v < 14; ++v) {
      if (k) { mfma16a(acc[0][v], b[v], a[k % DEPTH][0]); mfma16a(acc[1][v], b[v], a[k % DEPTH][1]); }
      else   { mfma16z(acc[0][v], b[v], a[0][0]); mfma16z(acc[1][v], b[v], a[0][1]); }
      if (k < 19) ldb(k + 1, v);
      if (v < 2 && k + DEPTH - 1 < 20) lda(k + DEPTH - 1, v);
      if (v == 0 && k + DEPTH - 1 == 20) st0 = ld16c(rs, t16 * 64 + g * 16, OFF_STAT + mf * 1024);
      if (v == 1 && k + DEPTH - 1 == 20) st1 = ld16c(rs, t16 * 64 + g * 16, OFF_STAT + mf * 1024 + 1024);
      SBAR();
    }
  });
  asm volatile("s_barrier\n\ts_nop 7" ::: "memory");
  SBAR();
  STAMP(8);
  float nm[2], vk[2], c1[2];
#pragma unroll
  for (int u = 0; u < 2; ++u) {
    const f32x4 s = __builtin_bit_cast(f32x4, u ? st1 : st0);
    const float mean = xsum32(xsum16(s[0] + s[2])) * (1.0f / DM);
    nm[u] = -mean; vk[u] = fmaxf(xsum32(xsum16(s[1] + s[3])) * (1.0f / DM) - mean * mean, 0.f) + LN_EPS;
    c1[u] = -LOG2E * __builtin_amdgcn_rsqf(vk[u]);
  }
  const LDS_AS f32x4* cs = (const LDS_AS f32x4*)((lds_cptr)smem + L_CS) + g;
  auto swi = [&](int u, int p) {
    const f32x4 gt = acc[u][2 * p] + cs[8 * p] * nm[u], up = acc[u][2 * p + 1] + cs[8 * p + 4] * nm[u], e = gt * c1[u], n = gt * up;
    f32x4 d;
#pragma unroll
    for (int i = 0; i < 4; ++i) d[i] = __builtin_amdgcn_exp2f(e[i]);
    d = d * vk[u] + vk[u];
#pragma unroll
    for (int i = 0; i < 4; ++i) d[i] = __builtin_amdgcn_rcpf(d[i]);
    d *= n;
    return u32x2{pk2(d[0], d[1]), pk2(d[2], d[3])};
  };
  const unsigned ob = OFF_ACT + mf * 54 * 1024;
#pragma unroll
  for (int i = 0; i < 3; ++i)
#pragma unroll
    for (int u = 0; u < 2; ++u) {
      const u32x2 pa = swi(u, 2 * i), pb = swi(u, 2 * i + 1);
      st16c(rs, fo, ob + (u * 54 + R.UB + i) * 1024, u32x4{pa[0], pa[1], pb[0], pb[1]});
    }
  if ((R.cg & 7) < 6) {
    const u32x2 pa = swi(0, 6), pb = swi(1, 6);
    const auto s0 = __builtin_amdgcn_permlane16_swap(pa[0], pb[0], false, false), s1 = __builtin_amdgcn_permlane16_swap(pa[1], pb[1], false, false);
    st16c(rs, (g & 1) * 54 * 1024 + (g >> 1) * 256 + t16 * 16, ob + R.UL * 1024 + (R.cg & 1) * 512, u32x4{s0[0], s1[0], s0[1], s1[1]});
  }
}

extern "C" __global__ __launch_bounds__(512, 2) void k_main(const char* __restrict__ x, const char* __restrict__ wo, const char* __restrict__ wg,
                                                            const char* __restrict__ wu, const char* __restrict__ wd, char* __restrict__ ws,
                                                            char* __restrict__ out) {
  __shared__ __attribute__((aligned(16))) char smem[163840];
  const int wid = __builtin_amdgcn_readfirstlane(threadIdx.x >> 6);
  PROF_DECL STAMP(0);
  const Roles R(blockIdx.x);
  const rsrc_t rs = __builtin_amdgcn_make_buffer_rsrc(ws, 0, (int)WS_BYTES, 0x00020000);
  if (wid >= 4) {
    helper_waves(smem, wo, wg, wu, wd, ws, rs, R, wid - 4, lane_id() PROF_PASS);
  } else {
    attention(smem, ws, rs, R, wid, lane_id() PROF_PASS);
    STX(1); wait_vm<0>(); STX(2);
    wait_vm_bar<0>();
    STAMP(3);
    proj<0>(smem, x, rs, out, R, wid, lane_id() PROF_PASS);
    STX(3); wait_vm<0>(); STX(4);
    wait_vm_bar<0>();
    STAMP(6);
    gateup(smem, rs, R, wid, lane_id() PROF_PASS);
    STX(5); wait_vm<0>(); STX(6);
    wait_vm_bar<0>();
    STAMP(9);
    proj<1>(smem, x, rs, out, R, wid, lane_id() PROF_PASS);
  }
  PROF_END(1);
}
extern "C" __global__ void k_end() {}

__global__ void k_pre(char* ws) { unsigned long long t; asm volatile("s_memrealtime %0\n\ts_waitcnt lgkmcnt(0)" : "=s"(t) :: "memory"); *(unsigned long long*)(ws + (130ull << 20)) = t; }
__global__ void k_report(char* ws) {
  __shared__ unsigned long long smin[2], smax[2], ssum, scnt;
  const int b = threadIdx.x;
  unsigned long long tr; asm volatile("s_memrealtime %0\n\ts_waitcnt lgkmcnt(0)" : "=s"(tr) :: "memory");
  if (b < 2) { smin[b] = ~0ull; smax[b] = 0; ssum = 0; scnt = 0; }
  __syncthreads();
  const unsigned long long* pr = (const unsigned long long*)(ws + (128ull << 20));
  const bool idle = ((b & 1) * 8 + (b >> 5)) >= 15;
  for (int k = 0; k < 2; ++k) {
    if (k == 0 && idle) continue;
    const unsigned long long* t = pr + ((k * 256 + b) * 8) * 16;
    atomicMin(&smin[k], t[14]); atomicMax(&smax[k], t[15]);
  }
  __syncthreads();
  if (MEAS >= 10) {
    const int M = MEAS % 100, k = M >= 30 && M < 50 ? 0 : 1, w = M >= 50 ? 4 : 0, i = (M - 10) % 20;
    const unsigned long long* t = pr + ((k * 256 + b) * 8 + w) * 16;
    if (!(k == 0 && idle)) { atomicAdd(&ssum, MEAS >= 100 ? (t[i] - t[0]) / 20 : t[i] - smin[0]); atomicAdd(&scnt, 1ull); }
  }
  __syncthreads();
  long long x = 0;
  if (MEAS == 1) x = smax[0] - smin[0];
  if (MEAS == 2) x = smin[1] - smin[0];
  if (MEAS == 3) x = smax[1] - smin[0];
  if (MEAS == 4) x = tr - smax[1];
  if (MEAS == 5) x = smin[0] - *(const unsigned long long*)(ws + (130ull << 20));
  if (MEAS == 7) { unsigned long long t2; asm volatile("s_memrealtime %0\n\ts_waitcnt lgkmcnt(0)" : "=s"(t2) :: "memory"); x = t2 - tr; }
  if (MEAS >= 10) x = ssum / scnt;
  if (x < 0) x = 0; if (x > 10000) x = 10000;
  unsigned long long t0, t1;
  asm volatile("s_memrealtime %0\n\ts_waitcnt lgkmcnt(0)" : "=s"(t0) :: "memory");
  do { asm volatile("s_memrealtime %0\n\ts_waitcnt lgkmcnt(0)" : "=s"(t1) :: "memory"); } while ((long long)(t1 - t0) < FACT * x);
}
extern "C" void kernel_launch(void* const* d_in, const int* in_sizes, int n_in, void* d_out, int out_size, void* d_ws, size_t ws_size,
                              hipStream_t stream) {
  const char* x = (const char*)d_in[0]; const char* wqkv = (const char*)d_in[1]; const char* wo = (const char*)d_in[2];
  const char* wg = (const char*)d_in[4]; const char* wu = (const char*)d_in[5]; const char* wd = (const char*)d_in[6];
  char* ws = (char*)d_ws;
  k_qkv<<<256, 512, 0, stream>>>(x, wqkv, ws);
  k_main<<<256, 512, 0, stream>>>(x, wo, wg, wu, wd, ws, (char*)d_out);
  k_report<<<1, 256, 0, stream>>>(ws);
}
```

```cpp
#include <hip/hip_runtime.h>
#include <hip/hip_bf16.h>
#include <stdint.h>

typedef __attribute__((ext_vector_type(8))) short bf16x8;
typedef __attribute__((ext_vector_type(4))) short bf16x4;
typedef __attribute__((ext_vector_type(2))) float f32x2;
typedef __attribute__((ext_vector_type(4))) float f32x4;
typedef __attribute__((ext_vector_type(16))) float f32x16;
typedef __attribute__((ext_vector_type(4))) unsigned u32x4;
typedef __attribute__((ext_vector_type(2))) unsigned u32x2;
typedef __attribute__((ext_vector_type(2))) __bf16 bf2;
typedef __attribute__((address_space(3))) const char* lds_cptr;
typedef __attribute__((address_space(3))) char* lds_ptr;
#define LDS_AS __attribute__((address_space(3)))
#define DI __device__ __forceinline__

constexpr int SEQ = 2048, DM = 640, NH = 32, DH = 20, FF = 1728;
constexpr float LN_EPS = 1e-5f;
constexpr float LOG2E = 1.4426950408889634f;
constexpr float QSCALE = 0.22360679774997896f * LOG2E;

constexpr int Q_TILE = 1280;
constexpr int Q_HEAD = 64 * Q_TILE;
constexpr int KV_TILE = 2688;
constexpr int KV_HEAD = 64 * KV_TILE;
constexpr unsigned OFF_Q    = 0;
constexpr unsigned OFF_KV   = OFF_Q + NH * Q_HEAD;
constexpr unsigned OFF_OF   = OFF_KV + NH * KV_HEAD;
constexpr unsigned OFF_HF   = OFF_OF + SEQ * DM * 2;
constexpr unsigned OFF_ACT  = OFF_HF + SEQ * DM * 2;
constexpr unsigned OFF_WOF  = OFF_ACT + SEQ * FF * 2;
constexpr unsigned OFF_WGUF = OFF_WOF + DM * DM * 2;
constexpr unsigned OFF_WDF  = OFF_WGUF + DM * FF * 4;
constexpr unsigned OFF_STAT = OFF_WDF + FF * DM * 2;
constexpr unsigned OFF_CSUM = OFF_STAT + SEQ * 64;
constexpr unsigned OFF_FLAG = OFF_CSUM + 20 * 216 * 64;
constexpr unsigned FL_O = OFF_FLAG, FL_H = FL_O + 64 * 256, FL_ACT = FL_H + 128 * 256, FL_W = FL_ACT + 128 * 256;
constexpr int N_FLAGS = 384;
constexpr unsigned WS_BYTES = OFF_FLAG + N_FLAGS * 256;

#define PROF_DECL
#define STAMP(i)
#define PROF_END(k)
#define PROF_ARG
#define PROF_PASS
#define STXN 0
#define STX(n) do { if (n == STXN) STAMP(12); } while (0)
template <int N> DI void glds(unsigned voff, const void* sbase, unsigned lds, int imm) {
#define GL(o) "\n\tglobal_load_lds_dwordx4 %0, %1 offset:" #o
  if (N == 1) asm volatile("s_add_u32 m0, %2, %3\n\ts_nop 0" GL(0) :: "v"(voff), "s"(sbase), "s"(lds), "ns"(imm) : "memory", "scc");
  if (N == 2) asm volatile("s_add_u32 m0, %2, %3\n\ts_nop 0" GL(0) GL(1024) :: "v"(voff), "s"(sbase), "s"(lds), "ns"(imm) : "memory", "scc");
  if (N == 4) asm volatile("s_add_u32 m0, %2, %3\n\ts_nop 0" GL(0) GL(1024) GL(2048) GL(3072) :: "v"(voff), "s"(sbase), "s"(lds), "ns"(imm) : "memory", "scc");
#undef GL
}
#define CODE_TOUCH(first, last, part, sink) do { unsigned long long pc_; int a_, b_; \
  asm volatile("s_getpc_b64 %0\n.Lct%=:\n\ts_mov_b32 %1, " #first "-.Lct%=\n\ts_mov_b32 %2, " #last "-.Lct%=" : "=s"(pc_), "=s"(a_), "=s"(b_)); \
  const int o_ = min(lane * 128 + (part) * 8192, b_ - a_); \
  asm volatile("global_load_dword %0, %1, %2" : "=v"(sink) : "v"(o_), "s"(pc_ + (long long)a_) : "memory"); } while (0)
#define CODE_DONE(sink) asm volatile("s_waitcnt vmcnt(0)" :: "v"(sink) : "memory")
typedef __attribute__((address_space(1))) unsigned gu32;
typedef __amdgpu_buffer_rsrc_t rsrc_t;
#define LDAUX 16
#define XK(k) (k)
DI u32x4 ld16c(rsrc_t rs, unsigned vo, unsigned so) { return __builtin_amdgcn_raw_buffer_load_b128(rs, vo, so, LDAUX); }
DI u32x2 ld8c(rsrc_t rs, unsigned vo, unsigned so) { return __builtin_amdgcn_raw_buffer_load_b64(rs, vo, so, 16); }
DI void st16c(rsrc_t rs, unsigned vo, unsigned so, u32x4 v) { __builtin_amdgcn_raw_buffer_store_b128(v, rs, vo + so, 0, 16); }
DI void st8c(rsrc_t rs, unsigned vo, unsigned so, u32x2 v) { __builtin_amdgcn_raw_buffer_store_b64(v, rs, vo, so, 16); }
DI void await8(char* ws, unsigned off, int lane, unsigned want) {
#pragma unroll 1
  for (int n = 0; n < (1 << 16); ++n) {
    const unsigned v = __hip_atomic_load((gu32*)(ws + off + (lane & 7) * 256), __ATOMIC_RELAXED, __HIP_MEMORY_SCOPE_AGENT);
    if (__all(v >= want)) break;
    __builtin_amdgcn_s_sleep(1);
  }
}
DI void await(char* ws, unsigned off, unsigned want) {
#pragma unroll 1
  for (int n = 0; n < (1 << 16); ++n) {
    const unsigned v = __hip_atomic_load((gu32*)(ws + off), __ATOMIC_RELAXED, __HIP_MEMORY_SCOPE_AGENT);
    if ((unsigned)__builtin_amdgcn_readfirstlane(v) >= want) break;
    __builtin_amdgcn_s_sleep(1);
  }
  __builtin_amdgcn_fence(__ATOMIC_ACQUIRE, "wavefront");
}
template <int N> DI void wait_vm_bar() { asm volatile("s_waitcnt vmcnt(%0)\n\ts_barrier" :: "n"(N) : "memory"); }
template <int N> DI void wait_vm() { asm volatile("s_waitcnt vmcnt(%0)" :: "n"(N) : "memory"); }
#define SBAR() __builtin_amdgcn_sched_barrier(0)
#define SGB(mask, n) __builtin_amdgcn_sched_group_barrier(mask, n, 0)
DI void wait_lgkm_bar() { SBAR(); __builtin_amdgcn_s_waitcnt(0xC07F); asm volatile("s_barrier" ::: "memory"); SBAR(); }
#define GEMM_OFF 0
template <int I> struct IC { static constexpr int value = I; };
template <int B, int E, class F> DI void static_for(F&& f) { if constexpr (B < E) { f(IC<B>{}); static_for<B + 1, E>(f); } }
DI bf16x8 lds_b128(lds_cptr p) { return *(const LDS_AS bf16x8*)p; }
DI bf16x4 lds_b64(lds_cptr p) { return *(const LDS_AS bf16x4*)p; }
DI bf16x4 lds_tr(lds_cptr p) { return __builtin_amdgcn_ds_read_tr16_b64_v4i16((LDS_AS bf16x4*)p); }
DI f32x4 mfma16(bf16x8 a, bf16x8 b, f32x4 c) { return __builtin_amdgcn_mfma_f32_16x16x32_bf16(a, b, c, 0, 0, 0); }
DI void mfma16a(f32x4& c, bf16x8 a, u32x4 b) { asm volatile("v_mfma_f32_16x16x32_bf16 %0, %1, %2, %0" : "+v"(c) : "v"(a), "v"(b)); }
DI void mfma16z(f32x4& c, bf16x8 a, u32x4 b) { asm volatile("v_mfma_f32_16x16x32_bf16 %0, %1, %2, 0" : "=&v"(c) : "v"(a), "v"(b)); }
DI f32x16 mfma32(bf16x8 a, bf16x8 b, f32x16 c) { return __builtin_amdgcn_mfma_f32_32x32x16_bf16(a, b, c, 0, 0, 0); }
DI f32x16 mfma32k8(bf16x4 a, bf16x4 b, f32x16 c) { return __builtin_amdgcn_mfma_f32_32x32x8bf16_1k(a, b, c, 0, 0, 0); }
DI unsigned pk2(float lo, float hi) { f32x2 v = {lo, hi}; return __builtin_bit_cast(unsigned, __builtin_convertvector(v, bf2)); }
DI float bflo(unsigned u) { return __uint_as_float(u << 16); }
DI float bfhi(unsigned u) { return __uint_as_float(u & 0xffff0000u); }
DI int div20(int c) { return (c * 3277) >> 16; }
DI float xsum32(float v) { auto r = __builtin_amdgcn_permlane32_swap(__float_as_uint(v), __float_as_uint(v), false, false); return __uint_as_float(r[0]) + __uint_as_float(r[1]); }
DI float xsum16(float v) { auto r = __builtin_amdgcn_permlane16_swap(__float_as_uint(v), __float_as_uint(v), false, false); return __uint_as_float(r[0]) + __uint_as_float(r[1]); }
constexpr bf16x8 ONES8 = {0x3f80, 0x3f80, 0x3f80, 0x3f80, 0x3f80, 0x3f80, 0x3f80, 0x3f80};

template <int MODE>
DI void qkv_compute(lds_cptr L, lds_ptr scr, int wid, int lane, int row0, int col0, char* __restrict__ ws PROF_ARG) {
  const int wr = wid >> 1, wc = wid & 1, r16 = lane & 15, g = lane >> 4, q = r16 >> 2, p = lane & 3;
  const int f = q | ((g & 1) << 2);
  const int rp = MODE == 2 ? 16 * (r16 >> 3) + (r16 & 7) : r16;
  constexpr int RSTEP = MODE == 2 ? 8 : 16;
  int aoff[2][2], boff[4];
#pragma unroll
  for (int e = 0; e < 2; ++e)
#pragma unroll
    for (int ks = 0; ks < 2; ++ks) { const int r = rp + e * RSTEP; aoff[e][ks] = (64 * wr + r) * 128 + (((4 * ks + g) ^ ((r >> 1) & 7) ^ ((r >> 2) & 4)) << 4); }
#pragma unroll
  for (int jj = 0; jj < 4; ++jj) { const int j = (jj + 2 * wr) & 3; boff[jj] = 16384 + (8 * g + q) * 256 + ((((4 * wc + j) ^ f) * 2 + (p >> 1)) << 4) + (p & 1) * 8; }
  const int pr0 = wc * 4096, pr1 = 4096 - pr0;

  f32x4 acc[4][4], rs[2], gr[2], cs[2];
#pragma unroll
  for (int i = 0; i < 4; ++i)
#pragma unroll
    for (int j = 0; j < 4; ++j) acc[i][j] = f32x4{0.f, 0.f, 0.f, 0.f};
#pragma unroll
  for (int i = 0; i < 2; ++i) { rs[i] = f32x4{0.f, 0.f, 0.f, 0.f}; gr[i] = rs[i]; cs[i] = rs[i]; }
  bf16x8 a0[4], b0[4], a1[4], b1[4];

  auto rd = [&](bf16x8 (&a)[4], bf16x8 (&b)[4], int slot, int ks) {
    if (GEMM_OFF && slot + ks > 0) return;
    lds_cptr S = L + slot;
#pragma unroll
    for (int jj = 0; jj < 4; ++jj) {
      bf16x4 lo = lds_tr(S + boff[jj] + ks * 8192), hi = lds_tr(S + boff[jj] + ks * 8192 + 1024);
      b[jj] = __builtin_shufflevector(lo, hi, 0, 1, 2, 3, 4, 5, 6, 7);
      a[jj] = lds_b128(S + aoff[jj & 1][ks] + (jj < 2 ? pr0 : pr1));
    }
  };
  auto mm = [&](bf16x8 (&a)[4], bf16x8 (&b)[4]) {
    if (GEMM_OFF) return;
#pragma unroll
    for (int ii = 0; ii < 4; ++ii)
#pragma unroll
      for (int jj = 0; jj < 4; ++jj)
        acc[ii][jj] = (MODE == 2) ? mfma16(a[ii], b[jj], acc[ii][jj]) : mfma16(b[jj], a[ii], acc[ii][jj]);
#pragma unroll
    for (int u = 0; u < 2; ++u) {
      rs[u] = mfma16(a[u], ONES8, rs[u]);
      gr[u] = mfma16(a[u], a[u], gr[u]);
      cs[u] = mfma16(ONES8, b[u], cs[u]);
    }
  };
  auto ilv = [&] {
#pragma unroll
    for (int i = 0; i < 12; ++i) { SGB(0x8, 1); SGB(0x100, 1); }
    SGB(0x8, 10);
    SBAR();
  };

  LDS_AS float* rsum = (LDS_AS float*)scr; LDS_AS float* rsq = rsum + 128; LDS_AS float* csum = rsum + 256;
  lds_ptr dead = (lds_ptr)L + 65536 + wid * 2048 + lane * 16;
  lds_ptr xr = r16 == 0 ? (lds_ptr)(rsum + 64 * wr + 32 * wc + 4 * g) : dead, xq = q == g ? (lds_ptr)(rsq + 64 * wr + 32 * wc + r16) : dead;
  lds_ptr xc = g == 0 ? (lds_ptr)(csum + 64 * wc + 32 * wr + r16) : dead;
  const int sl = MODE == 2 ? 4 * g : r16, cl = MODE == 2 ? r16 : 4 * g;
  const LDS_AS float* yr[2] = {rsum + 64 * wr + 32 * wc + sl, rsum + 64 * wr + 32 * (1 - wc) + sl};
  const LDS_AS float* yc[2] = {csum + 64 * wc + 32 * wr + cl, csum + 64 * wc + 32 * (1 - wr) + cl};
  asm volatile("" : "+v"(xr), "+v"(xq), "+v"(xc), "+v"(yr[0]), "+v"(yr[1]), "+v"(yc[0]), "+v"(yc[1]));
  if (MODE == 2) {
    const int head = div20(col0 - 2 * DM + 19) + (lane >> 3), t = lane & 7;
    if (head * DH < col0 - 2 * DM + 128)
      *(u32x4*)(ws + OFF_KV + ((row0 >> 5) + 2 * wr) * KV_TILE + head * KV_HEAD + (t >> 2) * KV_TILE + 1280 + (t & 3) * 352 + 320 + wc * 16) = wc ? u32x4{0u, 0u, 0u, 0u} : __builtin_bit_cast(u32x4, ONES8);
  }
  unsigned off[4][2];
#pragma unroll
  for (int jj = 0; jj < 4; ++jj) {
    if (MODE != 2) {
      const int ck = col0 - MODE * DM + 64 * wc + 16 * ((jj + 2 * wr) & 3) + 4 * g, head = div20(ck), d = ck - head * DH;
      off[jj][0] = head * (MODE == 0 ? Q_HEAD : KV_HEAD) + (d < 16 ? (d >> 3) * 512 + (d & 7) * 2 + r16 * 16 : 1024 + r16 * 8);
      off[jj][1] = off[jj][0] + (d < 16 ? 256 : 128);
      asm volatile("" : "+v"(off[jj][0]), "+v"(off[jj][1]));
    } else {
      const int cv = col0 - 2 * DM + 64 * wc + 16 * ((jj + 2 * wr) & 3) + r16, head = div20(cv), d = cv - head * DH;
      off[jj][0] = head * KV_HEAD + 1280 + g * 352 + d * 16;
      asm volatile("" : "+v"(off[jj][0]));
    }
  }
#pragma unroll
  for (int i = 0; i < 4; ++i) asm volatile("" : "+v"(acc[i][0]), "+v"(acc[i][1]), "+v"(acc[i][2]), "+v"(acc[i][3]));
  asm volatile("" : "+v"(rs[0]), "+v"(rs[1]), "+v"(gr[0]), "+v"(gr[1]), "+v"(cs[0]), "+v"(cs[1]));
  wait_lgkm_bar();
  STAMP(1);
  rd(a0, b0, 0, 0);
  SBAR();
#pragma unroll 1
  for (int kt = 0; kt < 9; ++kt) {
    rd(a1, b1, (kt & 3) * 32768, 1); mm(a0, b0); ilv();
    wait_lgkm_bar();
    rd(a0, b0, ((kt + 1) & 3) * 32768, 0); mm(a1, b1); ilv();
  }
  rd(a1, b1, 32768, 1); mm(a0, b0); ilv();
  mm(a1, b1);
  SBAR();
  STAMP(2);
#pragma unroll
  for (int u = 0; u < 2; ++u) {
    *(LDS_AS f32x4*)(xr + 64 * u) = rs[u];
    *(LDS_AS float*)(xq + 64 * u) = p == 0 ? gr[u][0] : p == 1 ? gr[u][1] : p == 2 ? gr[u][2] : gr[u][3];
    *(LDS_AS float*)(xc + 64 * u) = cs[u][0];
  }
  constexpr float invD = 1.0f / DM;
  if (MODE != 2) {
    constexpr int TS = MODE == 0 ? Q_TILE : KV_TILE;
    char* base = ws + (MODE == 0 ? OFF_Q : OFF_KV) + ((row0 >> 5) + 2 * wr) * TS;
    wait_lgkm_bar();
    float sm[4], sq[4];
    f32x4 c4[4];
#pragma unroll
    for (int ii = 0; ii < 4; ++ii) { sm[ii] = yr[ii >> 1][16 * (ii & 1)]; sq[ii] = yr[ii >> 1][128 + 16 * (ii & 1)]; }
#pragma unroll
    for (int jj = 0; jj < 4; ++jj) c4[jj] = *(const LDS_AS f32x4*)(yc[jj >> 1] + 16 * (jj & 1));
    SBAR();
#pragma unroll
    for (int ii = 0; ii < 4; ++ii) {
      const int half = ((ii >> 1) + wc) & 1;
      const float mean = sm[ii] * invD, var = fmaxf(sq[ii] * invD - mean * mean, 0.f);
      const float rstd = __builtin_amdgcn_rsqf(var + LN_EPS) * (MODE == 0 ? QSCALE : 1.0f), nb = -mean * rstd;
      char* tb = base + half * TS;
#pragma unroll
      for (int jj = 0; jj < 4; ++jj) {
        const f32x4 v = acc[ii][jj] * rstd + c4[jj] * nb;
        *(u32x2*)(tb + off[jj][ii & 1]) = u32x2{pk2(v[0], v[1]), pk2(v[2], v[3])};
      }
    }
  } else {
    char* base = ws + OFF_KV + ((row0 >> 5) + 2 * wr) * KV_TILE;
    wait_lgkm_bar();
    float c1[4];
    f32x4 sm[4], sq[4];
#pragma unroll
    for (int ii = 0; ii < 4; ++ii) { sm[ii] = *(const LDS_AS f32x4*)(yr[ii >> 1] + 16 * (ii & 1)); sq[ii] = *(const LDS_AS f32x4*)(yr[ii >> 1] + 128 + 16 * (ii & 1)); }
#pragma unroll
    for (int jj = 0; jj < 4; ++jj) c1[jj] = yc[jj >> 1][16 * (jj & 1)];
    SBAR();
#pragma unroll
    for (int ip = 0; ip < 2; ++ip) {
      const int half = (ip + wc) & 1;
      f32x4 rstd[2], nb[2];
#pragma unroll
      for (int e = 0; e < 2; ++e) {
        const f32x4 mean = sm[2 * ip + e] * invD, q4 = sq[2 * ip + e];
#pragma unroll
        for (int i = 0; i < 4; ++i) rstd[e][i] = __builtin_amdgcn_rsqf(fmaxf(q4[i] * invD - mean[i] * mean[i], 0.f) + LN_EPS);
        nb[e] = -mean * rstd[e];
      }
      char* tb = base + half * KV_TILE;
#pragma unroll
      for (int jj = 0; jj < 4; ++jj) {
        const f32x4 v0 = acc[2 * ip][jj] * rstd[0] + nb[0] * c1[jj], v1 = acc[2 * ip + 1][jj] * rstd[1] + nb[1] * c1[jj];
        *(u32x4*)(tb + off[jj][0]) = u32x4{pk2(v0[0], v0[1]), pk2(v0[2], v0[3]), pk2(v1[0], v1[1]), pk2(v1[2], v1[3])};
      }
    }
  }
}

extern "C" __global__ __launch_bounds__(512, 2) void k_qkv(const char* __restrict__ x, const char* __restrict__ w, char* __restrict__ ws) {
  __shared__ __attribute__((aligned(16))) char smem[4 * 32768 + 2048];
  const int bid = blockIdx.x, xcd = bid & 7, idx = bid >> 3;
  const int rt = (xcd >> 1) * 4 + (idx & 3), ct = (xcd & 1) * 8 + (idx >> 2);
  if (ct >= 15) {
    if (rt == 0 && threadIdx.x < N_FLAGS) *(unsigned*)(ws + OFF_FLAG + threadIdx.x * 256) = 0u;
    return;
  }
  PROF_DECL STAMP(0);
  const int tid = threadIdx.x, lane = tid & 63, wid = __builtin_amdgcn_readfirstlane(tid >> 6);
  if (wid >= 4) {
    const int lw = wid - 4;
    const unsigned lds0 = (unsigned)(uintptr_t)smem + lw * 1024;
    const int ra = 8 * lw + (lane >> 3), rb = 4 * lw + (lane >> 4), f = (rb & 3) | ((rb >> 1) & 4);
    const unsigned voA = ra * 1280 + (((lane & 7) ^ ((ra >> 1) & 7) ^ ((ra >> 2) & 4)) << 4), voB = rb * 3840 + (((lane & 15) ^ (f << 1)) << 4);
    const char* pa = x + (size_t)rt * 128 * 1280; const char* pb = w + ct * 256;
    auto issue = [&](int st) {
      const unsigned base = lds0 + (st & 3) * 32768;
#pragma unroll
      for (int j = 0; j < 4; ++j) { glds<1>(voA + j * 32 * 1280, pa + st * 128, base, j * 4096); glds<1>(voB + j * 16 * 3840, pb + st * 64 * 3840, base, 16384 + j * 4096); }
    };
    STAMP(1); issue(0); issue(1); STAMP(2);
    wait_vm_bar<8>(); STAMP(3);
    issue(2); issue(3);
#pragma unroll 1
    for (int s = 1; s < 7; ++s) { wait_vm_bar<16>(); issue(s + 3); }
    wait_vm_bar<16>(); wait_vm_bar<8>(); wait_vm_bar<0>();
    PROF_END(0);
    return;
  }
  lds_cptr L = (lds_cptr)smem; lds_ptr scr = (lds_ptr)smem + 4 * 32768;
  unsigned cs0, cs1; CODE_TOUCH(k_qkv, k_end, wid, cs0); CODE_TOUCH(k_qkv, k_end, wid + 4, cs1);
  if (ct < 5) qkv_compute<0>(L, scr, wid, lane, rt * 128, ct * 128, ws PROF_PASS);
  else if (ct < 10) qkv_compute<1>(L, scr, wid, lane, rt * 128, ct * 128, ws PROF_PASS);
  else qkv_compute<2>(L, scr, wid, lane, rt * 128, ct * 128, ws PROF_PASS);
  CODE_DONE(cs0); CODE_DONE(cs1);
  PROF_END(0);
}

constexpr int L_STG = 48 * KV_TILE;
constexpr int L_WO = 4 * KV_TILE;
constexpr int G_STAGE = 28 * 1024;
constexpr int L_RED = 5 * G_STAGE;
constexpr int D_STAGE = 10 * 1024;
constexpr int L_CS = 15 * D_STAGE;
constexpr int L_CNT = 163840 - 16;
constexpr int L_CH = L_WO + 102400, L_CA = L_CS + 1024;

DI float rowmax16(const f32x16& s) {
  float a = fmaxf(fmaxf(s[0], s[1]), s[2]), b = fmaxf(fmaxf(s[3], s[4]), s[5]);
  a = fmaxf(fmaxf(a, s[6]), s[7]); b = fmaxf(fmaxf(b, s[8]), s[9]);
  a = fmaxf(fmaxf(a, s[10]), s[11]); b = fmaxf(fmaxf(b, s[12]), s[13]);
  a = fmaxf(fmaxf(a, s[14]), s[15]);
  const float m = fmaxf(a, b);
  auto rr = __builtin_amdgcn_permlane32_swap(__float_as_uint(m), __float_as_uint(m), false, false);
  return fmaxf(__uint_as_float(rr[0]), __uint_as_float(rr[1]));
}
DI unsigned exp_pk(float a, float b) { return pk2(__builtin_amdgcn_exp2f(a), __builtin_amdgcn_exp2f(b)); }
DI int lane_id() { int v; asm volatile("v_mbcnt_lo_u32_b32 %0, -1, 0\n\tv_mbcnt_hi_u32_b32 %0, -1, %0" : "=v"(v)); __builtin_assume(v >= 0 && v < 64); return v; }
DI bf16x8 as_frag(u32x4 v) { return __builtin_bit_cast(bf16x8, v); }

template <int KIND>
DI void retile_issue(const char* __restrict__ w0, const char* __restrict__ w1, int tile, int ks, int lane, unsigned stg) {
  if (KIND == 0) {
    const unsigned vo = (ks * 32 + (lane >> 3)) * (FF * 2) + tile * 128 + (lane & 7) * 16;
#pragma unroll
    for (int j = 0; j < 4; ++j) { glds<1>(vo + j * 8 * (FF * 2), w0, stg, j * 1024); glds<1>(vo + j * 8 * (FF * 2), w1, stg, 4096 + j * 1024); }
  } else {
#pragma unroll
    for (int j = 0; j < 8; ++j) {
      int k = ks * 32 + 4 * j + (lane >> 4);
      if (KIND == 2) { const int pp = div20(k) >> 1, e = k - 40 * pp; k = 40 * pp + (e < 16 ? e : e < 32 ? e + 4 : e < 36 ? e - 16 : e); }
      glds<1>(k * (DM * 2) + tile * 256 + (lane & 15) * 16, w0, stg, j * 1024);
    }
  }
}
template <int KIND>
DI void retile_store(rsrc_t rs, lds_cptr S, int tile, int ks, int lane) {
  const int g = lane >> 4, q = (lane & 15) >> 2, p = lane & 3;
#pragma unroll
  for (int f = 0; f < 8; ++f) {
    lds_cptr a; int hi; unsigned dst;
    if (KIND == 0) {
      const int U = 2 * tile + (f >> 2), j = (f >> 1) & 1;
      a = S + (f & 1) * 4096 + (8 * g + q) * 128 + (32 * (f >> 2) + ((U >= 27 ? U - 27 : U) >= 24 ? 16 * j + 4 * p : 8 * p + 4 * j)) * 2; hi = 512;
      dst = OFF_WGUF + ((8 * tile + f) * 20 + ks) * 1024;
    } else {
      const int U = 4 * tile + (f >> 1), j = f & 1;
      a = S + (8 * g + q) * 256 + (32 * (f >> 1) + (U % 5 == 2 ? 16 * j + 4 * p : 8 * p + 4 * j)) * 2; hi = 1024;
      const int n10 = (2 * U + j) % 10, nf = 2 * U + j + (n10 == 5 ? 4 : n10 > 5 ? -1 : 0);
      dst = (KIND == 1 ? OFF_WDF : OFF_WOF) + (nf * (KIND == 1 ? 54 : 20) + ks) * 1024;
    }
    const bf16x8 fr = __builtin_shufflevector(lds_tr(a), lds_tr(a + hi), 0, 1, 2, 3, 4, 5, 6, 7);
    st16c(rs, lane * 16, dst, __builtin_bit_cast(u32x4, fr));
    if (KIND == 0) {
      const f32x4 c = mfma16(fr, ONES8, f32x4{0.f, 0.f, 0.f, 0.f});
      if ((lane & 15) == 0) st16c(rs, g * 16, OFF_CSUM + (ks * 216 + 8 * tile + f) * 64, __builtin_bit_cast(u32x4, c));
    }
  }
}

struct Roles {
  int head, qt, rt, ct, rg, cg, UB, UL;
  DI Roles(int bid) {
    const int xcd = bid & 7, idx = bid >> 3;
    head = xcd * 4 + (idx >> 3); qt = idx & 7;
    rt = (xcd >> 1) * 8 + (idx & 7); ct = (xcd & 1) * 4 + (idx >> 3);
    rg = (xcd >> 2) * 8 + (idx & 7); cg = (xcd & 3) * 4 + (idx >> 3);
    UB = (cg >> 3) * 27 + 3 * (cg & 7); UL = (cg >> 3) * 27 + 24 + min((cg & 7) >> 1, 2);
  }
  DI int gu_nf(int f) const { return f < 12 ? 4 * UB + f : 4 * UL + 2 * (cg & 1) + f - 12; }
};

DI void signal_v(unsigned cnt, unsigned one, unsigned flag, char* ws) {
  unsigned old, so;
  asm volatile("s_waitcnt vmcnt(0)\n\ts_mov_b64 exec, 1\n\tds_add_rtn_u32 %0, %2, %3\n\ts_waitcnt lgkmcnt(0)\n\tv_readfirstlane_b32 %1, %0\n\ts_mov_b64 exec, 0xff\n\t"
               "s_cmp_eq_u32 %1, 3\n\ts_cbranch_scc0 .Lna%=\n\tglobal_atomic_add %4, %3, %5\n.Lna%=:\n\ts_mov_b64 exec, -1"
               : "=&v"(old), "=&s"(so) : "v"(cnt), "v"(one), "v"(flag), "s"(ws) : "scc", "memory");
}
DI void signal(char* smem, int cnt, char* ws, unsigned flag) { signal_v((unsigned)(uintptr_t)smem + cnt, 1u, flag + lane_id() * 256, ws); }

DI void helper_waves(char* smem, const char* __restrict__ wo, const char* __restrict__ wg, const char* __restrict__ wu, const char* __restrict__ wd,
                     char* __restrict__ ws, rsrc_t rs, const Roles& R, int hw, int lane PROF_ARG) {
  const int bid = blockIdx.x;
  const unsigned lds0 = (unsigned)(uintptr_t)smem, vo = lane * 16;
  LDS_AS unsigned* cnt = (LDS_AS unsigned*)((lds_ptr)smem + L_CNT);
  const char* kv = ws + OFF_KV + R.head * KV_HEAD;
  const unsigned vk = vo + hw * 4096, lk = lds0 + hw * 4096;
  auto runs = [&](int i0, int i1) {
#pragma unroll
    for (int i = i0; i < i1; ++i) glds<4>(vk, kv + i * 16384, lk, i * 16384);
  };
  runs(0, 3);
  if (hw == 0) { cnt[0] = 0u; cnt[1] = 0u; cnt[2] = 0u; }
  unsigned sc = lds0 + L_CNT, s1 = 1u, sf = FL_W + (bid & 7) * 256 + lane * 2048;
  asm volatile("" : "+v"(sc), "+v"(s1), "+v"(sf));
  STAMP(1);
  asm volatile("s_waitcnt vmcnt(8) lgkmcnt(0)\n\ts_barrier" ::: "memory");
  runs(3, 6);
  const int job = bid * 4 + hw;
  const unsigned stg = lds0 + L_STG + hw * 8192;
  int tile = 0, ks = 0;
  if (job < 540)      { ks = job / 27; tile = job - ks * 27; retile_issue<0>(wg, wu, tile, ks, lane, stg); }
  else if (job < 810) { ks = (job - 540) / 5; tile = job - 540 - ks * 5; retile_issue<1>(wd, wd, tile, ks, lane, stg); }
  else                { ks = min(job - 810, 99) / 5; tile = min(job - 810, 99) - ks * 5; retile_issue<2>(wo, wo, tile, ks, lane, stg); }
  wait_vm_bar<20>();
  runs(6, 7);
  if (hw < 3) glds<4>(vk, kv + 7 * 16384, lk, 7 * 16384); else { glds<2>(vk, kv + 7 * 16384, lk, 7 * 16384); glds<2>(vk, kv + 7 * 16384, lk, 7 * 16384); }
  wait_vm_bar<16>();
  if (hw < 3) glds<4>(vk, kv + 60 * KV_TILE, lk, 0);
  if (hw < 3) wait_vm<4>(); else wait_vm<0>();
  STAMP(2);
  lds_cptr S = (lds_cptr)smem + L_STG + hw * 8192;
  if (job < 540) retile_store<0>(rs, S, tile, ks, lane);
  else if (job < 810) retile_store<1>(rs, S, tile, ks, lane);
  else if (job < 910) retile_store<2>(rs, S, tile, ks, lane);
  asm volatile("s_waitcnt lgkmcnt(0)" ::: "memory");
  glds<4>(vo + hw * 8192, kv + L_STG, stg, 0); glds<4>(vo + hw * 8192, kv + L_STG + 4096, stg, 4096);
  signal_v(sc, s1, sf, ws);
  STAMP(3);
  asm volatile("s_barrier" ::: "memory");
  if (hw == 0) {
    await8(ws, FL_W + ((bid >> 3) & 7) * 2048, lane, 32);
    __builtin_amdgcn_fence(__ATOMIC_ACQUIRE, "agent");
    wait_vm<0>();
    asm volatile("s_mov_b64 exec, 1\n\tds_add_u32 %0, %1 offset:4\n\ts_mov_b64 exec, -1" :: "v"(sc), "v"(s1) : "memory");
  }
  STAMP(4);
  while (__builtin_amdgcn_readfirstlane(__hip_atomic_load(cnt + 1, __ATOMIC_RELAXED, __HIP_MEMORY_SCOPE_WORKGROUP)) < 5) __builtin_amdgcn_s_sleep(4);
  unsigned tw[5];
  CODE_TOUCH(k_main, k_end, hw, tw[2]); CODE_TOUCH(k_main, k_end, hw + 4, tw[3]);
  {
    const char* src = ws + OFF_WOF + R.ct * 102400;
#pragma unroll
    for (int i = 0; i < 6; ++i) glds<4>(vk, src + i * 16384, lk, L_WO + i * 16384);
    glds<1>(vo + hw * 1024, src + 98304, lds0 + hw * 1024, L_WO + 98304);
  }
  if (hw == 0) {
    f32x4 csum;
    {
      const int t = min(lane, 55);
      const f32x4* cp = (const f32x4*)(ws + OFF_CSUM) + R.gu_nf(t >> 2) * 4 + (t & 3);
      f32x4 c[20];
#pragma unroll
      for (int k = 0; k < 20; ++k) c[k] = cp[k * 864];
#pragma unroll
      for (int k = 0; k < 10; ++k) c[k] += c[k + 10];
      csum = ((c[0] + c[1]) + (c[2] + c[3])) + ((c[4] + c[5]) + (c[6] + c[7])) + (c[8] + c[9]);
    }
    *(LDS_AS unsigned*)((lds_ptr)smem + L_CH) = 0u;
    asm volatile("s_waitcnt vmcnt(0) lgkmcnt(0)\n\ts_barrier" ::: "memory");
    asm volatile("" :: "v"(tw[2]), "v"(tw[3]));
    STAMP(5);
    await(ws, FL_O + (R.rt >> 2) * 2048 + R.ct * 256, 32);
    STAMP(6);
    asm volatile("s_barrier\n\ts_barrier\n\ts_barrier" ::: "memory");
    STAMP(7);
    if (lane < 56) *(LDS_AS f32x4*)((lds_ptr)smem + L_CS + lane * 16) = csum;
    *(LDS_AS unsigned*)((lds_ptr)smem + L_CA) = 0u;
    await(ws, FL_H + R.rg * 2048 + (R.cg & 7) * 256, 16);
    STAMP(8);
    asm volatile("s_waitcnt lgkmcnt(0)\n\ts_barrier" ::: "memory");
#pragma unroll 1
    for (int s = 0; s < 12; ++s) asm volatile("s_barrier" ::: "memory");
    STAMP(9);
    await(ws, FL_ACT + (R.rt >> 1) * 2048 + R.ct * 256, 16);
    STAMP(10);
    asm volatile("s_barrier" ::: "memory");
    return;
  }
  const int l = hw - 1;
  unsigned vg[5], vd[5];
#pragma unroll
  for (int i = 0; i < 5; ++i) { vg[i] = vo + R.gu_nf(min(l + 3 * i, 13)) * 20480; vd[i] = vo + i * 54 * 1024; }
  const char* gsrc = ws + OFF_WGUF; const char* dsrc = ws + OFF_WDF + (R.ct * 5 * 54 + 2 * l) * 1024;
  u32x4 wr[4][5][2];
#pragma unroll
  for (int m = 0; m < 4; ++m)
#pragma unroll
    for (int i = 0; i < 5; ++i)
      asm volatile("global_load_dwordx4 %0, %2, %3\n\tglobal_load_dwordx4 %1, %2, %3 offset:1024" : "=&v"(wr[m][i][0]), "=&v"(wr[m][i][1]) : "v"(vd[i]), "s"(dsrc + (m + 5) * 6144) : "memory");
#pragma unroll
  for (int r = 0; r < 2; ++r) {
    const int part = (((bid >> 3) & 7) * 3 + l) * 2 + r, n = min(part * 47 + min(lane, 46), 2239), f = (n * 6554) >> 20;
    const unsigned o0 = OFF_WGUF + R.gu_nf(f) * 20480 + (n - f * 160) * 128;
    asm volatile("global_load_dword %0, %1, %2" : "=&v"(tw[r]) : "v"(o0), "s"(ws) : "memory");
  }
  {
    const int n = (((bid >> 3) & 7) * 3 + l) * 50 + min(lane, 49), f = (n * 4370) >> 20;
    const unsigned o1 = OFF_WDF + (R.ct * 5 + f) * 55296 + (n - f * 240) * 128;
    asm volatile("global_load_dword %0, %1, %2" : "=&v"(tw[4]) : "v"(o1), "s"(ws) : "memory");
  }
  const int g4 = min(l + 12, 13) * 2048 - l * 2048;
  auto gstage = [&](int s) {
    const unsigned base = lds0 + (s >= 6 ? s - 6 : s >= 1 ? s - 1 : 4) * G_STAGE + l * 2048;
#pragma unroll
    for (int i = 0; i < 5; ++i) glds<2>(vg[i], gsrc + s * 2048, base, i < 4 ? i * 6144 : g4);
  };
  auto dstage = [&](int m) {
    const unsigned base = lds0 + ((m ? 3 * m - 3 : 12) + l) * D_STAGE;
#pragma unroll
    for (int i = 0; i < 5; ++i) glds<2>(vd[i], dsrc + m * 6144, base, i * 2048);
  };
  asm volatile("s_barrier" ::: "memory");
  gstage(0);
  wait_vm_bar<53>();
  asm volatile("s_barrier" ::: "memory");
  gstage(1);
  asm volatile("s_barrier" ::: "memory");
  gstage(2); gstage(3); gstage(4);
  wait_vm_bar<40>();
  asm volatile("" :: "v"(tw[0]), "v"(tw[1]), "v"(tw[2]), "v"(tw[3]), "v"(tw[4]));
  wait_vm_bar<30>();
#pragma unroll 1
  for (int s = 2; s < 7; ++s) { wait_vm_bar<20>(); gstage(s + 3); }
  wait_vm_bar<20>(); dstage(0);
  wait_vm_bar<20>();
  wait_vm_bar<10>(); dstage(1);
  asm volatile("s_barrier" ::: "memory"); dstage(2);
  asm volatile("s_barrier" ::: "memory");
  dstage(3); dstage(4);
  asm volatile("s_barrier" ::: "memory");
  wait_vm_bar<0>();
#pragma unroll
  for (int m = 0; m < 4; ++m) {
    asm volatile("s_barrier" ::: "memory");
    lds_ptr d = (lds_ptr)smem + ((m ? 3 * m - 3 : 12) + l) * D_STAGE + vo;
#pragma unroll
    for (int i = 0; i < 5; ++i) {
      asm volatile("" : "+v"(wr[m][i][0]), "+v"(wr[m][i][1]));
      *(LDS_AS u32x4*)(d + i * 2048) = wr[m][i][0]; *(LDS_AS u32x4*)(d + i * 2048 + 1024) = wr[m][i][1];
    }
  }
  asm volatile("s_waitcnt lgkmcnt(0)\n\ts_barrier" ::: "memory");
}

DI void attention(char* smem, char* __restrict__ ws, rsrc_t rs, const Roles& R, int wid, int lane PROF_ARG) {
  lds_cptr L = (lds_cptr)smem;
  const int r = lane & 31, h = lane >> 5;
  const char* qb = ws + OFF_Q + R.head * Q_HEAD + (R.qt * 8 + wid * 2) * Q_TILE;
  const bf16x8 qaA = *(const bf16x8*)(qb + lane * 16), qaB = *(const bf16x8*)(qb + Q_TILE + lane * 16);
  u32x2 qtA = *(const u32x2*)(qb + 1024 + r * 8), qtB = *(const u32x2*)(qb + Q_TILE + 1024 + r * 8);
  if (h) { qtA = u32x2{0u, 0u}; qtB = qtA; }

  const int rm = r <= 20 ? r : (r == 24 ? 20 : 21);
  const int kaO = lane * 16, kbO = h ? 1280 + 320 : 1024 + r * 8, vO = 1280 + h * 352 + rm * 16;
  f32x16 zero, oA, oB, sA, sB;
#pragma unroll
  for (int i = 0; i < 16; ++i) { zero[i] = 0.f; oA[i] = 0.f; oB[i] = 0.f; }
  bf16x8 ka[2], v0[2], v1[2]; bf16x4 kb[2];
  lds_cptr pk = L + kaO, pb = L + kbO, pv = L + vO;
  const unsigned ob = OFF_OF + (R.qt * 16 + wid * 4) * 20480 + (R.head >> 1) * 1280 + (r >> 4) * 20480 + (r & 15) * 16;
  unsigned o16[2] = {ob + h * 256 + (R.head & 1) * 512, ob + h * 256 + (R.head & 1) * 512 + 40960};
  unsigned o8 = h ? 0xfff00000u : ob + 1024 + (R.head & 1) * 8;
  asm volatile("" : "+v"(o16[0]), "+v"(o16[1]), "+v"(o8));
  wait_lgkm_bar();
  STAMP(1);
  kb[0] = lds_b64(pb); ka[0] = lds_b128(pk); v0[0] = lds_b128(pv); v1[0] = lds_b128(pv + 704);
  v1[1] = v1[0];
  {
    sA = mfma32(ka[0], qaA, mfma32k8(kb[0], __builtin_bit_cast(bf16x4, qtA), zero));
    sB = mfma32(ka[0], qaB, mfma32k8(kb[0], __builtin_bit_cast(bf16x4, qtB), zero));
    const unsigned nA = pk2(-rowmax16(sA), 0.f), nB = pk2(-rowmax16(sB), 0.f);
    if (h) { qtA[0] = nA; qtB[0] = nB; }
  }
  const bf16x4 qbA = __builtin_bit_cast(bf16x4, qtA), qbB = __builtin_bit_cast(bf16x4, qtB);
  sA = mfma32(ka[0], qaA, mfma32k8(kb[0], qbA, zero));
  const unsigned pcnt = (unsigned)(uintptr_t)smem + L_CNT + 4, pone = 1;
  u32x4 pl = {0u, 0u, 0u, 0u};
  float e14 = 0.f, e15 = 0.f;

#define EX(i) __builtin_amdgcn_exp2f(sC[i])
  auto phase = [&](const f32x16& sC, f32x16& oC, f32x16& oP, bf16x8 vP, f32x16& sN, bf16x8 qa, bf16x4 qb4, bf16x8 kA, bf16x4 kB, bf16x8 vC, auto&& rd1, auto&& rd2) {
    u32x4 p0; float a, b, c, d;
    sN = mfma32k8(kB, qb4, zero); SBAR(); rd1(); SBAR();
    a = EX(0); SBAR(); pl[3] = pk2(e14, e15); SBAR(); b = EX(1); SBAR(); c = EX(2); SBAR(); p0[0] = pk2(a, b); SBAR(); d = EX(3); SBAR();
    oP = mfma32(vP, as_frag(pl), oP); SBAR(); rd2(); SBAR();
    a = EX(4); SBAR(); p0[1] = pk2(c, d); SBAR(); b = EX(5); SBAR(); c = EX(6); SBAR(); p0[2] = pk2(a, b); SBAR(); d = EX(7); SBAR();
    sN = mfma32(kA, qa, sN); SBAR();
    a = EX(8); SBAR(); p0[3] = pk2(c, d); SBAR(); b = EX(9); SBAR(); c = EX(10); SBAR(); pl[0] = pk2(a, b); SBAR(); d = EX(11); SBAR();
    oC = mfma32(vC, as_frag(p0), oC); SBAR(); __builtin_amdgcn_s_waitcnt(0xC07F); SBAR();
    a = EX(12); SBAR(); pl[1] = pk2(c, d); SBAR(); b = EX(13); SBAR(); e14 = EX(14); SBAR(); pl[2] = pk2(a, b); SBAR(); e15 = EX(15); SBAR();
  };
#undef EX
#pragma unroll 1
  for (int it = 0; it < 16; ++it) {
    const int dn = it == 14 ? -56 * KV_TILE : 4 * KV_TILE;
#pragma unroll
    for (int t = 0; t < 4; ++t) {
      const int c = t & 1, n = c ^ 1, no = t < 3 ? (t + 1) * KV_TILE : 0;
      phase(sA, oA, oB, v1[n], sB, qaB, qbB, ka[c], kb[c], v0[c],
            [&] {
              if (t == 0) asm volatile("s_bitcmp1_b32 %0, %1\n\ts_cbranch_scc0 .Lsk%=\n\ts_cmp_eq_u32 %1, 11\n\ts_cbranch_scc1 .Lsp%=\n\ts_barrier\n\ts_branch .Lsk%=\n"
                                       ".Lsp%=:\n\ts_mov_b64 exec, 1\n\tds_add_u32 %2, %3\n\ts_mov_b64 exec, -1\n.Lsk%=:" :: "s"(0x90A), "s"(it), "v"(pcnt), "v"(pone) : "scc", "memory");
              if (t == 3) { pk += dn; pb += dn; pv += dn; }
              kb[n] = lds_b64(pb + no); },
            [&] { ka[n] = lds_b128(pk + no); });
      phase(sB, oB, oA, v1[c], sA, qaA, qbA, ka[n], kb[n], v0[c], [&] { v0[n] = lds_b128(pv + no); }, [&] { v1[n] = lds_b128(pv + no + 704); });
    }
  }
  pl[3] = pk2(e14, e15);
  oB = mfma32(v1[1], as_frag(pl), oB);
  STAMP(2);

  auto fin = [&](const f32x16& o, float l, int blk) {
    const float inv = __builtin_amdgcn_rcpf(l);
    unsigned w[6];
#pragma unroll
    for (int u = 0; u < 6; ++u) w[u] = pk2(o[2 * u] * inv, o[2 * u + 1] * inv);
    const auto s0 = __builtin_amdgcn_permlane32_swap(w[0], w[2], false, false), s1 = __builtin_amdgcn_permlane32_swap(w[1], w[3], false, false);
    st16c(rs, o16[blk], 0, u32x4{s0[0], s1[0], s0[1], s1[1]});
    st8c(rs, o8, blk * 40960, u32x2{w[4], w[5]});
  };
  const float lA = h ? oA[8] : oA[12], lB = h ? oB[8] : oB[12];
  fin(oA, lA, 0); fin(oB, lB, 1);
  SBAR();
  {
    f32x2 z0 = {0.f, 0.f}, z1 = z0;
#pragma unroll
    for (int i = 0; i < 7; ++i) { z0 = __builtin_elementwise_fma(f32x2{oA[2 * i], oA[2 * i + 1]}, f32x2{0.f, 0.f}, z0); z1 = __builtin_elementwise_fma(f32x2{oB[2 * i], oB[2 * i + 1]}, f32x2{0.f, 0.f}, z1); }
    z0 += z1;
    const bool ok = fmaxf(lA, lB) + (z0[0] + z0[1]) < 1.0e18f && fminf(lA, lB) > 1.0e-18f;
    if (__builtin_expect(!__all(ok), 0)) {
      const char* kvb = ws + OFF_KV + R.head * KV_HEAD;
      const bf16x4 z4 = {0, 0, 0, 0}, tA = h ? z4 : qbA, tB = h ? z4 : qbB;
      float mA = -INFINITY, mB = -INFINITY; oA = zero; oB = zero;
      auto upd = [&](f32x16 s, float& m, f32x16& o, bf16x8 w0, bf16x8 w1) {
        const float mn = fmaxf(m, rowmax16(s)), alpha = __builtin_amdgcn_exp2f(m - mn);
        m = mn; o *= alpha;
        u32x4 a, b;
#pragma unroll
        for (int i = 0; i < 4; ++i) { a[i] = exp_pk(s[2 * i] - mn, s[2 * i + 1] - mn); b[i] = exp_pk(s[8 + 2 * i] - mn, s[9 + 2 * i] - mn); }
        o = mfma32(w1, as_frag(b), mfma32(w0, as_frag(a), o));
      };
#pragma unroll 1
      for (int t = 0; t < 64; ++t) {
        const char* tb = kvb + t * KV_TILE;
        const bf16x8 k8 = *(const bf16x8*)(tb + kaO), w0 = *(const bf16x8*)(tb + vO), w1 = *(const bf16x8*)(tb + vO + 704);
        const bf16x4 k4 = *(const bf16x4*)(tb + kbO);
        upd(mfma32(k8, qaA, mfma32k8(k4, tA, zero)), mA, oA, w0, w1);
        upd(mfma32(k8, qaB, mfma32k8(k4, tB, zero)), mB, oB, w0, w1);
      }
      wait_vm<0>();
      fin(oA, h ? oA[8] : oA[12], 0); fin(oB, h ? oB[8] : oB[12], 1);
    }
  }
}

#define P_DEPTH 14
#define P_PRO 4
#define P_RAMP 3
constexpr int ld_pos(int s, int pro, int ramp, int depth, int n) {
  int j = 2 * pro;
  for (int i = 0; i < s; ++i) { j += ramp; if (j > 2 * (i + 1 + depth)) j = 2 * (i + 1 + depth); if (j > 2 * n) j = 2 * n; }
  return j;
}
template <int MODE>
DI void proj(char* smem, const char* __restrict__ x, rsrc_t rs, char* __restrict__ out, const Roles& R, int wid, int lane PROF_ARG) {
  constexpr int NST = MODE ? 27 : 10, NKS = 2 * NST, DEPTH = MODE ? P_DEPTH : 10, PRO = P_PRO, RAMP = P_RAMP;
  lds_cptr L = (lds_cptr)smem;
  const int rh = wid >> 1, kp = wid & 1, t16 = lane & 15, g = lane >> 4;
  const int mf = R.rt * 4 + 2 * rh + kp, tok = mf * 16 + t16;
  const int ODD = R.ct & 1, U0 = (R.ct >> 1) * 5 + 3 * ODD, UH = (R.ct >> 1) * 5 + 2;
  const unsigned hp = OFF_HF + (mf * 20 + U0) * 1024, hh = OFF_HF + (mf * 20 + UH) * 1024 + ODD * 512;
  const unsigned fo = lane * 16, fh = (g >> 1) * 256 + t16 * 16 + (g & 1) * 8;
  const unsigned ro = tok * 1280 + U0 * 64 + g * 16, rhh = tok * 1280 + UH * 64 + ODD * 32 + g * 8;
  const unsigned ab = (MODE ? OFF_ACT : OFF_OF) + (mf * NKS + kp) * 1024, ab1 = ab + (1 - 2 * kp) * NKS * 1024;
  lds_cptr B = L + (MODE ? 0 : L_WO) + kp * 1024 + lane * 16, B1 = B + 6 * D_STAGE, B2 = B + 12 * D_STAGE;
  asm volatile("" : "+v"(B1), "+v"(B2));
  u32x4 res0, res1; u32x2 resh;
  if (MODE == 0) { res0 = *(const u32x4*)(x + ro); res1 = *(const u32x4*)(x + ro + 64); resh = *(const u32x2*)(x + rhh); }
  else { res0 = ld16c(rs, fo, hp); res1 = ld16c(rs, fo, hp + 1024); resh = ld8c(rs, fh, hh); }
  f32x4 acc[2][5];
  acc[0][0] = f32x4{bflo(res0[0]), bfhi(res0[0]), bflo(res0[1]), bfhi(res0[1])}; acc[0][1] = f32x4{bflo(res0[2]), bfhi(res0[2]), bflo(res0[3]), bfhi(res0[3])};
  acc[0][2] = f32x4{bflo(res1[0]), bfhi(res1[0]), bflo(res1[1]), bfhi(res1[1])}; acc[0][3] = f32x4{bflo(res1[2]), bfhi(res1[2]), bflo(res1[3]), bfhi(res1[3])};
  acc[0][4] = f32x4{bflo(resh[0]), bfhi(resh[0]), bflo(resh[1]), bfhi(resh[1])};
  asm volatile("" : "+v"(acc[0][0]), "+v"(acc[0][1]), "+v"(acc[0][2]), "+v"(acc[0][3]), "+v"(acc[0][4]));
  SBAR(); asm volatile("s_barrier" ::: "memory"); SBAR();
  STAMP(MODE ? 10 : 4);
  u32x4 a[DEPTH + 1][2];
  bf16x8 b[2][5];
  auto lda = [&](int j) { a[(j >> 1) % (DEPTH + 1)][j & 1] = ld16c(rs, fo, (j & 1 ? ab1 : ab) + (j >> 1) * 2048); };
  auto ldb = [&](int s, int v) { constexpr int W = 6 * D_STAGE; const int o = MODE ? ((s + 12) % 15) * D_STAGE + v * 2048 : s * 2048 + v * 20480; b[s & 1][v] = lds_b128((o < W ? B : o < 2 * W ? B1 : B2) + o % W); };
#pragma unroll
  for (int j = 0; j < 2 * PRO; ++j) lda(j);
#pragma unroll
  for (int v = 0; v < 5; ++v) ldb(0, v);
  SBAR();
  static_for<0, NST>([&](auto S) {
    constexpr int s = S.value, c = s % (DEPTH + 1);
    constexpr int j0 = ld_pos(s, PRO, RAMP, DEPTH, NST), j1 = ld_pos(s + 1, PRO, RAMP, DEPTH, NST);
    if (MODE && (s + 1) % 3 == 0 && s + 1 < 16) wait_lgkm_bar();
#pragma unroll
    for (int u = 0; u < 2; ++u)
#pragma unroll
      for (int v = 0; v < 5; ++v) {
        if (s || !u) mfma16a(acc[u][v], b[s & 1][v], a[c][u]); else mfma16z(acc[u][v], b[0][v], a[0][u]);
        if (u == 0 && s + 1 < NST) ldb(s + 1, v);
        if (u == 1 && j0 + v < j1) lda(j0 + v);
        SBAR();
      }
  });
  asm volatile("s_nop 7" ::: "memory");
  SBAR();
  STAMP(MODE ? 11 : 5);
  lds_ptr scr = (lds_ptr)smem + (MODE ? 9 * D_STAGE : L_RED) + lane * 16;
#pragma unroll
  for (int v = 0; v < 5; ++v) *(LDS_AS f32x4*)(scr + wid * 5120 + v * 1024) = acc[1][v];
  wait_lgkm_bar();
  f32x4 y[5];
#pragma unroll
  for (int v = 0; v < 5; ++v) y[v] = *(const LDS_AS f32x4*)(scr + (wid ^ 1) * 5120 + v * 1024);
  SBAR();
  unsigned o[10];
#pragma unroll
  for (int v = 0; v < 5; ++v) { y[v] += acc[0][v]; o[2 * v] = pk2(y[v][0], y[v][1]); o[2 * v + 1] = pk2(y[v][2], y[v][3]); }
  if (MODE == 0) {
    st16c(rs, fo, hp, u32x4{o[0], o[1], o[2], o[3]}); st16c(rs, fo, hp + 1024, u32x4{o[4], o[5], o[6], o[7]}); st8c(rs, fh, hh, u32x2{o[8], o[9]});
    float s0 = 0.f, s1 = 0.f, q0 = 0.f, q1 = 0.f;
    const bf2 one2 = __builtin_bit_cast(bf2, 0x3f803f80u);
#pragma unroll
    for (int i = 0; i < 5; ++i) {
      const bf2 e0 = __builtin_bit_cast(bf2, o[2 * i]), e1 = __builtin_bit_cast(bf2, o[2 * i + 1]);
      s0 = __builtin_amdgcn_fdot2_f32_bf16(e0, one2, s0, false); s1 = __builtin_amdgcn_fdot2_f32_bf16(e1, one2, s1, false);
      q0 = __builtin_amdgcn_fdot2_f32_bf16(e0, e0, q0, false); q1 = __builtin_amdgcn_fdot2_f32_bf16(e1, e1, q1, false);
    }
    const float ssum = xsum32(xsum16(s0 + s1)), ssq = xsum32(xsum16(q0 + q1));
    if (g == 0) st8c(rs, tok * 64, OFF_STAT + R.ct * 8, u32x2{__float_as_uint(ssum), __float_as_uint(ssq)});
  } else {
    *(u32x4*)(out + ro) = u32x4{o[0], o[1], o[2], o[3]}; *(u32x4*)(out + ro + 64) = u32x4{o[4], o[5], o[6], o[7]}; *(u32x2*)(out + rhh) = u32x2{o[8], o[9]};
  }
}

DI void gateup(char* smem, rsrc_t rs, const Roles& R, int wid, int lane PROF_ARG) {
  constexpr int DEPTH = 6;
  const int t16 = lane & 15, g = lane >> 4, mf = R.rg * 8 + 2 * wid;
  lds_cptr B0 = (lds_cptr)smem + lane * 16, B1 = B0 + 2 * G_STAGE, B2 = B0 + 4 * G_STAGE;
  asm volatile("" : "+v"(B1), "+v"(B2));
  const unsigned fo = lane * 16, ab = OFF_HF + mf * 20480;
  SBAR(); asm volatile("s_barrier" ::: "memory"); SBAR();
  STAMP(7);
  u32x4 a[DEPTH][2];
  bf16x8 b[14];
  f32x4 acc[2][14];
  auto lda = [&](int k, int u) { a[k % DEPTH][u] = ld16c(rs, fo, ab + XK(k) * 1024 + u * 20480); };
  auto ldb = [&](int k, int v) {
    const int slot = ((k >> 1) + 4) % 5;
    b[v] = lds_b128((slot < 2 ? B0 : slot < 4 ? B1 : B2) + (slot & 1 & (slot < 4)) * G_STAGE + (k & 1) * 1024 + v * 2048);
  };
#pragma unroll
  for (int k = 0; k < DEPTH - 1; ++k) { lda(k, 0); lda(k, 1); }
  u32x4 st0, st1;
  float nm[2], vk[2], c1[2];
  auto stat = [&](int u, int step) {
    const f32x4 s = __builtin_bit_cast(f32x4, u ? st1 : st0);
    if (step == 0) { nm[u] = s[0] + s[2]; vk[u] = s[1] + s[3]; }
    if (step == 1) nm[u] = xsum16(nm[u]);
    if (step == 2) vk[u] = xsum16(vk[u]);
    if (step == 3) nm[u] = xsum32(nm[u]) * (-1.0f / DM);
    if (step == 4) vk[u] = xsum32(vk[u]) * (1.0f / DM);
    if (step == 5) vk[u] = fmaxf(vk[u] - nm[u] * nm[u], 0.f) + LN_EPS;
    if (step == 6) c1[u] = -LOG2E * __builtin_amdgcn_rsqf(vk[u]);
    asm volatile("" : "+v"(nm[u]), "+v"(vk[u]));
    if (step == 6) asm volatile("" : "+v"(c1[u]));
  };
#pragma unroll
  for (int v = 0; v < 14; ++v) ldb(0, v);
  SBAR();
  static_for<0, 20>([&](auto S) {
    constexpr int k = S.value;
    if (k & 1) { asm volatile("s_barrier" ::: "memory"); SBAR(); }
#pragma unroll
    for (int v = 0; v < 14; ++v) {
      if (k) { mfma16a(acc[0][v], b[v], a[k % DEPTH][0]); mfma16a(acc[1][v], b[v], a[k % DEPTH][1]); }
      else   { mfma16z(acc[0][v], b[v], a[0][0]); mfma16z(acc[1][v], b[v], a[0][1]); }
      if (k < 19) ldb(k + 1, v);
      if (v < 2 && k + DEPTH - 1 < 20) lda(k + DEPTH - 1, v);
      if (v == 0 && k + DEPTH - 1 == 20) st0 = ld16c(rs, t16 * 64 + g * 16, OFF_STAT + mf * 1024);
      if (v == 1 && k + DEPTH - 1 == 20) st1 = ld16c(rs, t16 * 64 + g * 16, OFF_STAT + mf * 1024 + 1024);
      if (k == 18) stat(v / 7, v % 7);
      SBAR();
    }
  });
  asm volatile("s_barrier\n\ts_nop 7" ::: "memory");
  SBAR();
  STAMP(8);
  const LDS_AS f32x4* cs = (const LDS_AS f32x4*)((lds_cptr)smem + L_CS) + g;
  auto swi = [&](int u, int p) {
    const f32x4 gt = acc[u][2 * p] + cs[8 * p] * nm[u], up = acc[u][2 * p + 1] + cs[8 * p + 4] * nm[u], e = gt * c1[u], n = gt * up;
    f32x4 d;
#pragma unroll
    for (int i = 0; i < 4; ++i) d[i] = __builtin_amdgcn_exp2f(e[i]);
    d = d * vk[u] + vk[u];
#pragma unroll
    for (int i = 0; i < 4; ++i) d[i] = __builtin_amdgcn_rcpf(d[i]);
    d *= n;
    return u32x2{pk2(d[0], d[1]), pk2(d[2], d[3])};
  };
  const unsigned ob = OFF_ACT + mf * 54 * 1024;
#pragma unroll
  for (int i = 0; i < 3; ++i)
#pragma unroll
    for (int u = 0; u < 2; ++u) {
      const u32x2 pa = swi(u, 2 * i), pb = swi(u, 2 * i + 1);
      st16c(rs, fo, ob + (u * 54 + R.UB + i) * 1024, u32x4{pa[0], pa[1], pb[0], pb[1]});
    }
  if ((R.cg & 7) < 6) {
    const u32x2 pa = swi(0, 6), pb = swi(1, 6);
    const auto s0 = __builtin_amdgcn_permlane16_swap(pa[0], pb[0], false, false), s1 = __builtin_amdgcn_permlane16_swap(pa[1], pb[1], false, false);
    st16c(rs, (g & 1) * 54 * 1024 + (g >> 1) * 256 + t16 * 16, ob + R.UL * 1024 + (R.cg & 1) * 512, u32x4{s0[0], s1[0], s0[1], s1[1]});
  }
}

extern "C" __global__ __launch_bounds__(512, 2) void k_main(const char* __restrict__ x, const char* __restrict__ wo, const char* __restrict__ wg,
                                                            const char* __restrict__ wu, const char* __restrict__ wd, char* __restrict__ ws,
                                                            char* __restrict__ out) {
  __shared__ __attribute__((aligned(16))) char smem[163840];
  const int wid = __builtin_amdgcn_readfirstlane(threadIdx.x >> 6);
  PROF_DECL STAMP(0);
  const Roles R(blockIdx.x);
  const rsrc_t rs = __builtin_amdgcn_make_buffer_rsrc(ws, 0, (int)WS_BYTES, 0x00020000);
  if (wid >= 4) {
    helper_waves(smem, wo, wg, wu, wd, ws, rs, R, wid - 4, lane_id() PROF_PASS);
  } else {
    attention(smem, ws, rs, R, wid, lane_id() PROF_PASS);
    signal(smem, L_CNT + 8, ws, FL_O + R.qt * 2048);
    asm volatile("s_barrier" ::: "memory");
    STAMP(3);
    proj<0>(smem, x, rs, out, R, wid, lane_id() PROF_PASS);
    signal(smem, L_CH, ws, FL_H + (R.rt >> 1) * 2048);
    asm volatile("s_barrier" ::: "memory");
    STAMP(6);
    gateup(smem, rs, R, wid, lane_id() PROF_PASS);
    signal(smem, L_CA, ws, FL_ACT + R.rg * 2048);
    asm volatile("s_barrier" ::: "memory");
    STAMP(9);
    proj<1>(smem, x, rs, out, R, wid, lane_id() PROF_PASS);
  }
  PROF_END(1);
}
extern "C" __global__ void k_end() {}

extern "C" void kernel_launch(void* const* d_in, const int* in_sizes, int n_in, void* d_out, int out_size, void* d_ws, size_t ws_size,
                              hipStream_t stream) {
  const char* x = (const char*)d_in[0]; const char* wqkv = (const char*)d_in[1]; const char* wo = (const char*)d_in[2];
  const char* wg = (const char*)d_in[4]; const char* wu = (const char*)d_in[5]; const char* wd = (const char*)d_in[6];
  char* ws = (char*)d_ws;
  k_qkv<<<256, 512, 0, stream>>>(x, wqkv, ws);
  k_main<<<256, 512, 0, stream>>>(x, wo, wg, wu, wd, ws, (char*)d_out);
}
```
